```python
import torch

def _softmax(x, axis=-1):
    return torch.softmax(x, dim=axis)
def _gelu(x):
    return torch.nn.functional.gelu(x, approximate="tanh")
def _silu(x):
    return torch.nn.functional.silu(x)
def _sigmoid(x):
    return torch.sigmoid(x)
def _layernorm(x, axis=-1, eps=1e-5):
    m = x.mean(dim=axis, keepdim=True)
    v = x.var(dim=axis, keepdim=True, unbiased=False)
    return (x - m) / torch.sqrt(v + eps)
def _scatter_add(tgt, idx, src, axis=0):
    return tgt.clone().index_add_(axis, idx, src)
def _segment_sum(src, seg, n):
    out = torch.zeros((n,) + src.shape[1:], dtype=src.dtype)
    return out.index_add_(0, seg, src)
def _pad(x, pw):
    flat = tuple(v for pair in reversed(pw) for v in pair)
    return torch.nn.functional.pad(x, flat)

kernel_name = 'concat_heads__mha_block__spectral_fnet__prompt'

def setup_inputs():

    _w = (lambda a: (lambda t: t.to(torch.bfloat16) if t.is_floating_point() else t)(torch.as_tensor(a)))
    def _A_setup():
        B, S, D, H = 1, 2048, 1024, 32
        return {
            "x": _w((lambda *s: torch.randn(*s, dtype=torch.bfloat16) * 0.2)(B, S, D)),
            "wqkv": _w((lambda *s: torch.randn(*s, dtype=torch.bfloat16) * 0.8)(D, 3 * D)),
            "wo": _w((lambda *s: torch.randn(*s, dtype=torch.bfloat16) * 0.2)(D, D)),
        }
    _A_in = _A_setup()

    def _B_setup():
        B, S, D = 1, 2048, 1024
        ns = torch.arange(S)[:, None] * torch.arange(S)[None, :]
        nd = torch.arange(D)[:, None] * torch.arange(D)[None, :]
        Ws_re = torch.cos(-2.0 * torch.pi * ns / S).to(float)
        Ws_im = torch.sin(-2.0 * torch.pi * ns / S).to(float)
        Wd_re = torch.cos(-2.0 * torch.pi * nd / D).to(float)
        Wd_im = torch.sin(-2.0 * torch.pi * nd / D).to(float)
        return {
            "x": _w((lambda *s: torch.randn(*s, dtype=torch.bfloat16) * 0.05)(B, S, D)),
            "dft_seq_re": _w(Ws_re),
            "dft_seq_im": _w(Ws_im),
            "dft_hid_re": _w(Wd_re),
            "dft_hid_im": _w(Wd_im),
        }
    _B_in = _B_setup()

    return {
        "A_x": _A_in["x"],
        "A_wqkv": _A_in["wqkv"],
        "A_wo": _A_in["wo"],
        "B_x": _B_in["x"],
        "B_dft_seq_re": _B_in["dft_seq_re"],
        "B_dft_seq_im": _B_in["dft_seq_im"],
        "B_dft_hid_re": _B_in["dft_hid_re"],
        "B_dft_hid_im": _B_in["dft_hid_im"],
    }

def reference(A_x, A_wqkv, A_wo, B_x, B_dft_seq_re, B_dft_seq_im, B_dft_hid_re, B_dft_hid_im):
    def _A_ref(x, wqkv, wo):
        B, S, D = x.shape
        H = 32
        Dh = D // H
        qkv = (lambda x, *d: x.permute(*d))((x @ wqkv).reshape(B, S, 3, H, Dh), 2, 0, 3, 1, 4)
        q, k, v = qkv[0], qkv[1], qkv[2]
        scores = torch.einsum("bhqd,bhkd->bhqk", q, k) * (Dh ** -0.5)
        p = _softmax(scores, axis=-1)
        o = torch.einsum("bhqk,bhkd->bhqd", p, v)
        return (lambda x, *d: x.permute(*d))(o, 0, 2, 1, 3).reshape(B, S, D) @ wo

    def _B_ref(x, dft_seq_re, dft_seq_im, dft_hid_re, dft_hid_im):
        a = torch.einsum("bsd,ed->bse", x, dft_hid_re)
        b = torch.einsum("bsd,ed->bse", x, dft_hid_im)
        y = torch.einsum("ts,bse->bte", dft_seq_re, a) - torch.einsum("ts,bse->bte", dft_seq_im, b)
        return y

    a = _A_ref(A_x, A_wqkv, A_wo)
    b = _B_ref(B_x, B_dft_seq_re, B_dft_seq_im, B_dft_hid_re, B_dft_hid_im)
    return torch.concatenate([a, b], axis=-1)
```

```cpp
#include <hip/hip_runtime.h>
#include <hip/hip_bf16.h>
#include <stdint.h>
#define ROUND_SCORES 0
using bf16x8 = __attribute__((ext_vector_type(8))) short;
using f32x16 = __attribute__((ext_vector_type(16))) float;
using u32x4 = __attribute__((ext_vector_type(4))) unsigned;
typedef unsigned short u16;
#define MFMA(a, b, c) __builtin_amdgcn_mfma_f32_32x32x16_bf16(a, b, c, 0, 0, 0)
__device__ __forceinline__ unsigned cvtpk(float lo, float hi) { unsigned r; asm("v_cvt_pk_bf16_f32 %0, %1, %2" : "=v"(r) : "v"(lo), "v"(hi)); return r; }
__device__ __forceinline__ float rbf(float x) { return __uint_as_float(cvtpk(x, x) << 16); }
__device__ __forceinline__ int crow(int r, int hi) { return (r & 3) + 8 * (r >> 2) + 4 * hi; }

__global__ void transpose_k(const u16* __restrict__ in, u16* __restrict__ out, int R, int C) {
    __shared__ u16 t[32][33];
    int bx = blockIdx.x * 32, by = blockIdx.y * 32;
    for (int i = threadIdx.y; i < 32; i += 8) t[i][threadIdx.x] = in[(long)(by + i) * C + bx + threadIdx.x];
    __syncthreads();
    for (int i = threadIdx.y; i < 32; i += 8) out[(long)(bx + i) * R + by + threadIdx.x] = t[threadIdx.x][i];
}
__global__ __launch_bounds__(256) void gemm_nt(const u16* __restrict__ A, const u16* __restrict__ A2, int lda, int Khalf, const u16* __restrict__ B, int ldb,
                                               u16* __restrict__ C, int ldc, int K, float sgn) {
    int wid = threadIdx.x >> 6, lane = threadIdx.x & 63, r = lane & 31, h = lane >> 5;
    int m0 = blockIdx.y * 128 + (wid >> 1) * 64, n0 = blockIdx.x * 128 + (wid & 1) * 64;
    f32x16 acc[2][2] = {};
    for (int k = 0; k < K; k += 16) {
        const u16* Ap = k < Khalf ? A + k : A2 + (k - Khalf);
        bf16x8 a[2], b[2];
        for (int i = 0; i < 2; ++i) a[i] = *(const bf16x8*)(Ap + (long)(m0 + 32 * i + r) * lda + 8 * h);
        for (int i = 0; i < 2; ++i) b[i] = *(const bf16x8*)(B + (long)(n0 + 32 * i + r) * ldb + k + 8 * h);
        for (int i = 0; i < 2; ++i) for (int j = 0; j < 2; ++j) acc[i][j] = MFMA(a[i], b[j], acc[i][j]);
    }
    for (int i = 0; i < 2; ++i) for (int j = 0; j < 2; ++j) for (int q = 0; q < 16; ++q)
        C[(long)(m0 + 32 * i + crow(q, h)) * ldc + n0 + 32 * j + r] = (u16)(cvtpk(sgn * acc[i][j][q], 0.f) & 0xffff);
}
__global__ __launch_bounds__(256) void attn(const u16* __restrict__ qkv, const u16* __restrict__ vT, u16* __restrict__ o) {
    int wid = threadIdx.x >> 6, lane = threadIdx.x & 63, r = lane & 31, h = lane >> 5;
    int head = blockIdx.y, q0 = blockIdx.x * 128 + wid * 32;
    bf16x8 qf[2];
    for (int s = 0; s < 2; ++s) qf[s] = *(const bf16x8*)(qkv + (long)(q0 + r) * 3072 + head * 32 + 16 * s + 8 * h);
    f32x16 oacc = {}; float m = -1e30f, l = 0.f;
    const float scale = 0.17677669529663687f, L2E = 1.4426950408889634f;
    for (int k0 = 0; k0 < 2048; k0 += 32) {
        f32x16 s = {};
        for (int t = 0; t < 2; ++t) { bf16x8 kf = *(const bf16x8*)(qkv + (long)(k0 + r) * 3072 + 1024 + head * 32 + 16 * t + 8 * h); s = MFMA(kf, qf[t], s); }
        float mx = -1e30f;
        for (int i = 0; i < 16; ++i) {
            s[i] = s[i] * scale;
            mx = fmaxf(mx, s[i]); }
        mx = fmaxf(mx, __shfl_xor(mx, 32));
        float mn = fmaxf(m, mx), al = __builtin_amdgcn_exp2f((m - mn) * L2E); m = mn;
        float ps = 0.f;
        for (int i = 0; i < 16; ++i) { s[i] = __builtin_amdgcn_exp2f((s[i] - mn) * L2E); ps += s[i]; }
        ps += __shfl_xor(ps, 32);
        l = l * al + ps;
        for (int i = 0; i < 16; ++i) oacc[i] *= al;
        for (int t = 0; t < 2; ++t) {
            u32x4 pw = {cvtpk(s[8 * t], s[8 * t + 1]), cvtpk(s[8 * t + 2], s[8 * t + 3]), cvtpk(s[8 * t + 4], s[8 * t + 5]), cvtpk(s[8 * t + 6], s[8 * t + 7])};
            const u16* vp = vT + (long)(head * 32 + r) * 2048 + k0 + 16 * t + 4 * h;
            uint2 lo = *(const uint2*)vp, hi = *(const uint2*)(vp + 8);
            u32x4 vw = {lo.x, lo.y, hi.x, hi.y};
            oacc = MFMA(__builtin_bit_cast(bf16x8, vw), __builtin_bit_cast(bf16x8, pw), oacc);
        }
    }
    float inv = 1.f / l;
    for (int i = 0; i < 16; ++i) o[(long)(q0 + r) * 1024 + head * 32 + crow(i, h)] = (u16)(cvtpk(oacc[i] * inv, 0.f) & 0xffff);
}

extern "C" void kernel_launch(void* const* d_in, const int* in_sizes, int n_in, void* d_out, int out_size, void* d_ws, size_t ws_size, hipStream_t stream) {
    const u16* Ax = (const u16*)d_in[0]; const u16* wqkv = (const u16*)d_in[1]; const u16* wo = (const u16*)d_in[2]; const u16* Bx = (const u16*)d_in[3];
    const u16* sre = (const u16*)d_in[4]; const u16* sim = (const u16*)d_in[5]; const u16* hre = (const u16*)d_in[6]; const u16* him = (const u16*)d_in[7];
    u16* out = (u16*)d_out; char* ws = (char*)d_ws;
    u16* wqkvT = (u16*)(ws); u16* woT = (u16*)(ws + (8 << 20)); u16* qkv = (u16*)(ws + (16 << 20)); u16* vT = (u16*)(ws + (32 << 20));
    u16* abT = (u16*)(ws + (40 << 20)); u16* o = (u16*)(ws + (56 << 20));
    transpose_k<<<dim3(3072 / 32, 1024 / 32), dim3(32, 8), 0, stream>>>(wqkv, wqkvT, 1024, 3072);
    transpose_k<<<dim3(1024 / 32, 1024 / 32), dim3(32, 8), 0, stream>>>(wo, woT, 1024, 1024);
    gemm_nt<<<dim3(3072 / 128, 2048 / 128), 256, 0, stream>>>(Ax, Ax, 1024, 1 << 30, wqkvT, 1024, qkv, 3072, 1024, 1.f);
    gemm_nt<<<dim3(2048 / 128, 1024 / 128), 256, 0, stream>>>(wqkvT + 2048 * 1024, wqkvT, 1024, 1 << 30, Ax, 1024, vT, 2048, 1024, 1.f);
    gemm_nt<<<dim3(2048 / 128, 1024 / 128), 256, 0, stream>>>(hre, hre, 1024, 1 << 30, Bx, 1024, abT, 4096, 1024, 1.f);
    gemm_nt<<<dim3(2048 / 128, 1024 / 128), 256, 0, stream>>>(him, him, 1024, 1 << 30, Bx, 1024, abT + 2048, 4096, 1024, -1.f);
    attn<<<dim3(2048 / 128, 32), 256, 0, stream>>>(qkv, vT, o);
    gemm_nt<<<dim3(1024 / 128, 2048 / 128), 256, 0, stream>>>(o, o, 1024, 1 << 30, woT, 1024, out, 2048, 1024, 1.f);
    gemm_nt<<<dim3(1024 / 128, 2048 / 128), 256, 0, stream>>>(sre, sim, 2048, 2048, abT, 4096, out + 1024, 2048, 4096, 1.f);
}
```

```cpp
#include <hip/hip_runtime.h>
#include <stdint.h>

#define LAS __attribute__((address_space(3)))
#define DI __device__ __forceinline__
typedef float f32x16 __attribute__((ext_vector_type(16)));
typedef unsigned u32x4 __attribute__((ext_vector_type(4)));
typedef unsigned short u16;
typedef unsigned long long u64;
typedef LAS char* ldsp;
typedef __amdgpu_buffer_rsrc_t srd_t;

#define UU_ASM \
    "s_nop 4\n\t" \
    "v_mov_b32 v252, 0\n\t" \
    "s_mov_b64 s[82:83], %[pe]\n\t" \
    "s_nop 0\n\t" \
    "global_load_dwordx2 v[250:251], v252, s[82:83] sc1\n\t" \
    "s_mov_b64 s[72:73], %[pf0]\n\t" \
    "s_mov_b64 s[74:75], %[pf1]\n\t" \
    "s_mov_b64 s[76:77], %[pf2]\n\t" \
    "s_cmp_lg_u32 %[isld], 0\n\t" \
    "s_cbranch_scc1 Lld_%=\n\t" \
    "v_mov_b32 v224, %[ap]\n\t" \
    "v_mov_b32 v226, %[aq]\n\t" \
    "v_xor_b32 v225, 64, v224\n\t" \
    "v_xor_b32 v227, 32, v226\n\t" \
    "v_xor_b32 v228, 64, v226\n\t" \
    "v_xor_b32 v229, 96, v226\n\t" \
    "v_mov_b32 v0, 0\n\t" \
    "v_mov_b32 v1, 0\n\t" \
    "v_mov_b32 v2, 0\n\t" \
    "v_mov_b32 v3, 0\n\t" \
    "v_mov_b32 v4, 0\n\t" \
    "v_mov_b32 v5, 0\n\t" \
    "v_mov_b32 v6, 0\n\t" \
    "v_mov_b32 v7, 0\n\t" \
    "v_mov_b32 v8, 0\n\t" \
    "v_mov_b32 v9, 0\n\t" \
    "v_mov_b32 v10, 0\n\t" \
    "v_mov_b32 v11, 0\n\t" \
    "v_mov_b32 v12, 0\n\t" \
    "v_mov_b32 v13, 0\n\t" \
    "v_mov_b32 v14, 0\n\t" \
    "v_mov_b32 v15, 0\n\t" \
    "v_mov_b32 v16, 0\n\t" \
    "v_mov_b32 v17, 0\n\t" \
    "v_mov_b32 v18, 0\n\t" \
    "v_mov_b32 v19, 0\n\t" \
    "v_mov_b32 v20, 0\n\t" \
    "v_mov_b32 v21, 0\n\t" \
    "v_mov_b32 v22, 0\n\t" \
    "v_mov_b32 v23, 0\n\t" \
    "v_mov_b32 v24, 0\n\t" \
    "v_mov_b32 v25, 0\n\t" \
    "v_mov_b32 v26, 0\n\t" \
    "v_mov_b32 v27, 0\n\t" \
    "v_mov_b32 v28, 0\n\t" \
    "v_mov_b32 v29, 0\n\t" \
    "v_mov_b32 v30, 0\n\t" \
    "v_mov_b32 v31, 0\n\t" \
    "v_mov_b32 v32, 0\n\t" \
    "v_mov_b32 v33, 0\n\t" \
    "v_mov_b32 v34, 0\n\t" \
    "v_mov_b32 v35, 0\n\t" \
    "v_mov_b32 v36, 0\n\t" \
    "v_mov_b32 v37, 0\n\t" \
    "v_mov_b32 v38, 0\n\t" \
    "v_mov_b32 v39, 0\n\t" \
    "v_mov_b32 v40, 0\n\t" \
    "v_mov_b32 v41, 0\n\t" \
    "v_mov_b32 v42, 0\n\t" \
    "v_mov_b32 v43, 0\n\t" \
    "v_mov_b32 v44, 0\n\t" \
    "v_mov_b32 v45, 0\n\t" \
    "v_mov_b32 v46, 0\n\t" \
    "v_mov_b32 v47, 0\n\t" \
    "v_mov_b32 v48, 0\n\t" \
    "v_mov_b32 v49, 0\n\t" \
    "v_mov_b32 v50, 0\n\t" \
    "v_mov_b32 v51, 0\n\t" \
    "v_mov_b32 v52, 0\n\t" \
    "v_mov_b32 v53, 0\n\t" \
    "v_mov_b32 v54, 0\n\t" \
    "v_mov_b32 v55, 0\n\t" \
    "v_mov_b32 v56, 0\n\t" \
    "v_mov_b32 v57, 0\n\t" \
    "v_mov_b32 v58, 0\n\t" \
    "v_mov_b32 v59, 0\n\t" \
    "v_mov_b32 v60, 0\n\t" \
    "v_mov_b32 v61, 0\n\t" \
    "v_mov_b32 v62, 0\n\t" \
    "v_mov_b32 v63, 0\n\t" \
    "v_mov_b32 v64, 0\n\t" \
    "v_mov_b32 v65, 0\n\t" \
    "v_mov_b32 v66, 0\n\t" \
    "v_mov_b32 v67, 0\n\t" \
    "v_mov_b32 v68, 0\n\t" \
    "v_mov_b32 v69, 0\n\t" \
    "v_mov_b32 v70, 0\n\t" \
    "v_mov_b32 v71, 0\n\t" \
    "v_mov_b32 v72, 0\n\t" \
    "v_mov_b32 v73, 0\n\t" \
    "v_mov_b32 v74, 0\n\t" \
    "v_mov_b32 v75, 0\n\t" \
    "v_mov_b32 v76, 0\n\t" \
    "v_mov_b32 v77, 0\n\t" \
    "v_mov_b32 v78, 0\n\t" \
    "v_mov_b32 v79, 0\n\t" \
    "v_mov_b32 v80, 0\n\t" \
    "v_mov_b32 v81, 0\n\t" \
    "v_mov_b32 v82, 0\n\t" \
    "v_mov_b32 v83, 0\n\t" \
    "v_mov_b32 v84, 0\n\t" \
    "v_mov_b32 v85, 0\n\t" \
    "v_mov_b32 v86, 0\n\t" \
    "v_mov_b32 v87, 0\n\t" \
    "v_mov_b32 v88, 0\n\t" \
    "v_mov_b32 v89, 0\n\t" \
    "v_mov_b32 v90, 0\n\t" \
    "v_mov_b32 v91, 0\n\t" \
    "v_mov_b32 v92, 0\n\t" \
    "v_mov_b32 v93, 0\n\t" \
    "v_mov_b32 v94, 0\n\t" \
    "v_mov_b32 v95, 0\n\t" \
    "v_mov_b32 v96, 0\n\t" \
    "v_mov_b32 v97, 0\n\t" \
    "v_mov_b32 v98, 0\n\t" \
    "v_mov_b32 v99, 0\n\t" \
    "v_mov_b32 v100, 0\n\t" \
    "v_mov_b32 v101, 0\n\t" \
    "v_mov_b32 v102, 0\n\t" \
    "v_mov_b32 v103, 0\n\t" \
    "v_mov_b32 v104, 0\n\t" \
    "v_mov_b32 v105, 0\n\t" \
    "v_mov_b32 v106, 0\n\t" \
    "v_mov_b32 v107, 0\n\t" \
    "v_mov_b32 v108, 0\n\t" \
    "v_mov_b32 v109, 0\n\t" \
    "v_mov_b32 v110, 0\n\t" \
    "v_mov_b32 v111, 0\n\t" \
    "v_mov_b32 v112, 0\n\t" \
    "v_mov_b32 v113, 0\n\t" \
    "v_mov_b32 v114, 0\n\t" \
    "v_mov_b32 v115, 0\n\t" \
    "v_mov_b32 v116, 0\n\t" \
    "v_mov_b32 v117, 0\n\t" \
    "v_mov_b32 v118, 0\n\t" \
    "v_mov_b32 v119, 0\n\t" \
    "v_mov_b32 v120, 0\n\t" \
    "v_mov_b32 v121, 0\n\t" \
    "v_mov_b32 v122, 0\n\t" \
    "v_mov_b32 v123, 0\n\t" \
    "v_mov_b32 v124, 0\n\t" \
    "v_mov_b32 v125, 0\n\t" \
    "v_mov_b32 v126, 0\n\t" \
    "v_mov_b32 v127, 0\n\t" \
    "s_mov_b32 s51, 0\n\t" \
    "s_mov_b32 s49, 32\n\t" \
    "s_mov_b32 s53, 4294868992\n\t" \
    "s_mov_b32 s54, 49152\n\t" \
    "s_waitcnt vmcnt(0)\n\t" \
    "s_barrier\n\t" \
    "ds_read_b64_tr_b16 v[192:193], v226 offset:0\n\t" \
    "ds_read_b64_tr_b16 v[194:195], v226 offset:512\n\t" \
    "ds_read_b128 v[128:131], v224 offset:0\n\t" \
    "ds_read_b64_tr_b16 v[196:197], v227 offset:0\n\t" \
    "ds_read_b64_tr_b16 v[198:199], v227 offset:512\n\t" \
    "ds_read_b64_tr_b16 v[200:201], v228 offset:0\n\t" \
    "ds_read_b64_tr_b16 v[202:203], v228 offset:512\n\t" \
    "ds_read_b64_tr_b16 v[204:205], v229 offset:0\n\t" \
    "ds_read_b64_tr_b16 v[206:207], v229 offset:512\n\t" \
    "ds_read_b128 v[132:135], v224 offset:2048\n\t" \
    "ds_read_b128 v[136:139], v224 offset:4096\n\t" \
    "ds_read_b128 v[140:143], v224 offset:6144\n\t" \
    "ds_read_b128 v[144:147], v224 offset:8192\n\t" \
    "ds_read_b128 v[148:151], v224 offset:10240\n\t" \
    "ds_read_b128 v[152:155], v224 offset:12288\n\t" \
    "ds_read_b128 v[156:159], v224 offset:14336\n\t" \
    "s_waitcnt lgkmcnt(13)\n\t" \
    "v_mfma_f32_16x16x32_bf16 v[0:3], v[192:195], v[128:131], v[0:3]\n\t" \
    "ds_read_b64_tr_b16 v[208:209], v226 offset:4096\n\t" \
    "s_waitcnt lgkmcnt(12)\n\t" \
    "v_mfma_f32_16x16x32_bf16 v[4:7], v[196:199], v[128:131], v[4:7]\n\t" \
    "ds_read_b64_tr_b16 v[210:211], v226 offset:4608\n\t" \
    "s_waitcnt lgkmcnt(11)\n\t" \
    "v_mfma_f32_16x16x32_bf16 v[8:11], v[200:203], v[128:131], v[8:11]\n\t" \
    "ds_read_b128 v[160:163], v225 offset:0\n\t" \
    "s_waitcnt lgkmcnt(10)\n\t" \
    "v_mfma_f32_16x16x32_bf16 v[12:15], v[204:207], v[128:131], v[12:15]\n\t" \
    "ds_read_b64_tr_b16 v[212:213], v227 offset:4096\n\t" \
    "s_waitcnt lgkmcnt(10)\n\t" \
    "v_mfma_f32_16x16x32_bf16 v[16:19], v[192:195], v[132:135], v[16:19]\n\t" \
    "ds_read_b64_tr_b16 v[214:215], v227 offset:4608\n\t" \
    "v_mfma_f32_16x16x32_bf16 v[20:23], v[196:199], v[132:135], v[20:23]\n\t" \
    "ds_read_b64_tr_b16 v[216:217], v228 offset:4096\n\t" \
    "v_mfma_f32_16x16x32_bf16 v[24:27], v[200:203], v[132:135], v[24:27]\n\t" \
    "ds_read_b64_tr_b16 v[218:219], v228 offset:4608\n\t" \
    "v_mfma_f32_16x16x32_bf16 v[28:31], v[204:207], v[132:135], v[28:31]\n\t" \
    "ds_read_b64_tr_b16 v[220:221], v229 offset:4096\n\t" \
    "s_waitcnt lgkmcnt(13)\n\t" \
    "v_mfma_f32_16x16x32_bf16 v[32:35], v[192:195], v[136:139], v[32:35]\n\t" \
    "ds_read_b64_tr_b16 v[222:223], v229 offset:4608\n\t" \
    "v_mfma_f32_16x16x32_bf16 v[36:39], v[196:199], v[136:139], v[36:39]\n\t" \
    "ds_read_b128 v[164:167], v225 offset:2048\n\t" \
    "v_mfma_f32_16x16x32_bf16 v[40:43], v[200:203], v[136:139], v[40:43]\n\t" \
    "ds_read_b128 v[168:171], v225 offset:4096\n\t" \
    "v_mfma_f32_16x16x32_bf16 v[44:47], v[204:207], v[136:139], v[44:47]\n\t" \
    "ds_read_b128 v[172:175], v225 offset:6144\n\t" \
    "s_waitcnt lgkmcnt(15)\n\t" \
    "v_mfma_f32_16x16x32_bf16 v[48:51], v[192:195], v[140:143], v[48:51]\n\t" \
    "ds_read_b128 v[176:179], v225 offset:8192\n\t" \
    "v_mfma_f32_16x16x32_bf16 v[52:55], v[196:199], v[140:143], v[52:55]\n\t" \
    "ds_read_b128 v[180:183], v225 offset:10240\n\t" \
    "v_mfma_f32_16x16x32_bf16 v[56:59], v[200:203], v[140:143], v[56:59]\n\t" \
    "ds_read_b128 v[184:187], v225 offset:12288\n\t" \
    "v_mfma_f32_16x16x32_bf16 v[60:63], v[204:207], v[140:143], v[60:63]\n\t" \
    "ds_read_b128 v[188:191], v225 offset:14336\n\t" \
    "v_mfma_f32_16x16x32_bf16 v[64:67], v[192:195], v[144:147], v[64:67]\n\t" \
    "s_add_u32 s51, s51, 49152\n\t" \
    "v_mfma_f32_16x16x32_bf16 v[68:71], v[196:199], v[144:147], v[68:71]\n\t" \
    "s_cmp_eq_u32 s51, 147456\n\t" \
    "v_mfma_f32_16x16x32_bf16 v[72:75], v[200:203], v[144:147], v[72:75]\n\t" \
    "s_cselect_b32 s50, s53, s54\n\t" \
    "v_mfma_f32_16x16x32_bf16 v[76:79], v[204:207], v[144:147], v[76:79]\n\t" \
    "s_cselect_b32 s51, 0, s51\n\t" \
    "s_waitcnt lgkmcnt(15)\n\t" \
    "v_mfma_f32_16x16x32_bf16 v[80:83], v[192:195], v[148:151], v[80:83]\n\t" \
    "v_add_u32 v224, s50, v224\n\t" \
    "v_mfma_f32_16x16x32_bf16 v[84:87], v[196:199], v[148:151], v[84:87]\n\t" \
    "v_add_u32 v225, s50, v225\n\t" \
    "v_mfma_f32_16x16x32_bf16 v[88:91], v[200:203], v[148:151], v[88:91]\n\t" \
    "v_add_u32 v226, s50, v226\n\t" \
    "v_mfma_f32_16x16x32_bf16 v[92:95], v[204:207], v[148:151], v[92:95]\n\t" \
    "v_add_u32 v227, s50, v227\n\t" \
    "v_mfma_f32_16x16x32_bf16 v[96:99], v[192:195], v[152:155], v[96:99]\n\t" \
    "v_add_u32 v228, s50, v228\n\t" \
    "v_mfma_f32_16x16x32_bf16 v[100:103], v[196:199], v[152:155], v[100:103]\n\t" \
    "v_add_u32 v229, s50, v229\n\t" \
    "v_mfma_f32_16x16x32_bf16 v[104:107], v[200:203], v[152:155], v[104:107]\n\t" \
    "v_mfma_f32_16x16x32_bf16 v[108:111], v[204:207], v[152:155], v[108:111]\n\t" \
    "v_mfma_f32_16x16x32_bf16 v[112:115], v[192:195], v[156:159], v[112:115]\n\t" \
    "v_mfma_f32_16x16x32_bf16 v[116:119], v[196:199], v[156:159], v[116:119]\n\t" \
    "v_mfma_f32_16x16x32_bf16 v[120:123], v[200:203], v[156:159], v[120:123]\n\t" \
    "v_mfma_f32_16x16x32_bf16 v[124:127], v[204:207], v[156:159], v[124:127]\n\t" \
    "s_waitcnt lgkmcnt(0)\n\t" \
    "s_barrier\n\t" \
    "v_mfma_f32_16x16x32_bf16 v[0:3], v[208:211], v[160:163], v[0:3]\n\t" \
    "ds_read_b64_tr_b16 v[192:193], v226 offset:0\n\t" \
    "v_mfma_f32_16x16x32_bf16 v[4:7], v[212:215], v[160:163], v[4:7]\n\t" \
    "ds_read_b64_tr_b16 v[194:195], v226 offset:512\n\t" \
    "v_mfma_f32_16x16x32_bf16 v[8:11], v[216:219], v[160:163], v[8:11]\n\t" \
    "ds_read_b128 v[128:131], v224 offset:0\n\t" \
    "v_mfma_f32_16x16x32_bf16 v[12:15], v[220:223], v[160:163], v[12:15]\n\t" \
    "ds_read_b64_tr_b16 v[196:197], v227 offset:0\n\t" \
    "v_mfma_f32_16x16x32_bf16 v[16:19], v[208:211], v[164:167], v[16:19]\n\t" \
    "ds_read_b64_tr_b16 v[198:199], v227 offset:512\n\t" \
    "v_mfma_f32_16x16x32_bf16 v[20:23], v[212:215], v[164:167], v[20:23]\n\t" \
    "ds_read_b64_tr_b16 v[200:201], v228 offset:0\n\t" \
    "v_mfma_f32_16x16x32_bf16 v[24:27], v[216:219], v[164:167], v[24:27]\n\t" \
    "ds_read_b64_tr_b16 v[202:203], v228 offset:512\n\t" \
    "v_mfma_f32_16x16x32_bf16 v[28:31], v[220:223], v[164:167], v[28:31]\n\t" \
    "ds_read_b64_tr_b16 v[204:205], v229 offset:0\n\t" \
    "v_mfma_f32_16x16x32_bf16 v[32:35], v[208:211], v[168:171], v[32:35]\n\t" \
    "ds_read_b64_tr_b16 v[206:207], v229 offset:512\n\t" \
    "v_mfma_f32_16x16x32_bf16 v[36:39], v[212:215], v[168:171], v[36:39]\n\t" \
    "ds_read_b128 v[132:135], v224 offset:2048\n\t" \
    "v_mfma_f32_16x16x32_bf16 v[40:43], v[216:219], v[168:171], v[40:43]\n\t" \
    "ds_read_b128 v[136:139], v224 offset:4096\n\t" \
    "v_mfma_f32_16x16x32_bf16 v[44:47], v[220:223], v[168:171], v[44:47]\n\t" \
    "ds_read_b128 v[140:143], v224 offset:6144\n\t" \
    "v_mfma_f32_16x16x32_bf16 v[48:51], v[208:211], v[172:175], v[48:51]\n\t" \
    "ds_read_b128 v[144:147], v224 offset:8192\n\t" \
    "v_mfma_f32_16x16x32_bf16 v[52:55], v[212:215], v[172:175], v[52:55]\n\t" \
    "ds_read_b128 v[148:151], v224 offset:10240\n\t" \
    "v_mfma_f32_16x16x32_bf16 v[56:59], v[216:219], v[172:175], v[56:59]\n\t" \
    "ds_read_b128 v[152:155], v224 offset:12288\n\t" \
    "v_mfma_f32_16x16x32_bf16 v[60:63], v[220:223], v[172:175], v[60:63]\n\t" \
    "ds_read_b128 v[156:159], v224 offset:14336\n\t" \
    "v_mfma_f32_16x16x32_bf16 v[64:67], v[208:211], v[176:179], v[64:67]\n\t" \
    "s_add_u32 s70, s72, 1024\n\t" \
    "s_addc_u32 s71, s73, 0\n\t" \
    "global_load_dwordx4 v[252:255], %[vl16], s[70:71]\n\t" \
    "v_mfma_f32_16x16x32_bf16 v[68:71], v[212:215], v[176:179], v[68:71]\n\t" \
    "v_mfma_f32_16x16x32_bf16 v[72:75], v[216:219], v[176:179], v[72:75]\n\t" \
    "v_mfma_f32_16x16x32_bf16 v[76:79], v[220:223], v[176:179], v[76:79]\n\t" \
    "v_mfma_f32_16x16x32_bf16 v[80:83], v[208:211], v[180:183], v[80:83]\n\t" \
    "v_mfma_f32_16x16x32_bf16 v[84:87], v[212:215], v[180:183], v[84:87]\n\t" \
    "v_mfma_f32_16x16x32_bf16 v[88:91], v[216:219], v[180:183], v[88:91]\n\t" \
    "v_mfma_f32_16x16x32_bf16 v[92:95], v[220:223], v[180:183], v[92:95]\n\t" \
    "v_mfma_f32_16x16x32_bf16 v[96:99], v[208:211], v[184:187], v[96:99]\n\t" \
    "v_mfma_f32_16x16x32_bf16 v[100:103], v[212:215], v[184:187], v[100:103]\n\t" \
    "v_mfma_f32_16x16x32_bf16 v[104:107], v[216:219], v[184:187], v[104:107]\n\t" \
    "v_mfma_f32_16x16x32_bf16 v[108:111], v[220:223], v[184:187], v[108:111]\n\t" \
    "v_mfma_f32_16x16x32_bf16 v[112:115], v[208:211], v[188:191], v[112:115]\n\t" \
    "v_mfma_f32_16x16x32_bf16 v[116:119], v[212:215], v[188:191], v[116:119]\n\t" \
    "v_mfma_f32_16x16x32_bf16 v[120:123], v[216:219], v[188:191], v[120:123]\n\t" \
    "v_mfma_f32_16x16x32_bf16 v[124:127], v[220:223], v[188:191], v[124:127]\n\t" \
    "s_waitcnt lgkmcnt(13)\n\t" \
    "v_mfma_f32_16x16x32_bf16 v[0:3], v[192:195], v[128:131], v[0:3]\n\t" \
    "ds_read_b64_tr_b16 v[208:209], v226 offset:4096\n\t" \
    "s_waitcnt lgkmcnt(12)\n\t" \
    "v_mfma_f32_16x16x32_bf16 v[4:7], v[196:199], v[128:131], v[4:7]\n\t" \
    "ds_read_b64_tr_b16 v[210:211], v226 offset:4608\n\t" \
    "s_waitcnt lgkmcnt(11)\n\t" \
    "v_mfma_f32_16x16x32_bf16 v[8:11], v[200:203], v[128:131], v[8:11]\n\t" \
    "ds_read_b128 v[160:163], v225 offset:0\n\t" \
    "s_waitcnt lgkmcnt(10)\n\t" \
    "v_mfma_f32_16x16x32_bf16 v[12:15], v[204:207], v[128:131], v[12:15]\n\t" \
    "ds_read_b64_tr_b16 v[212:213], v227 offset:4096\n\t" \
    "s_waitcnt lgkmcnt(10)\n\t" \
    "v_mfma_f32_16x16x32_bf16 v[16:19], v[192:195], v[132:135], v[16:19]\n\t" \
    "ds_read_b64_tr_b16 v[214:215], v227 offset:4608\n\t" \
    "v_mfma_f32_16x16x32_bf16 v[20:23], v[196:199], v[132:135], v[20:23]\n\t" \
    "ds_read_b64_tr_b16 v[216:217], v228 offset:4096\n\t" \
    "v_mfma_f32_16x16x32_bf16 v[24:27], v[200:203], v[132:135], v[24:27]\n\t" \
    "ds_read_b64_tr_b16 v[218:219], v228 offset:4608\n\t" \
    "v_mfma_f32_16x16x32_bf16 v[28:31], v[204:207], v[132:135], v[28:31]\n\t" \
    "ds_read_b64_tr_b16 v[220:221], v229 offset:4096\n\t" \
    "s_waitcnt lgkmcnt(13)\n\t" \
    "v_mfma_f32_16x16x32_bf16 v[32:35], v[192:195], v[136:139], v[32:35]\n\t" \
    "ds_read_b64_tr_b16 v[222:223], v229 offset:4608\n\t" \
    "v_mfma_f32_16x16x32_bf16 v[36:39], v[196:199], v[136:139], v[36:39]\n\t" \
    "ds_read_b128 v[164:167], v225 offset:2048\n\t" \
    "v_mfma_f32_16x16x32_bf16 v[40:43], v[200:203], v[136:139], v[40:43]\n\t" \
    "ds_read_b128 v[168:171], v225 offset:4096\n\t" \
    "v_mfma_f32_16x16x32_bf16 v[44:47], v[204:207], v[136:139], v[44:47]\n\t" \
    "ds_read_b128 v[172:175], v225 offset:6144\n\t" \
    "s_waitcnt lgkmcnt(15)\n\t" \
    "v_mfma_f32_16x16x32_bf16 v[48:51], v[192:195], v[140:143], v[48:51]\n\t" \
    "ds_read_b128 v[176:179], v225 offset:8192\n\t" \
    "v_mfma_f32_16x16x32_bf16 v[52:55], v[196:199], v[140:143], v[52:55]\n\t" \
    "ds_read_b128 v[180:183], v225 offset:10240\n\t" \
    "v_mfma_f32_16x16x32_bf16 v[56:59], v[200:203], v[140:143], v[56:59]\n\t" \
    "ds_read_b128 v[184:187], v225 offset:12288\n\t" \
    "v_mfma_f32_16x16x32_bf16 v[60:63], v[204:207], v[140:143], v[60:63]\n\t" \
    "ds_read_b128 v[188:191], v225 offset:14336\n\t" \
    "v_mfma_f32_16x16x32_bf16 v[64:67], v[192:195], v[144:147], v[64:67]\n\t" \
    "s_add_u32 s51, s51, 49152\n\t" \
    "v_mfma_f32_16x16x32_bf16 v[68:71], v[196:199], v[144:147], v[68:71]\n\t" \
    "s_cmp_eq_u32 s51, 147456\n\t" \
    "v_mfma_f32_16x16x32_bf16 v[72:75], v[200:203], v[144:147], v[72:75]\n\t" \
    "s_cselect_b32 s50, s53, s54\n\t" \
    "v_mfma_f32_16x16x32_bf16 v[76:79], v[204:207], v[144:147], v[76:79]\n\t" \
    "s_cselect_b32 s51, 0, s51\n\t" \
    "s_waitcnt lgkmcnt(15)\n\t" \
    "v_mfma_f32_16x16x32_bf16 v[80:83], v[192:195], v[148:151], v[80:83]\n\t" \
    "v_add_u32 v224, s50, v224\n\t" \
    "v_mfma_f32_16x16x32_bf16 v[84:87], v[196:199], v[148:151], v[84:87]\n\t" \
    "v_add_u32 v225, s50, v225\n\t" \
    "v_mfma_f32_16x16x32_bf16 v[88:91], v[200:203], v[148:151], v[88:91]\n\t" \
    "v_add_u32 v226, s50, v226\n\t" \
    "v_mfma_f32_16x16x32_bf16 v[92:95], v[204:207], v[148:151], v[92:95]\n\t" \
    "v_add_u32 v227, s50, v227\n\t" \
    "v_mfma_f32_16x16x32_bf16 v[96:99], v[192:195], v[152:155], v[96:99]\n\t" \
    "v_add_u32 v228, s50, v228\n\t" \
    "v_mfma_f32_16x16x32_bf16 v[100:103], v[196:199], v[152:155], v[100:103]\n\t" \
    "v_add_u32 v229, s50, v229\n\t" \
    "v_mfma_f32_16x16x32_bf16 v[104:107], v[200:203], v[152:155], v[104:107]\n\t" \
    "v_mfma_f32_16x16x32_bf16 v[108:111], v[204:207], v[152:155], v[108:111]\n\t" \
    "v_mfma_f32_16x16x32_bf16 v[112:115], v[192:195], v[156:159], v[112:115]\n\t" \
    "v_mfma_f32_16x16x32_bf16 v[116:119], v[196:199], v[156:159], v[116:119]\n\t" \
    "v_mfma_f32_16x16x32_bf16 v[120:123], v[200:203], v[156:159], v[120:123]\n\t" \
    "v_mfma_f32_16x16x32_bf16 v[124:127], v[204:207], v[156:159], v[124:127]\n\t" \
    "s_waitcnt lgkmcnt(0)\n\t" \
    "s_barrier\n\t" \
    "v_mfma_f32_16x16x32_bf16 v[0:3], v[208:211], v[160:163], v[0:3]\n\t" \
    "ds_read_b64_tr_b16 v[192:193], v226 offset:0\n\t" \
    "v_mfma_f32_16x16x32_bf16 v[4:7], v[212:215], v[160:163], v[4:7]\n\t" \
    "ds_read_b64_tr_b16 v[194:195], v226 offset:512\n\t" \
    "v_mfma_f32_16x16x32_bf16 v[8:11], v[216:219], v[160:163], v[8:11]\n\t" \
    "ds_read_b128 v[128:131], v224 offset:0\n\t" \
    "v_mfma_f32_16x16x32_bf16 v[12:15], v[220:223], v[160:163], v[12:15]\n\t" \
    "ds_read_b64_tr_b16 v[196:197], v227 offset:0\n\t" \
    "v_mfma_f32_16x16x32_bf16 v[16:19], v[208:211], v[164:167], v[16:19]\n\t" \
    "ds_read_b64_tr_b16 v[198:199], v227 offset:512\n\t" \
    "v_mfma_f32_16x16x32_bf16 v[20:23], v[212:215], v[164:167], v[20:23]\n\t" \
    "ds_read_b64_tr_b16 v[200:201], v228 offset:0\n\t" \
    "v_mfma_f32_16x16x32_bf16 v[24:27], v[216:219], v[164:167], v[24:27]\n\t" \
    "ds_read_b64_tr_b16 v[202:203], v228 offset:512\n\t" \
    "v_mfma_f32_16x16x32_bf16 v[28:31], v[220:223], v[164:167], v[28:31]\n\t" \
    "ds_read_b64_tr_b16 v[204:205], v229 offset:0\n\t" \
    "v_mfma_f32_16x16x32_bf16 v[32:35], v[208:211], v[168:171], v[32:35]\n\t" \
    "ds_read_b64_tr_b16 v[206:207], v229 offset:512\n\t" \
    "v_mfma_f32_16x16x32_bf16 v[36:39], v[212:215], v[168:171], v[36:39]\n\t" \
    "ds_read_b128 v[132:135], v224 offset:2048\n\t" \
    "v_mfma_f32_16x16x32_bf16 v[40:43], v[216:219], v[168:171], v[40:43]\n\t" \
    "ds_read_b128 v[136:139], v224 offset:4096\n\t" \
    "v_mfma_f32_16x16x32_bf16 v[44:47], v[220:223], v[168:171], v[44:47]\n\t" \
    "ds_read_b128 v[140:143], v224 offset:6144\n\t" \
    "v_mfma_f32_16x16x32_bf16 v[48:51], v[208:211], v[172:175], v[48:51]\n\t" \
    "ds_read_b128 v[144:147], v224 offset:8192\n\t" \
    "v_mfma_f32_16x16x32_bf16 v[52:55], v[212:215], v[172:175], v[52:55]\n\t" \
    "ds_read_b128 v[148:151], v224 offset:10240\n\t" \
    "v_mfma_f32_16x16x32_bf16 v[56:59], v[216:219], v[172:175], v[56:59]\n\t" \
    "ds_read_b128 v[152:155], v224 offset:12288\n\t" \
    "v_mfma_f32_16x16x32_bf16 v[60:63], v[220:223], v[172:175], v[60:63]\n\t" \
    "ds_read_b128 v[156:159], v224 offset:14336\n\t" \
    "v_mfma_f32_16x16x32_bf16 v[64:67], v[208:211], v[176:179], v[64:67]\n\t" \
    "s_add_u32 s70, s72, 2098176\n\t" \
    "s_addc_u32 s71, s73, 0\n\t" \
    "global_load_dwordx4 v[252:255], %[vl16], s[70:71]\n\t" \
    "v_mfma_f32_16x16x32_bf16 v[68:71], v[212:215], v[176:179], v[68:71]\n\t" \
    "v_mfma_f32_16x16x32_bf16 v[72:75], v[216:219], v[176:179], v[72:75]\n\t" \
    "v_mfma_f32_16x16x32_bf16 v[76:79], v[220:223], v[176:179], v[76:79]\n\t" \
    "v_mfma_f32_16x16x32_bf16 v[80:83], v[208:211], v[180:183], v[80:83]\n\t" \
    "v_mfma_f32_16x16x32_bf16 v[84:87], v[212:215], v[180:183], v[84:87]\n\t" \
    "v_mfma_f32_16x16x32_bf16 v[88:91], v[216:219], v[180:183], v[88:91]\n\t" \
    "v_mfma_f32_16x16x32_bf16 v[92:95], v[220:223], v[180:183], v[92:95]\n\t" \
    "v_mfma_f32_16x16x32_bf16 v[96:99], v[208:211], v[184:187], v[96:99]\n\t" \
    "v_mfma_f32_16x16x32_bf16 v[100:103], v[212:215], v[184:187], v[100:103]\n\t" \
    "v_mfma_f32_16x16x32_bf16 v[104:107], v[216:219], v[184:187], v[104:107]\n\t" \
    "v_mfma_f32_16x16x32_bf16 v[108:111], v[220:223], v[184:187], v[108:111]\n\t" \
    "v_mfma_f32_16x16x32_bf16 v[112:115], v[208:211], v[188:191], v[112:115]\n\t" \
    "v_mfma_f32_16x16x32_bf16 v[116:119], v[212:215], v[188:191], v[116:119]\n\t" \
    "v_mfma_f32_16x16x32_bf16 v[120:123], v[216:219], v[188:191], v[120:123]\n\t" \
    "v_mfma_f32_16x16x32_bf16 v[124:127], v[220:223], v[188:191], v[124:127]\n\t" \
    "s_waitcnt lgkmcnt(13)\n\t" \
    "v_mfma_f32_16x16x32_bf16 v[0:3], v[192:195], v[128:131], v[0:3]\n\t" \
    "ds_read_b64_tr_b16 v[208:209], v226 offset:4096\n\t" \
    "s_waitcnt lgkmcnt(12)\n\t" \
    "v_mfma_f32_16x16x32_bf16 v[4:7], v[196:199], v[128:131], v[4:7]\n\t" \
    "ds_read_b64_tr_b16 v[210:211], v226 offset:4608\n\t" \
    "s_waitcnt lgkmcnt(11)\n\t" \
    "v_mfma_f32_16x16x32_bf16 v[8:11], v[200:203], v[128:131], v[8:11]\n\t" \
    "ds_read_b128 v[160:163], v225 offset:0\n\t" \
    "s_waitcnt lgkmcnt(10)\n\t" \
    "v_mfma_f32_16x16x32_bf16 v[12:15], v[204:207], v[128:131], v[12:15]\n\t" \
    "ds_read_b64_tr_b16 v[212:213], v227 offset:4096\n\t" \
    "s_waitcnt lgkmcnt(10)\n\t" \
    "v_mfma_f32_16x16x32_bf16 v[16:19], v[192:195], v[132:135], v[16:19]\n\t" \
    "ds_read_b64_tr_b16 v[214:215], v227 offset:4608\n\t" \
    "v_mfma_f32_16x16x32_bf16 v[20:23], v[196:199], v[132:135], v[20:23]\n\t" \
    "ds_read_b64_tr_b16 v[216:217], v228 offset:4096\n\t" \
    "v_mfma_f32_16x16x32_bf16 v[24:27], v[200:203], v[132:135], v[24:27]\n\t" \
    "ds_read_b64_tr_b16 v[218:219], v228 offset:4608\n\t" \
    "v_mfma_f32_16x16x32_bf16 v[28:31], v[204:207], v[132:135], v[28:31]\n\t" \
    "ds_read_b64_tr_b16 v[220:221], v229 offset:4096\n\t" \
    "s_waitcnt lgkmcnt(13)\n\t" \
    "v_mfma_f32_16x16x32_bf16 v[32:35], v[192:195], v[136:139], v[32:35]\n\t" \
    "ds_read_b64_tr_b16 v[222:223], v229 offset:4608\n\t" \
    "v_mfma_f32_16x16x32_bf16 v[36:39], v[196:199], v[136:139], v[36:39]\n\t" \
    "ds_read_b128 v[164:167], v225 offset:2048\n\t" \
    "v_mfma_f32_16x16x32_bf16 v[40:43], v[200:203], v[136:139], v[40:43]\n\t" \
    "ds_read_b128 v[168:171], v225 offset:4096\n\t" \
    "v_mfma_f32_16x16x32_bf16 v[44:47], v[204:207], v[136:139], v[44:47]\n\t" \
    "ds_read_b128 v[172:175], v225 offset:6144\n\t" \
    "s_waitcnt lgkmcnt(15)\n\t" \
    "v_mfma_f32_16x16x32_bf16 v[48:51], v[192:195], v[140:143], v[48:51]\n\t" \
    "ds_read_b128 v[176:179], v225 offset:8192\n\t" \
    "v_mfma_f32_16x16x32_bf16 v[52:55], v[196:199], v[140:143], v[52:55]\n\t" \
    "ds_read_b128 v[180:183], v225 offset:10240\n\t" \
    "v_mfma_f32_16x16x32_bf16 v[56:59], v[200:203], v[140:143], v[56:59]\n\t" \
    "ds_read_b128 v[184:187], v225 offset:12288\n\t" \
    "v_mfma_f32_16x16x32_bf16 v[60:63], v[204:207], v[140:143], v[60:63]\n\t" \
    "ds_read_b128 v[188:191], v225 offset:14336\n\t" \
    "v_mfma_f32_16x16x32_bf16 v[64:67], v[192:195], v[144:147], v[64:67]\n\t" \
    "s_add_u32 s51, s51, 49152\n\t" \
    "v_mfma_f32_16x16x32_bf16 v[68:71], v[196:199], v[144:147], v[68:71]\n\t" \
    "s_cmp_eq_u32 s51, 147456\n\t" \
    "v_mfma_f32_16x16x32_bf16 v[72:75], v[200:203], v[144:147], v[72:75]\n\t" \
    "s_cselect_b32 s50, s53, s54\n\t" \
    "v_mfma_f32_16x16x32_bf16 v[76:79], v[204:207], v[144:147], v[76:79]\n\t" \
    "s_cselect_b32 s51, 0, s51\n\t" \
    "s_waitcnt lgkmcnt(15)\n\t" \
    "v_mfma_f32_16x16x32_bf16 v[80:83], v[192:195], v[148:151], v[80:83]\n\t" \
    "v_add_u32 v224, s50, v224\n\t" \
    "v_mfma_f32_16x16x32_bf16 v[84:87], v[196:199], v[148:151], v[84:87]\n\t" \
    "v_add_u32 v225, s50, v225\n\t" \
    "v_mfma_f32_16x16x32_bf16 v[88:91], v[200:203], v[148:151], v[88:91]\n\t" \
    "v_add_u32 v226, s50, v226\n\t" \
    "v_mfma_f32_16x16x32_bf16 v[92:95], v[204:207], v[148:151], v[92:95]\n\t" \
    "v_add_u32 v227, s50, v227\n\t" \
    "v_mfma_f32_16x16x32_bf16 v[96:99], v[192:195], v[152:155], v[96:99]\n\t" \
    "v_add_u32 v228, s50, v228\n\t" \
    "v_mfma_f32_16x16x32_bf16 v[100:103], v[196:199], v[152:155], v[100:103]\n\t" \
    "v_add_u32 v229, s50, v229\n\t" \
    "v_mfma_f32_16x16x32_bf16 v[104:107], v[200:203], v[152:155], v[104:107]\n\t" \
    "v_mfma_f32_16x16x32_bf16 v[108:111], v[204:207], v[152:155], v[108:111]\n\t" \
    "v_mfma_f32_16x16x32_bf16 v[112:115], v[192:195], v[156:159], v[112:115]\n\t" \
    "v_mfma_f32_16x16x32_bf16 v[116:119], v[196:199], v[156:159], v[116:119]\n\t" \
    "v_mfma_f32_16x16x32_bf16 v[120:123], v[200:203], v[156:159], v[120:123]\n\t" \
    "v_mfma_f32_16x16x32_bf16 v[124:127], v[204:207], v[156:159], v[124:127]\n\t" \
    "s_waitcnt lgkmcnt(0)\n\t" \
    "s_barrier\n\t" \
    "v_mfma_f32_16x16x32_bf16 v[0:3], v[208:211], v[160:163], v[0:3]\n\t" \
    "ds_read_b64_tr_b16 v[192:193], v226 offset:0\n\t" \
    "v_mfma_f32_16x16x32_bf16 v[4:7], v[212:215], v[160:163], v[4:7]\n\t" \
    "ds_read_b64_tr_b16 v[194:195], v226 offset:512\n\t" \
    "v_mfma_f32_16x16x32_bf16 v[8:11], v[216:219], v[160:163], v[8:11]\n\t" \
    "ds_read_b128 v[128:131], v224 offset:0\n\t" \
    "v_mfma_f32_16x16x32_bf16 v[12:15], v[220:223], v[160:163], v[12:15]\n\t" \
    "ds_read_b64_tr_b16 v[196:197], v227 offset:0\n\t" \
    "v_mfma_f32_16x16x32_bf16 v[16:19], v[208:211], v[164:167], v[16:19]\n\t" \
    "ds_read_b64_tr_b16 v[198:199], v227 offset:512\n\t" \
    "v_mfma_f32_16x16x32_bf16 v[20:23], v[212:215], v[164:167], v[20:23]\n\t" \
    "ds_read_b64_tr_b16 v[200:201], v228 offset:0\n\t" \
    "v_mfma_f32_16x16x32_bf16 v[24:27], v[216:219], v[164:167], v[24:27]\n\t" \
    "ds_read_b64_tr_b16 v[202:203], v228 offset:512\n\t" \
    "v_mfma_f32_16x16x32_bf16 v[28:31], v[220:223], v[164:167], v[28:31]\n\t" \
    "ds_read_b64_tr_b16 v[204:205], v229 offset:0\n\t" \
    "v_mfma_f32_16x16x32_bf16 v[32:35], v[208:211], v[168:171], v[32:35]\n\t" \
    "ds_read_b64_tr_b16 v[206:207], v229 offset:512\n\t" \
    "v_mfma_f32_16x16x32_bf16 v[36:39], v[212:215], v[168:171], v[36:39]\n\t" \
    "ds_read_b128 v[132:135], v224 offset:2048\n\t" \
    "v_mfma_f32_16x16x32_bf16 v[40:43], v[216:219], v[168:171], v[40:43]\n\t" \
    "ds_read_b128 v[136:139], v224 offset:4096\n\t" \
    "v_mfma_f32_16x16x32_bf16 v[44:47], v[220:223], v[168:171], v[44:47]\n\t" \
    "ds_read_b128 v[140:143], v224 offset:6144\n\t" \
    "v_mfma_f32_16x16x32_bf16 v[48:51], v[208:211], v[172:175], v[48:51]\n\t" \
    "ds_read_b128 v[144:147], v224 offset:8192\n\t" \
    "v_mfma_f32_16x16x32_bf16 v[52:55], v[212:215], v[172:175], v[52:55]\n\t" \
    "ds_read_b128 v[148:151], v224 offset:10240\n\t" \
    "v_mfma_f32_16x16x32_bf16 v[56:59], v[216:219], v[172:175], v[56:59]\n\t" \
    "ds_read_b128 v[152:155], v224 offset:12288\n\t" \
    "v_mfma_f32_16x16x32_bf16 v[60:63], v[220:223], v[172:175], v[60:63]\n\t" \
    "ds_read_b128 v[156:159], v224 offset:14336\n\t" \
    "v_mfma_f32_16x16x32_bf16 v[64:67], v[208:211], v[176:179], v[64:67]\n\t" \
    "s_add_u32 s70, s72, 4195328\n\t" \
    "s_addc_u32 s71, s73, 0\n\t" \
    "global_load_dwordx4 v[252:255], %[vl16], s[70:71]\n\t" \
    "v_mfma_f32_16x16x32_bf16 v[68:71], v[212:215], v[176:179], v[68:71]\n\t" \
    "v_mfma_f32_16x16x32_bf16 v[72:75], v[216:219], v[176:179], v[72:75]\n\t" \
    "v_mfma_f32_16x16x32_bf16 v[76:79], v[220:223], v[176:179], v[76:79]\n\t" \
    "v_mfma_f32_16x16x32_bf16 v[80:83], v[208:211], v[180:183], v[80:83]\n\t" \
    "v_mfma_f32_16x16x32_bf16 v[84:87], v[212:215], v[180:183], v[84:87]\n\t" \
    "v_mfma_f32_16x16x32_bf16 v[88:91], v[216:219], v[180:183], v[88:91]\n\t" \
    "v_mfma_f32_16x16x32_bf16 v[92:95], v[220:223], v[180:183], v[92:95]\n\t" \
    "v_mfma_f32_16x16x32_bf16 v[96:99], v[208:211], v[184:187], v[96:99]\n\t" \
    "v_mfma_f32_16x16x32_bf16 v[100:103], v[212:215], v[184:187], v[100:103]\n\t" \
    "v_mfma_f32_16x16x32_bf16 v[104:107], v[216:219], v[184:187], v[104:107]\n\t" \
    "v_mfma_f32_16x16x32_bf16 v[108:111], v[220:223], v[184:187], v[108:111]\n\t" \
    "v_mfma_f32_16x16x32_bf16 v[112:115], v[208:211], v[188:191], v[112:115]\n\t" \
    "v_mfma_f32_16x16x32_bf16 v[116:119], v[212:215], v[188:191], v[116:119]\n\t" \
    "v_mfma_f32_16x16x32_bf16 v[120:123], v[216:219], v[188:191], v[120:123]\n\t" \
    "v_mfma_f32_16x16x32_bf16 v[124:127], v[220:223], v[188:191], v[124:127]\n\t" \
    "s_waitcnt lgkmcnt(13)\n\t" \
    "v_mfma_f32_16x16x32_bf16 v[0:3], v[192:195], v[128:131], v[0:3]\n\t" \
    "ds_read_b64_tr_b16 v[208:209], v226 offset:4096\n\t" \
    "s_waitcnt lgkmcnt(12)\n\t" \
    "v_mfma_f32_16x16x32_bf16 v[4:7], v[196:199], v[128:131], v[4:7]\n\t" \
    "ds_read_b64_tr_b16 v[210:211], v226 offset:4608\n\t" \
    "s_waitcnt lgkmcnt(11)\n\t" \
    "v_mfma_f32_16x16x32_bf16 v[8:11], v[200:203], v[128:131], v[8:11]\n\t" \
    "ds_read_b128 v[160:163], v225 offset:0\n\t" \
    "s_waitcnt lgkmcnt(10)\n\t" \
    "v_mfma_f32_16x16x32_bf16 v[12:15], v[204:207], v[128:131], v[12:15]\n\t" \
    "ds_read_b64_tr_b16 v[212:213], v227 offset:4096\n\t" \
    "s_waitcnt lgkmcnt(10)\n\t" \
    "v_mfma_f32_16x16x32_bf16 v[16:19], v[192:195], v[132:135], v[16:19]\n\t" \
    "ds_read_b64_tr_b16 v[214:215], v227 offset:4608\n\t" \
    "v_mfma_f32_16x16x32_bf16 v[20:23], v[196:199], v[132:135], v[20:23]\n\t" \
    "ds_read_b64_tr_b16 v[216:217], v228 offset:4096\n\t" \
    "v_mfma_f32_16x16x32_bf16 v[24:27], v[200:203], v[132:135], v[24:27]\n\t" \
    "ds_read_b64_tr_b16 v[218:219], v228 offset:4608\n\t" \
    "v_mfma_f32_16x16x32_bf16 v[28:31], v[204:207], v[132:135], v[28:31]\n\t" \
    "ds_read_b64_tr_b16 v[220:221], v229 offset:4096\n\t" \
    "s_waitcnt lgkmcnt(13)\n\t" \
    "v_mfma_f32_16x16x32_bf16 v[32:35], v[192:195], v[136:139], v[32:35]\n\t" \
    "ds_read_b64_tr_b16 v[222:223], v229 offset:4608\n\t" \
    "v_mfma_f32_16x16x32_bf16 v[36:39], v[196:199], v[136:139], v[36:39]\n\t" \
    "ds_read_b128 v[164:167], v225 offset:2048\n\t" \
    "v_mfma_f32_16x16x32_bf16 v[40:43], v[200:203], v[136:139], v[40:43]\n\t" \
    "ds_read_b128 v[168:171], v225 offset:4096\n\t" \
    "v_mfma_f32_16x16x32_bf16 v[44:47], v[204:207], v[136:139], v[44:47]\n\t" \
    "ds_read_b128 v[172:175], v225 offset:6144\n\t" \
    "s_waitcnt lgkmcnt(15)\n\t" \
    "v_mfma_f32_16x16x32_bf16 v[48:51], v[192:195], v[140:143], v[48:51]\n\t" \
    "ds_read_b128 v[176:179], v225 offset:8192\n\t" \
    "v_mfma_f32_16x16x32_bf16 v[52:55], v[196:199], v[140:143], v[52:55]\n\t" \
    "ds_read_b128 v[180:183], v225 offset:10240\n\t" \
    "v_mfma_f32_16x16x32_bf16 v[56:59], v[200:203], v[140:143], v[56:59]\n\t" \
    "ds_read_b128 v[184:187], v225 offset:12288\n\t" \
    "v_mfma_f32_16x16x32_bf16 v[60:63], v[204:207], v[140:143], v[60:63]\n\t" \
    "ds_read_b128 v[188:191], v225 offset:14336\n\t" \
    "v_mfma_f32_16x16x32_bf16 v[64:67], v[192:195], v[144:147], v[64:67]\n\t" \
    "s_add_u32 s51, s51, 49152\n\t" \
    "v_mfma_f32_16x16x32_bf16 v[68:71], v[196:199], v[144:147], v[68:71]\n\t" \
    "s_cmp_eq_u32 s51, 147456\n\t" \
    "v_mfma_f32_16x16x32_bf16 v[72:75], v[200:203], v[144:147], v[72:75]\n\t" \
    "s_cselect_b32 s50, s53, s54\n\t" \
    "v_mfma_f32_16x16x32_bf16 v[76:79], v[204:207], v[144:147], v[76:79]\n\t" \
    "s_cselect_b32 s51, 0, s51\n\t" \
    "s_waitcnt lgkmcnt(15)\n\t" \
    "v_mfma_f32_16x16x32_bf16 v[80:83], v[192:195], v[148:151], v[80:83]\n\t" \
    "v_add_u32 v224, s50, v224\n\t" \
    "v_mfma_f32_16x16x32_bf16 v[84:87], v[196:199], v[148:151], v[84:87]\n\t" \
    "v_add_u32 v225, s50, v225\n\t" \
    "v_mfma_f32_16x16x32_bf16 v[88:91], v[200:203], v[148:151], v[88:91]\n\t" \
    "v_add_u32 v226, s50, v226\n\t" \
    "v_mfma_f32_16x16x32_bf16 v[92:95], v[204:207], v[148:151], v[92:95]\n\t" \
    "v_add_u32 v227, s50, v227\n\t" \
    "v_mfma_f32_16x16x32_bf16 v[96:99], v[192:195], v[152:155], v[96:99]\n\t" \
    "v_add_u32 v228, s50, v228\n\t" \
    "v_mfma_f32_16x16x32_bf16 v[100:103], v[196:199], v[152:155], v[100:103]\n\t" \
    "v_add_u32 v229, s50, v229\n\t" \
    "v_mfma_f32_16x16x32_bf16 v[104:107], v[200:203], v[152:155], v[104:107]\n\t" \
    "v_mfma_f32_16x16x32_bf16 v[108:111], v[204:207], v[152:155], v[108:111]\n\t" \
    "v_mfma_f32_16x16x32_bf16 v[112:115], v[192:195], v[156:159], v[112:115]\n\t" \
    "v_mfma_f32_16x16x32_bf16 v[116:119], v[196:199], v[156:159], v[116:119]\n\t" \
    "v_mfma_f32_16x16x32_bf16 v[120:123], v[200:203], v[156:159], v[120:123]\n\t" \
    "v_mfma_f32_16x16x32_bf16 v[124:127], v[204:207], v[156:159], v[124:127]\n\t" \
    "s_waitcnt lgkmcnt(0)\n\t" \
    "s_barrier\n\t" \
    "v_mfma_f32_16x16x32_bf16 v[0:3], v[208:211], v[160:163], v[0:3]\n\t" \
    "ds_read_b64_tr_b16 v[192:193], v226 offset:0\n\t" \
    "v_mfma_f32_16x16x32_bf16 v[4:7], v[212:215], v[160:163], v[4:7]\n\t" \
    "ds_read_b64_tr_b16 v[194:195], v226 offset:512\n\t" \
    "v_mfma_f32_16x16x32_bf16 v[8:11], v[216:219], v[160:163], v[8:11]\n\t" \
    "ds_read_b128 v[128:131], v224 offset:0\n\t" \
    "v_mfma_f32_16x16x32_bf16 v[12:15], v[220:223], v[160:163], v[12:15]\n\t" \
    "ds_read_b64_tr_b16 v[196:197], v227 offset:0\n\t" \
    "v_mfma_f32_16x16x32_bf16 v[16:19], v[208:211], v[164:167], v[16:19]\n\t" \
    "ds_read_b64_tr_b16 v[198:199], v227 offset:512\n\t" \
    "v_mfma_f32_16x16x32_bf16 v[20:23], v[212:215], v[164:167], v[20:23]\n\t" \
    "ds_read_b64_tr_b16 v[200:201], v228 offset:0\n\t" \
    "v_mfma_f32_16x16x32_bf16 v[24:27], v[216:219], v[164:167], v[24:27]\n\t" \
    "ds_read_b64_tr_b16 v[202:203], v228 offset:512\n\t" \
    "v_mfma_f32_16x16x32_bf16 v[28:31], v[220:223], v[164:167], v[28:31]\n\t" \
    "ds_read_b64_tr_b16 v[204:205], v229 offset:0\n\t" \
    "v_mfma_f32_16x16x32_bf16 v[32:35], v[208:211], v[168:171], v[32:35]\n\t" \
    "ds_read_b64_tr_b16 v[206:207], v229 offset:512\n\t" \
    "v_mfma_f32_16x16x32_bf16 v[36:39], v[212:215], v[168:171], v[36:39]\n\t" \
    "ds_read_b128 v[132:135], v224 offset:2048\n\t" \
    "v_mfma_f32_16x16x32_bf16 v[40:43], v[216:219], v[168:171], v[40:43]\n\t" \
    "ds_read_b128 v[136:139], v224 offset:4096\n\t" \
    "v_mfma_f32_16x16x32_bf16 v[44:47], v[220:223], v[168:171], v[44:47]\n\t" \
    "ds_read_b128 v[140:143], v224 offset:6144\n\t" \
    "v_mfma_f32_16x16x32_bf16 v[48:51], v[208:211], v[172:175], v[48:51]\n\t" \
    "ds_read_b128 v[144:147], v224 offset:8192\n\t" \
    "v_mfma_f32_16x16x32_bf16 v[52:55], v[212:215], v[172:175], v[52:55]\n\t" \
    "ds_read_b128 v[148:151], v224 offset:10240\n\t" \
    "v_mfma_f32_16x16x32_bf16 v[56:59], v[216:219], v[172:175], v[56:59]\n\t" \
    "ds_read_b128 v[152:155], v224 offset:12288\n\t" \
    "v_mfma_f32_16x16x32_bf16 v[60:63], v[220:223], v[172:175], v[60:63]\n\t" \
    "ds_read_b128 v[156:159], v224 offset:14336\n\t" \
    "v_mfma_f32_16x16x32_bf16 v[64:67], v[208:211], v[176:179], v[64:67]\n\t" \
    "s_add_u32 s70, s72, 6292480\n\t" \
    "s_addc_u32 s71, s73, 0\n\t" \
    "global_load_dwordx4 v[252:255], %[vl16], s[70:71]\n\t" \
    "v_mfma_f32_16x16x32_bf16 v[68:71], v[212:215], v[176:179], v[68:71]\n\t" \
    "v_mfma_f32_16x16x32_bf16 v[72:75], v[216:219], v[176:179], v[72:75]\n\t" \
    "v_mfma_f32_16x16x32_bf16 v[76:79], v[220:223], v[176:179], v[76:79]\n\t" \
    "v_mfma_f32_16x16x32_bf16 v[80:83], v[208:211], v[180:183], v[80:83]\n\t" \
    "v_mfma_f32_16x16x32_bf16 v[84:87], v[212:215], v[180:183], v[84:87]\n\t" \
    "v_mfma_f32_16x16x32_bf16 v[88:91], v[216:219], v[180:183], v[88:91]\n\t" \
    "v_mfma_f32_16x16x32_bf16 v[92:95], v[220:223], v[180:183], v[92:95]\n\t" \
    "v_mfma_f32_16x16x32_bf16 v[96:99], v[208:211], v[184:187], v[96:99]\n\t" \
    "v_mfma_f32_16x16x32_bf16 v[100:103], v[212:215], v[184:187], v[100:103]\n\t" \
    "v_mfma_f32_16x16x32_bf16 v[104:107], v[216:219], v[184:187], v[104:107]\n\t" \
    "v_mfma_f32_16x16x32_bf16 v[108:111], v[220:223], v[184:187], v[108:111]\n\t" \
    "v_mfma_f32_16x16x32_bf16 v[112:115], v[208:211], v[188:191], v[112:115]\n\t" \
    "v_mfma_f32_16x16x32_bf16 v[116:119], v[212:215], v[188:191], v[116:119]\n\t" \
    "v_mfma_f32_16x16x32_bf16 v[120:123], v[216:219], v[188:191], v[120:123]\n\t" \
    "v_mfma_f32_16x16x32_bf16 v[124:127], v[220:223], v[188:191], v[124:127]\n\t" \
    "s_waitcnt lgkmcnt(13)\n\t" \
    "v_mfma_f32_16x16x32_bf16 v[0:3], v[192:195], v[128:131], v[0:3]\n\t" \
    "ds_read_b64_tr_b16 v[208:209], v226 offset:4096\n\t" \
    "s_waitcnt lgkmcnt(12)\n\t" \
    "v_mfma_f32_16x16x32_bf16 v[4:7], v[196:199], v[128:131], v[4:7]\n\t" \
    "ds_read_b64_tr_b16 v[210:211], v226 offset:4608\n\t" \
    "s_waitcnt lgkmcnt(11)\n\t" \
    "v_mfma_f32_16x16x32_bf16 v[8:11], v[200:203], v[128:131], v[8:11]\n\t" \
    "ds_read_b128 v[160:163], v225 offset:0\n\t" \
    "s_waitcnt lgkmcnt(10)\n\t" \
    "v_mfma_f32_16x16x32_bf16 v[12:15], v[204:207], v[128:131], v[12:15]\n\t" \
    "ds_read_b64_tr_b16 v[212:213], v227 offset:4096\n\t" \
    "s_waitcnt lgkmcnt(10)\n\t" \
    "v_mfma_f32_16x16x32_bf16 v[16:19], v[192:195], v[132:135], v[16:19]\n\t" \
    "ds_read_b64_tr_b16 v[214:215], v227 offset:4608\n\t" \
    "v_mfma_f32_16x16x32_bf16 v[20:23], v[196:199], v[132:135], v[20:23]\n\t" \
    "ds_read_b64_tr_b16 v[216:217], v228 offset:4096\n\t" \
    "v_mfma_f32_16x16x32_bf16 v[24:27], v[200:203], v[132:135], v[24:27]\n\t" \
    "ds_read_b64_tr_b16 v[218:219], v228 offset:4608\n\t" \
    "v_mfma_f32_16x16x32_bf16 v[28:31], v[204:207], v[132:135], v[28:31]\n\t" \
    "ds_read_b64_tr_b16 v[220:221], v229 offset:4096\n\t" \
    "s_waitcnt lgkmcnt(13)\n\t" \
    "v_mfma_f32_16x16x32_bf16 v[32:35], v[192:195], v[136:139], v[32:35]\n\t" \
    "ds_read_b64_tr_b16 v[222:223], v229 offset:4608\n\t" \
    "v_mfma_f32_16x16x32_bf16 v[36:39], v[196:199], v[136:139], v[36:39]\n\t" \
    "ds_read_b128 v[164:167], v225 offset:2048\n\t" \
    "v_mfma_f32_16x16x32_bf16 v[40:43], v[200:203], v[136:139], v[40:43]\n\t" \
    "ds_read_b128 v[168:171], v225 offset:4096\n\t" \
    "v_mfma_f32_16x16x32_bf16 v[44:47], v[204:207], v[136:139], v[44:47]\n\t" \
    "ds_read_b128 v[172:175], v225 offset:6144\n\t" \
    "s_waitcnt lgkmcnt(15)\n\t" \
    "v_mfma_f32_16x16x32_bf16 v[48:51], v[192:195], v[140:143], v[48:51]\n\t" \
    "ds_read_b128 v[176:179], v225 offset:8192\n\t" \
    "v_mfma_f32_16x16x32_bf16 v[52:55], v[196:199], v[140:143], v[52:55]\n\t" \
    "ds_read_b128 v[180:183], v225 offset:10240\n\t" \
    "v_mfma_f32_16x16x32_bf16 v[56:59], v[200:203], v[140:143], v[56:59]\n\t" \
    "ds_read_b128 v[184:187], v225 offset:12288\n\t" \
    "v_mfma_f32_16x16x32_bf16 v[60:63], v[204:207], v[140:143], v[60:63]\n\t" \
    "ds_read_b128 v[188:191], v225 offset:14336\n\t" \
    "v_mfma_f32_16x16x32_bf16 v[64:67], v[192:195], v[144:147], v[64:67]\n\t" \
    "s_add_u32 s51, s51, 49152\n\t" \
    "v_mfma_f32_16x16x32_bf16 v[68:71], v[196:199], v[144:147], v[68:71]\n\t" \
    "s_cmp_eq_u32 s51, 147456\n\t" \
    "v_mfma_f32_16x16x32_bf16 v[72:75], v[200:203], v[144:147], v[72:75]\n\t" \
    "s_cselect_b32 s50, s53, s54\n\t" \
    "v_mfma_f32_16x16x32_bf16 v[76:79], v[204:207], v[144:147], v[76:79]\n\t" \
    "s_cselect_b32 s51, 0, s51\n\t" \
    "s_waitcnt lgkmcnt(15)\n\t" \
    "v_mfma_f32_16x16x32_bf16 v[80:83], v[192:195], v[148:151], v[80:83]\n\t" \
    "v_add_u32 v224, s50, v224\n\t" \
    "v_mfma_f32_16x16x32_bf16 v[84:87], v[196:199], v[148:151], v[84:87]\n\t" \
    "v_add_u32 v225, s50, v225\n\t" \
    "v_mfma_f32_16x16x32_bf16 v[88:91], v[200:203], v[148:151], v[88:91]\n\t" \
    "v_add_u32 v226, s50, v226\n\t" \
    "v_mfma_f32_16x16x32_bf16 v[92:95], v[204:207], v[148:151], v[92:95]\n\t" \
    "v_add_u32 v227, s50, v227\n\t" \
    "v_mfma_f32_16x16x32_bf16 v[96:99], v[192:195], v[152:155], v[96:99]\n\t" \
    "v_add_u32 v228, s50, v228\n\t" \
    "v_mfma_f32_16x16x32_bf16 v[100:103], v[196:199], v[152:155], v[100:103]\n\t" \
    "v_add_u32 v229, s50, v229\n\t" \
    "v_mfma_f32_16x16x32_bf16 v[104:107], v[200:203], v[152:155], v[104:107]\n\t" \
    "v_mfma_f32_16x16x32_bf16 v[108:111], v[204:207], v[152:155], v[108:111]\n\t" \
    "v_mfma_f32_16x16x32_bf16 v[112:115], v[192:195], v[156:159], v[112:115]\n\t" \
    "v_mfma_f32_16x16x32_bf16 v[116:119], v[196:199], v[156:159], v[116:119]\n\t" \
    "v_mfma_f32_16x16x32_bf16 v[120:123], v[200:203], v[156:159], v[120:123]\n\t" \
    "v_mfma_f32_16x16x32_bf16 v[124:127], v[204:207], v[156:159], v[124:127]\n\t" \
    "s_waitcnt lgkmcnt(0)\n\t" \
    "s_barrier\n\t" \
    "v_mfma_f32_16x16x32_bf16 v[0:3], v[208:211], v[160:163], v[0:3]\n\t" \
    "ds_read_b64_tr_b16 v[192:193], v226 offset:0\n\t" \
    "v_mfma_f32_16x16x32_bf16 v[4:7], v[212:215], v[160:163], v[4:7]\n\t" \
    "ds_read_b64_tr_b16 v[194:195], v226 offset:512\n\t" \
    "v_mfma_f32_16x16x32_bf16 v[8:11], v[216:219], v[160:163], v[8:11]\n\t" \
    "ds_read_b128 v[128:131], v224 offset:0\n\t" \
    "v_mfma_f32_16x16x32_bf16 v[12:15], v[220:223], v[160:163], v[12:15]\n\t" \
    "ds_read_b64_tr_b16 v[196:197], v227 offset:0\n\t" \
    "v_mfma_f32_16x16x32_bf16 v[16:19], v[208:211], v[164:167], v[16:19]\n\t" \
    "ds_read_b64_tr_b16 v[198:199], v227 offset:512\n\t" \
    "v_mfma_f32_16x16x32_bf16 v[20:23], v[212:215], v[164:167], v[20:23]\n\t" \
    "ds_read_b64_tr_b16 v[200:201], v228 offset:0\n\t" \
    "v_mfma_f32_16x16x32_bf16 v[24:27], v[216:219], v[164:167], v[24:27]\n\t" \
    "ds_read_b64_tr_b16 v[202:203], v228 offset:512\n\t" \
    "v_mfma_f32_16x16x32_bf16 v[28:31], v[220:223], v[164:167], v[28:31]\n\t" \
    "ds_read_b64_tr_b16 v[204:205], v229 offset:0\n\t" \
    "v_mfma_f32_16x16x32_bf16 v[32:35], v[208:211], v[168:171], v[32:35]\n\t" \
    "ds_read_b64_tr_b16 v[206:207], v229 offset:512\n\t" \
    "v_mfma_f32_16x16x32_bf16 v[36:39], v[212:215], v[168:171], v[36:39]\n\t" \
    "ds_read_b128 v[132:135], v224 offset:2048\n\t" \
    "v_mfma_f32_16x16x32_bf16 v[40:43], v[216:219], v[168:171], v[40:43]\n\t" \
    "ds_read_b128 v[136:139], v224 offset:4096\n\t" \
    "v_mfma_f32_16x16x32_bf16 v[44:47], v[220:223], v[168:171], v[44:47]\n\t" \
    "ds_read_b128 v[140:143], v224 offset:6144\n\t" \
    "v_mfma_f32_16x16x32_bf16 v[48:51], v[208:211], v[172:175], v[48:51]\n\t" \
    "ds_read_b128 v[144:147], v224 offset:8192\n\t" \
    "v_mfma_f32_16x16x32_bf16 v[52:55], v[212:215], v[172:175], v[52:55]\n\t" \
    "ds_read_b128 v[148:151], v224 offset:10240\n\t" \
    "v_mfma_f32_16x16x32_bf16 v[56:59], v[216:219], v[172:175], v[56:59]\n\t" \
    "ds_read_b128 v[152:155], v224 offset:12288\n\t" \
    "v_mfma_f32_16x16x32_bf16 v[60:63], v[220:223], v[172:175], v[60:63]\n\t" \
    "ds_read_b128 v[156:159], v224 offset:14336\n\t" \
    "v_mfma_f32_16x16x32_bf16 v[64:67], v[208:211], v[176:179], v[64:67]\n\t" \
    "s_add_u32 s70, s74, 1024\n\t" \
    "s_addc_u32 s71, s75, 0\n\t" \
    "global_load_dwordx4 v[252:255], %[vl16], s[70:71]\n\t" \
    "v_mfma_f32_16x16x32_bf16 v[68:71], v[212:215], v[176:179], v[68:71]\n\t" \
    "s_add_u32 s70, s72, 2048\n\t" \
    "s_addc_u32 s71, s73, 0\n\t" \
    "global_load_dwordx4 v[252:255], %[vl16], s[70:71]\n\t" \
    "v_mfma_f32_16x16x32_bf16 v[72:75], v[216:219], v[176:179], v[72:75]\n\t" \
    "v_mfma_f32_16x16x32_bf16 v[76:79], v[220:223], v[176:179], v[76:79]\n\t" \
    "v_mfma_f32_16x16x32_bf16 v[80:83], v[208:211], v[180:183], v[80:83]\n\t" \
    "v_mfma_f32_16x16x32_bf16 v[84:87], v[212:215], v[180:183], v[84:87]\n\t" \
    "v_mfma_f32_16x16x32_bf16 v[88:91], v[216:219], v[180:183], v[88:91]\n\t" \
    "v_mfma_f32_16x16x32_bf16 v[92:95], v[220:223], v[180:183], v[92:95]\n\t" \
    "v_mfma_f32_16x16x32_bf16 v[96:99], v[208:211], v[184:187], v[96:99]\n\t" \
    "v_mfma_f32_16x16x32_bf16 v[100:103], v[212:215], v[184:187], v[100:103]\n\t" \
    "v_mfma_f32_16x16x32_bf16 v[104:107], v[216:219], v[184:187], v[104:107]\n\t" \
    "v_mfma_f32_16x16x32_bf16 v[108:111], v[220:223], v[184:187], v[108:111]\n\t" \
    "v_mfma_f32_16x16x32_bf16 v[112:115], v[208:211], v[188:191], v[112:115]\n\t" \
    "v_mfma_f32_16x16x32_bf16 v[116:119], v[212:215], v[188:191], v[116:119]\n\t" \
    "v_mfma_f32_16x16x32_bf16 v[120:123], v[216:219], v[188:191], v[120:123]\n\t" \
    "v_mfma_f32_16x16x32_bf16 v[124:127], v[220:223], v[188:191], v[124:127]\n\t" \
    "s_waitcnt lgkmcnt(13)\n\t" \
    "v_mfma_f32_16x16x32_bf16 v[0:3], v[192:195], v[128:131], v[0:3]\n\t" \
    "ds_read_b64_tr_b16 v[208:209], v226 offset:4096\n\t" \
    "s_waitcnt lgkmcnt(12)\n\t" \
    "v_mfma_f32_16x16x32_bf16 v[4:7], v[196:199], v[128:131], v[4:7]\n\t" \
    "ds_read_b64_tr_b16 v[210:211], v226 offset:4608\n\t" \
    "s_waitcnt lgkmcnt(11)\n\t" \
    "v_mfma_f32_16x16x32_bf16 v[8:11], v[200:203], v[128:131], v[8:11]\n\t" \
    "ds_read_b128 v[160:163], v225 offset:0\n\t" \
    "s_waitcnt lgkmcnt(10)\n\t" \
    "v_mfma_f32_16x16x32_bf16 v[12:15], v[204:207], v[128:131], v[12:15]\n\t" \
    "ds_read_b64_tr_b16 v[212:213], v227 offset:4096\n\t" \
    "s_waitcnt lgkmcnt(10)\n\t" \
    "v_mfma_f32_16x16x32_bf16 v[16:19], v[192:195], v[132:135], v[16:19]\n\t" \
    "ds_read_b64_tr_b16 v[214:215], v227 offset:4608\n\t" \
    "v_mfma_f32_16x16x32_bf16 v[20:23], v[196:199], v[132:135], v[20:23]\n\t" \
    "ds_read_b64_tr_b16 v[216:217], v228 offset:4096\n\t" \
    "v_mfma_f32_16x16x32_bf16 v[24:27], v[200:203], v[132:135], v[24:27]\n\t" \
    "ds_read_b64_tr_b16 v[218:219], v228 offset:4608\n\t" \
    "v_mfma_f32_16x16x32_bf16 v[28:31], v[204:207], v[132:135], v[28:31]\n\t" \
    "ds_read_b64_tr_b16 v[220:221], v229 offset:4096\n\t" \
    "s_waitcnt lgkmcnt(13)\n\t" \
    "v_mfma_f32_16x16x32_bf16 v[32:35], v[192:195], v[136:139], v[32:35]\n\t" \
    "ds_read_b64_tr_b16 v[222:223], v229 offset:4608\n\t" \
    "v_mfma_f32_16x16x32_bf16 v[36:39], v[196:199], v[136:139], v[36:39]\n\t" \
    "ds_read_b128 v[164:167], v225 offset:2048\n\t" \
    "v_mfma_f32_16x16x32_bf16 v[40:43], v[200:203], v[136:139], v[40:43]\n\t" \
    "ds_read_b128 v[168:171], v225 offset:4096\n\t" \
    "v_mfma_f32_16x16x32_bf16 v[44:47], v[204:207], v[136:139], v[44:47]\n\t" \
    "ds_read_b128 v[172:175], v225 offset:6144\n\t" \
    "s_waitcnt lgkmcnt(15)\n\t" \
    "v_mfma_f32_16x16x32_bf16 v[48:51], v[192:195], v[140:143], v[48:51]\n\t" \
    "ds_read_b128 v[176:179], v225 offset:8192\n\t" \
    "v_mfma_f32_16x16x32_bf16 v[52:55], v[196:199], v[140:143], v[52:55]\n\t" \
    "ds_read_b128 v[180:183], v225 offset:10240\n\t" \
    "v_mfma_f32_16x16x32_bf16 v[56:59], v[200:203], v[140:143], v[56:59]\n\t" \
    "ds_read_b128 v[184:187], v225 offset:12288\n\t" \
    "v_mfma_f32_16x16x32_bf16 v[60:63], v[204:207], v[140:143], v[60:63]\n\t" \
    "ds_read_b128 v[188:191], v225 offset:14336\n\t" \
    "v_mfma_f32_16x16x32_bf16 v[64:67], v[192:195], v[144:147], v[64:67]\n\t" \
    "s_add_u32 s51, s51, 49152\n\t" \
    "v_mfma_f32_16x16x32_bf16 v[68:71], v[196:199], v[144:147], v[68:71]\n\t" \
    "s_cmp_eq_u32 s51, 147456\n\t" \
    "v_mfma_f32_16x16x32_bf16 v[72:75], v[200:203], v[144:147], v[72:75]\n\t" \
    "s_cselect_b32 s50, s53, s54\n\t" \
    "v_mfma_f32_16x16x32_bf16 v[76:79], v[204:207], v[144:147], v[76:79]\n\t" \
    "s_cselect_b32 s51, 0, s51\n\t" \
    "s_waitcnt lgkmcnt(15)\n\t" \
    "v_mfma_f32_16x16x32_bf16 v[80:83], v[192:195], v[148:151], v[80:83]\n\t" \
    "v_add_u32 v224, s50, v224\n\t" \
    "v_mfma_f32_16x16x32_bf16 v[84:87], v[196:199], v[148:151], v[84:87]\n\t" \
    "v_add_u32 v225, s50, v225\n\t" \
    "v_mfma_f32_16x16x32_bf16 v[88:91], v[200:203], v[148:151], v[88:91]\n\t" \
    "v_add_u32 v226, s50, v226\n\t" \
    "v_mfma_f32_16x16x32_bf16 v[92:95], v[204:207], v[148:151], v[92:95]\n\t" \
    "v_add_u32 v227, s50, v227\n\t" \
    "v_mfma_f32_16x16x32_bf16 v[96:99], v[192:195], v[152:155], v[96:99]\n\t" \
    "v_add_u32 v228, s50, v228\n\t" \
    "v_mfma_f32_16x16x32_bf16 v[100:103], v[196:199], v[152:155], v[100:103]\n\t" \
    "v_add_u32 v229, s50, v229\n\t" \
    "v_mfma_f32_16x16x32_bf16 v[104:107], v[200:203], v[152:155], v[104:107]\n\t" \
    "v_mfma_f32_16x16x32_bf16 v[108:111], v[204:207], v[152:155], v[108:111]\n\t" \
    "v_mfma_f32_16x16x32_bf16 v[112:115], v[192:195], v[156:159], v[112:115]\n\t" \
    "v_mfma_f32_16x16x32_bf16 v[116:119], v[196:199], v[156:159], v[116:119]\n\t" \
    "v_mfma_f32_16x16x32_bf16 v[120:123], v[200:203], v[156:159], v[120:123]\n\t" \
    "v_mfma_f32_16x16x32_bf16 v[124:127], v[204:207], v[156:159], v[124:127]\n\t" \
    "s_waitcnt lgkmcnt(0)\n\t" \
    "s_barrier\n\t" \
    "v_mfma_f32_16x16x32_bf16 v[0:3], v[208:211], v[160:163], v[0:3]\n\t" \
    "ds_read_b64_tr_b16 v[192:193], v226 offset:0\n\t" \
    "v_mfma_f32_16x16x32_bf16 v[4:7], v[212:215], v[160:163], v[4:7]\n\t" \
    "ds_read_b64_tr_b16 v[194:195], v226 offset:512\n\t" \
    "v_mfma_f32_16x16x32_bf16 v[8:11], v[216:219], v[160:163], v[8:11]\n\t" \
    "ds_read_b128 v[128:131], v224 offset:0\n\t" \
    "v_mfma_f32_16x16x32_bf16 v[12:15], v[220:223], v[160:163], v[12:15]\n\t" \
    "ds_read_b64_tr_b16 v[196:197], v227 offset:0\n\t" \
    "v_mfma_f32_16x16x32_bf16 v[16:19], v[208:211], v[164:167], v[16:19]\n\t" \
    "ds_read_b64_tr_b16 v[198:199], v227 offset:512\n\t" \
    "v_mfma_f32_16x16x32_bf16 v[20:23], v[212:215], v[164:167], v[20:23]\n\t" \
    "ds_read_b64_tr_b16 v[200:201], v228 offset:0\n\t" \
    "v_mfma_f32_16x16x32_bf16 v[24:27], v[216:219], v[164:167], v[24:27]\n\t" \
    "ds_read_b64_tr_b16 v[202:203], v228 offset:512\n\t" \
    "v_mfma_f32_16x16x32_bf16 v[28:31], v[220:223], v[164:167], v[28:31]\n\t" \
    "ds_read_b64_tr_b16 v[204:205], v229 offset:0\n\t" \
    "v_mfma_f32_16x16x32_bf16 v[32:35], v[208:211], v[168:171], v[32:35]\n\t" \
    "ds_read_b64_tr_b16 v[206:207], v229 offset:512\n\t" \
    "v_mfma_f32_16x16x32_bf16 v[36:39], v[212:215], v[168:171], v[36:39]\n\t" \
    "ds_read_b128 v[132:135], v224 offset:2048\n\t" \
    "v_mfma_f32_16x16x32_bf16 v[40:43], v[216:219], v[168:171], v[40:43]\n\t" \
    "ds_read_b128 v[136:139], v224 offset:4096\n\t" \
    "v_mfma_f32_16x16x32_bf16 v[44:47], v[220:223], v[168:171], v[44:47]\n\t" \
    "ds_read_b128 v[140:143], v224 offset:6144\n\t" \
    "v_mfma_f32_16x16x32_bf16 v[48:51], v[208:211], v[172:175], v[48:51]\n\t" \
    "ds_read_b128 v[144:147], v224 offset:8192\n\t" \
    "v_mfma_f32_16x16x32_bf16 v[52:55], v[212:215], v[172:175], v[52:55]\n\t" \
    "ds_read_b128 v[148:151], v224 offset:10240\n\t" \
    "v_mfma_f32_16x16x32_bf16 v[56:59], v[216:219], v[172:175], v[56:59]\n\t" \
    "ds_read_b128 v[152:155], v224 offset:12288\n\t" \
    "v_mfma_f32_16x16x32_bf16 v[60:63], v[220:223], v[172:175], v[60:63]\n\t" \
    "ds_read_b128 v[156:159], v224 offset:14336\n\t" \
    "v_mfma_f32_16x16x32_bf16 v[64:67], v[208:211], v[176:179], v[64:67]\n\t" \
    "s_add_u32 s70, s74, 2098176\n\t" \
    "s_addc_u32 s71, s75, 0\n\t" \
    "global_load_dwordx4 v[252:255], %[vl16], s[70:71]\n\t" \
    "v_mfma_f32_16x16x32_bf16 v[68:71], v[212:215], v[176:179], v[68:71]\n\t" \
    "s_add_u32 s70, s72, 2099200\n\t" \
    "s_addc_u32 s71, s73, 0\n\t" \
    "global_load_dwordx4 v[252:255], %[vl16], s[70:71]\n\t" \
    "v_mfma_f32_16x16x32_bf16 v[72:75], v[216:219], v[176:179], v[72:75]\n\t" \
    "v_mfma_f32_16x16x32_bf16 v[76:79], v[220:223], v[176:179], v[76:79]\n\t" \
    "v_mfma_f32_16x16x32_bf16 v[80:83], v[208:211], v[180:183], v[80:83]\n\t" \
    "v_mfma_f32_16x16x32_bf16 v[84:87], v[212:215], v[180:183], v[84:87]\n\t" \
    "v_mfma_f32_16x16x32_bf16 v[88:91], v[216:219], v[180:183], v[88:91]\n\t" \
    "v_mfma_f32_16x16x32_bf16 v[92:95], v[220:223], v[180:183], v[92:95]\n\t" \
    "v_mfma_f32_16x16x32_bf16 v[96:99], v[208:211], v[184:187], v[96:99]\n\t" \
    "v_mfma_f32_16x16x32_bf16 v[100:103], v[212:215], v[184:187], v[100:103]\n\t" \
    "v_mfma_f32_16x16x32_bf16 v[104:107], v[216:219], v[184:187], v[104:107]\n\t" \
    "v_mfma_f32_16x16x32_bf16 v[108:111], v[220:223], v[184:187], v[108:111]\n\t" \
    "v_mfma_f32_16x16x32_bf16 v[112:115], v[208:211], v[188:191], v[112:115]\n\t" \
    "v_mfma_f32_16x16x32_bf16 v[116:119], v[212:215], v[188:191], v[116:119]\n\t" \
    "v_mfma_f32_16x16x32_bf16 v[120:123], v[216:219], v[188:191], v[120:123]\n\t" \
    "v_mfma_f32_16x16x32_bf16 v[124:127], v[220:223], v[188:191], v[124:127]\n\t" \
    "s_waitcnt lgkmcnt(13)\n\t" \
    "v_mfma_f32_16x16x32_bf16 v[0:3], v[192:195], v[128:131], v[0:3]\n\t" \
    "ds_read_b64_tr_b16 v[208:209], v226 offset:4096\n\t" \
    "s_waitcnt lgkmcnt(12)\n\t" \
    "v_mfma_f32_16x16x32_bf16 v[4:7], v[196:199], v[128:131], v[4:7]\n\t" \
    "ds_read_b64_tr_b16 v[210:211], v226 offset:4608\n\t" \
    "s_waitcnt lgkmcnt(11)\n\t" \
    "v_mfma_f32_16x16x32_bf16 v[8:11], v[200:203], v[128:131], v[8:11]\n\t" \
    "ds_read_b128 v[160:163], v225 offset:0\n\t" \
    "s_waitcnt lgkmcnt(10)\n\t" \
    "v_mfma_f32_16x16x32_bf16 v[12:15], v[204:207], v[128:131], v[12:15]\n\t" \
    "ds_read_b64_tr_b16 v[212:213], v227 offset:4096\n\t" \
    "s_waitcnt lgkmcnt(10)\n\t" \
    "v_mfma_f32_16x16x32_bf16 v[16:19], v[192:195], v[132:135], v[16:19]\n\t" \
    "ds_read_b64_tr_b16 v[214:215], v227 offset:4608\n\t" \
    "v_mfma_f32_16x16x32_bf16 v[20:23], v[196:199], v[132:135], v[20:23]\n\t" \
    "ds_read_b64_tr_b16 v[216:217], v228 offset:4096\n\t" \
    "v_mfma_f32_16x16x32_bf16 v[24:27], v[200:203], v[132:135], v[24:27]\n\t" \
    "ds_read_b64_tr_b16 v[218:219], v228 offset:4608\n\t" \
    "v_mfma_f32_16x16x32_bf16 v[28:31], v[204:207], v[132:135], v[28:31]\n\t" \
    "ds_read_b64_tr_b16 v[220:221], v229 offset:4096\n\t" \
    "s_waitcnt lgkmcnt(13)\n\t" \
    "v_mfma_f32_16x16x32_bf16 v[32:35], v[192:195], v[136:139], v[32:35]\n\t" \
    "ds_read_b64_tr_b16 v[222:223], v229 offset:4608\n\t" \
    "v_mfma_f32_16x16x32_bf16 v[36:39], v[196:199], v[136:139], v[36:39]\n\t" \
    "ds_read_b128 v[164:167], v225 offset:2048\n\t" \
    "v_mfma_f32_16x16x32_bf16 v[40:43], v[200:203], v[136:139], v[40:43]\n\t" \
    "ds_read_b128 v[168:171], v225 offset:4096\n\t" \
    "v_mfma_f32_16x16x32_bf16 v[44:47], v[204:207], v[136:139], v[44:47]\n\t" \
    "ds_read_b128 v[172:175], v225 offset:6144\n\t" \
    "s_waitcnt lgkmcnt(15)\n\t" \
    "v_mfma_f32_16x16x32_bf16 v[48:51], v[192:195], v[140:143], v[48:51]\n\t" \
    "ds_read_b128 v[176:179], v225 offset:8192\n\t" \
    "v_mfma_f32_16x16x32_bf16 v[52:55], v[196:199], v[140:143], v[52:55]\n\t" \
    "ds_read_b128 v[180:183], v225 offset:10240\n\t" \
    "v_mfma_f32_16x16x32_bf16 v[56:59], v[200:203], v[140:143], v[56:59]\n\t" \
    "ds_read_b128 v[184:187], v225 offset:12288\n\t" \
    "v_mfma_f32_16x16x32_bf16 v[60:63], v[204:207], v[140:143], v[60:63]\n\t" \
    "ds_read_b128 v[188:191], v225 offset:14336\n\t" \
    "v_mfma_f32_16x16x32_bf16 v[64:67], v[192:195], v[144:147], v[64:67]\n\t" \
    "s_add_u32 s51, s51, 49152\n\t" \
    "v_mfma_f32_16x16x32_bf16 v[68:71], v[196:199], v[144:147], v[68:71]\n\t" \
    "s_cmp_eq_u32 s51, 147456\n\t" \
    "v_mfma_f32_16x16x32_bf16 v[72:75], v[200:203], v[144:147], v[72:75]\n\t" \
    "s_cselect_b32 s50, s53, s54\n\t" \
    "v_mfma_f32_16x16x32_bf16 v[76:79], v[204:207], v[144:147], v[76:79]\n\t" \
    "s_cselect_b32 s51, 0, s51\n\t" \
    "s_waitcnt lgkmcnt(15)\n\t" \
    "v_mfma_f32_16x16x32_bf16 v[80:83], v[192:195], v[148:151], v[80:83]\n\t" \
    "v_add_u32 v224, s50, v224\n\t" \
    "v_mfma_f32_16x16x32_bf16 v[84:87], v[196:199], v[148:151], v[84:87]\n\t" \
    "v_add_u32 v225, s50, v225\n\t" \
    "v_mfma_f32_16x16x32_bf16 v[88:91], v[200:203], v[148:151], v[88:91]\n\t" \
    "v_add_u32 v226, s50, v226\n\t" \
    "v_mfma_f32_16x16x32_bf16 v[92:95], v[204:207], v[148:151], v[92:95]\n\t" \
    "v_add_u32 v227, s50, v227\n\t" \
    "v_mfma_f32_16x16x32_bf16 v[96:99], v[192:195], v[152:155], v[96:99]\n\t" \
    "v_add_u32 v228, s50, v228\n\t" \
    "v_mfma_f32_16x16x32_bf16 v[100:103], v[196:199], v[152:155], v[100:103]\n\t" \
    "v_add_u32 v229, s50, v229\n\t" \
    "v_mfma_f32_16x16x32_bf16 v[104:107], v[200:203], v[152:155], v[104:107]\n\t" \
    "v_mfma_f32_16x16x32_bf16 v[108:111], v[204:207], v[152:155], v[108:111]\n\t" \
    "v_mfma_f32_16x16x32_bf16 v[112:115], v[192:195], v[156:159], v[112:115]\n\t" \
    "v_mfma_f32_16x16x32_bf16 v[116:119], v[196:199], v[156:159], v[116:119]\n\t" \
    "v_mfma_f32_16x16x32_bf16 v[120:123], v[200:203], v[156:159], v[120:123]\n\t" \
    "v_mfma_f32_16x16x32_bf16 v[124:127], v[204:207], v[156:159], v[124:127]\n\t" \
    "s_waitcnt lgkmcnt(0)\n\t" \
    "s_barrier\n\t" \
    "v_mfma_f32_16x16x32_bf16 v[0:3], v[208:211], v[160:163], v[0:3]\n\t" \
    "ds_read_b64_tr_b16 v[192:193], v226 offset:0\n\t" \
    "v_mfma_f32_16x16x32_bf16 v[4:7], v[212:215], v[160:163], v[4:7]\n\t" \
    "ds_read_b64_tr_b16 v[194:195], v226 offset:512\n\t" \
    "v_mfma_f32_16x16x32_bf16 v[8:11], v[216:219], v[160:163], v[8:11]\n\t" \
    "ds_read_b128 v[128:131], v224 offset:0\n\t" \
    "v_mfma_f32_16x16x32_bf16 v[12:15], v[220:223], v[160:163], v[12:15]\n\t" \
    "ds_read_b64_tr_b16 v[196:197], v227 offset:0\n\t" \
    "v_mfma_f32_16x16x32_bf16 v[16:19], v[208:211], v[164:167], v[16:19]\n\t" \
    "ds_read_b64_tr_b16 v[198:199], v227 offset:512\n\t" \
    "v_mfma_f32_16x16x32_bf16 v[20:23], v[212:215], v[164:167], v[20:23]\n\t" \
    "ds_read_b64_tr_b16 v[200:201], v228 offset:0\n\t" \
    "v_mfma_f32_16x16x32_bf16 v[24:27], v[216:219], v[164:167], v[24:27]\n\t" \
    "ds_read_b64_tr_b16 v[202:203], v228 offset:512\n\t" \
    "v_mfma_f32_16x16x32_bf16 v[28:31], v[220:223], v[164:167], v[28:31]\n\t" \
    "ds_read_b64_tr_b16 v[204:205], v229 offset:0\n\t" \
    "v_mfma_f32_16x16x32_bf16 v[32:35], v[208:211], v[168:171], v[32:35]\n\t" \
    "ds_read_b64_tr_b16 v[206:207], v229 offset:512\n\t" \
    "v_mfma_f32_16x16x32_bf16 v[36:39], v[212:215], v[168:171], v[36:39]\n\t" \
    "ds_read_b128 v[132:135], v224 offset:2048\n\t" \
    "v_mfma_f32_16x16x32_bf16 v[40:43], v[216:219], v[168:171], v[40:43]\n\t" \
    "ds_read_b128 v[136:139], v224 offset:4096\n\t" \
    "v_mfma_f32_16x16x32_bf16 v[44:47], v[220:223], v[168:171], v[44:47]\n\t" \
    "ds_read_b128 v[140:143], v224 offset:6144\n\t" \
    "v_mfma_f32_16x16x32_bf16 v[48:51], v[208:211], v[172:175], v[48:51]\n\t" \
    "ds_read_b128 v[144:147], v224 offset:8192\n\t" \
    "v_mfma_f32_16x16x32_bf16 v[52:55], v[212:215], v[172:175], v[52:55]\n\t" \
    "ds_read_b128 v[148:151], v224 offset:10240\n\t" \
    "v_mfma_f32_16x16x32_bf16 v[56:59], v[216:219], v[172:175], v[56:59]\n\t" \
    "ds_read_b128 v[152:155], v224 offset:12288\n\t" \
    "v_mfma_f32_16x16x32_bf16 v[60:63], v[220:223], v[172:175], v[60:63]\n\t" \
    "ds_read_b128 v[156:159], v224 offset:14336\n\t" \
    "v_mfma_f32_16x16x32_bf16 v[64:67], v[208:211], v[176:179], v[64:67]\n\t" \
    "s_add_u32 s70, s74, 4195328\n\t" \
    "s_addc_u32 s71, s75, 0\n\t" \
    "global_load_dwordx4 v[252:255], %[vl16], s[70:71]\n\t" \
    "v_mfma_f32_16x16x32_bf16 v[68:71], v[212:215], v[176:179], v[68:71]\n\t" \
    "s_add_u32 s70, s72, 4196352\n\t" \
    "s_addc_u32 s71, s73, 0\n\t" \
    "global_load_dwordx4 v[252:255], %[vl16], s[70:71]\n\t" \
    "v_mfma_f32_16x16x32_bf16 v[72:75], v[216:219], v[176:179], v[72:75]\n\t" \
    "v_mfma_f32_16x16x32_bf16 v[76:79], v[220:223], v[176:179], v[76:79]\n\t" \
    "v_mfma_f32_16x16x32_bf16 v[80:83], v[208:211], v[180:183], v[80:83]\n\t" \
    "v_mfma_f32_16x16x32_bf16 v[84:87], v[212:215], v[180:183], v[84:87]\n\t" \
    "v_mfma_f32_16x16x32_bf16 v[88:91], v[216:219], v[180:183], v[88:91]\n\t" \
    "v_mfma_f32_16x16x32_bf16 v[92:95], v[220:223], v[180:183], v[92:95]\n\t" \
    "v_mfma_f32_16x16x32_bf16 v[96:99], v[208:211], v[184:187], v[96:99]\n\t" \
    "v_mfma_f32_16x16x32_bf16 v[100:103], v[212:215], v[184:187], v[100:103]\n\t" \
    "v_mfma_f32_16x16x32_bf16 v[104:107], v[216:219], v[184:187], v[104:107]\n\t" \
    "v_mfma_f32_16x16x32_bf16 v[108:111], v[220:223], v[184:187], v[108:111]\n\t" \
    "v_mfma_f32_16x16x32_bf16 v[112:115], v[208:211], v[188:191], v[112:115]\n\t" \
    "v_mfma_f32_16x16x32_bf16 v[116:119], v[212:215], v[188:191], v[116:119]\n\t" \
    "v_mfma_f32_16x16x32_bf16 v[120:123], v[216:219], v[188:191], v[120:123]\n\t" \
    "v_mfma_f32_16x16x32_bf16 v[124:127], v[220:223], v[188:191], v[124:127]\n\t" \
    "s_waitcnt lgkmcnt(13)\n\t" \
    "v_mfma_f32_16x16x32_bf16 v[0:3], v[192:195], v[128:131], v[0:3]\n\t" \
    "ds_read_b64_tr_b16 v[208:209], v226 offset:4096\n\t" \
    "s_waitcnt lgkmcnt(12)\n\t" \
    "v_mfma_f32_16x16x32_bf16 v[4:7], v[196:199], v[128:131], v[4:7]\n\t" \
    "ds_read_b64_tr_b16 v[210:211], v226 offset:4608\n\t" \
    "s_waitcnt lgkmcnt(11)\n\t" \
    "v_mfma_f32_16x16x32_bf16 v[8:11], v[200:203], v[128:131], v[8:11]\n\t" \
    "ds_read_b128 v[160:163], v225 offset:0\n\t" \
    "s_waitcnt lgkmcnt(10)\n\t" \
    "v_mfma_f32_16x16x32_bf16 v[12:15], v[204:207], v[128:131], v[12:15]\n\t" \
    "ds_read_b64_tr_b16 v[212:213], v227 offset:4096\n\t" \
    "s_waitcnt lgkmcnt(10)\n\t" \
    "v_mfma_f32_16x16x32_bf16 v[16:19], v[192:195], v[132:135], v[16:19]\n\t" \
    "ds_read_b64_tr_b16 v[214:215], v227 offset:4608\n\t" \
    "v_mfma_f32_16x16x32_bf16 v[20:23], v[196:199], v[132:135], v[20:23]\n\t" \
    "ds_read_b64_tr_b16 v[216:217], v228 offset:4096\n\t" \
    "v_mfma_f32_16x16x32_bf16 v[24:27], v[200:203], v[132:135], v[24:27]\n\t" \
    "ds_read_b64_tr_b16 v[218:219], v228 offset:4608\n\t" \
    "v_mfma_f32_16x16x32_bf16 v[28:31], v[204:207], v[132:135], v[28:31]\n\t" \
    "ds_read_b64_tr_b16 v[220:221], v229 offset:4096\n\t" \
    "s_waitcnt lgkmcnt(13)\n\t" \
    "v_mfma_f32_16x16x32_bf16 v[32:35], v[192:195], v[136:139], v[32:35]\n\t" \
    "ds_read_b64_tr_b16 v[222:223], v229 offset:4608\n\t" \
    "v_mfma_f32_16x16x32_bf16 v[36:39], v[196:199], v[136:139], v[36:39]\n\t" \
    "ds_read_b128 v[164:167], v225 offset:2048\n\t" \
    "v_mfma_f32_16x16x32_bf16 v[40:43], v[200:203], v[136:139], v[40:43]\n\t" \
    "ds_read_b128 v[168:171], v225 offset:4096\n\t" \
    "v_mfma_f32_16x16x32_bf16 v[44:47], v[204:207], v[136:139], v[44:47]\n\t" \
    "ds_read_b128 v[172:175], v225 offset:6144\n\t" \
    "s_waitcnt lgkmcnt(15)\n\t" \
    "v_mfma_f32_16x16x32_bf16 v[48:51], v[192:195], v[140:143], v[48:51]\n\t" \
    "ds_read_b128 v[176:179], v225 offset:8192\n\t" \
    "v_mfma_f32_16x16x32_bf16 v[52:55], v[196:199], v[140:143], v[52:55]\n\t" \
    "ds_read_b128 v[180:183], v225 offset:10240\n\t" \
    "v_mfma_f32_16x16x32_bf16 v[56:59], v[200:203], v[140:143], v[56:59]\n\t" \
    "ds_read_b128 v[184:187], v225 offset:12288\n\t" \
    "v_mfma_f32_16x16x32_bf16 v[60:63], v[204:207], v[140:143], v[60:63]\n\t" \
    "ds_read_b128 v[188:191], v225 offset:14336\n\t" \
    "v_mfma_f32_16x16x32_bf16 v[64:67], v[192:195], v[144:147], v[64:67]\n\t" \
    "s_add_u32 s51, s51, 49152\n\t" \
    "v_mfma_f32_16x16x32_bf16 v[68:71], v[196:199], v[144:147], v[68:71]\n\t" \
    "s_cmp_eq_u32 s51, 147456\n\t" \
    "v_mfma_f32_16x16x32_bf16 v[72:75], v[200:203], v[144:147], v[72:75]\n\t" \
    "s_cselect_b32 s50, s53, s54\n\t" \
    "v_mfma_f32_16x16x32_bf16 v[76:79], v[204:207], v[144:147], v[76:79]\n\t" \
    "s_cselect_b32 s51, 0, s51\n\t" \
    "s_waitcnt lgkmcnt(15)\n\t" \
    "v_mfma_f32_16x16x32_bf16 v[80:83], v[192:195], v[148:151], v[80:83]\n\t" \
    "v_add_u32 v224, s50, v224\n\t" \
    "v_mfma_f32_16x16x32_bf16 v[84:87], v[196:199], v[148:151], v[84:87]\n\t" \
    "v_add_u32 v225, s50, v225\n\t" \
    "v_mfma_f32_16x16x32_bf16 v[88:91], v[200:203], v[148:151], v[88:91]\n\t" \
    "v_add_u32 v226, s50, v226\n\t" \
    "v_mfma_f32_16x16x32_bf16 v[92:95], v[204:207], v[148:151], v[92:95]\n\t" \
    "v_add_u32 v227, s50, v227\n\t" \
    "v_mfma_f32_16x16x32_bf16 v[96:99], v[192:195], v[152:155], v[96:99]\n\t" \
    "v_add_u32 v228, s50, v228\n\t" \
    "v_mfma_f32_16x16x32_bf16 v[100:103], v[196:199], v[152:155], v[100:103]\n\t" \
    "v_add_u32 v229, s50, v229\n\t" \
    "v_mfma_f32_16x16x32_bf16 v[104:107], v[200:203], v[152:155], v[104:107]\n\t" \
    "v_mfma_f32_16x16x32_bf16 v[108:111], v[204:207], v[152:155], v[108:111]\n\t" \
    "v_mfma_f32_16x16x32_bf16 v[112:115], v[192:195], v[156:159], v[112:115]\n\t" \
    "v_mfma_f32_16x16x32_bf16 v[116:119], v[196:199], v[156:159], v[116:119]\n\t" \
    "v_mfma_f32_16x16x32_bf16 v[120:123], v[200:203], v[156:159], v[120:123]\n\t" \
    "v_mfma_f32_16x16x32_bf16 v[124:127], v[204:207], v[156:159], v[124:127]\n\t" \
    "s_waitcnt lgkmcnt(0)\n\t" \
    "s_barrier\n\t" \
    "v_mfma_f32_16x16x32_bf16 v[0:3], v[208:211], v[160:163], v[0:3]\n\t" \
    "ds_read_b64_tr_b16 v[192:193], v226 offset:0\n\t" \
    "v_mfma_f32_16x16x32_bf16 v[4:7], v[212:215], v[160:163], v[4:7]\n\t" \
    "ds_read_b64_tr_b16 v[194:195], v226 offset:512\n\t" \
    "v_mfma_f32_16x16x32_bf16 v[8:11], v[216:219], v[160:163], v[8:11]\n\t" \
    "ds_read_b128 v[128:131], v224 offset:0\n\t" \
    "v_mfma_f32_16x16x32_bf16 v[12:15], v[220:223], v[160:163], v[12:15]\n\t" \
    "ds_read_b64_tr_b16 v[196:197], v227 offset:0\n\t" \
    "v_mfma_f32_16x16x32_bf16 v[16:19], v[208:211], v[164:167], v[16:19]\n\t" \
    "ds_read_b64_tr_b16 v[198:199], v227 offset:512\n\t" \
    "v_mfma_f32_16x16x32_bf16 v[20:23], v[212:215], v[164:167], v[20:23]\n\t" \
    "ds_read_b64_tr_b16 v[200:201], v228 offset:0\n\t" \
    "v_mfma_f32_16x16x32_bf16 v[24:27], v[216:219], v[164:167], v[24:27]\n\t" \
    "ds_read_b64_tr_b16 v[202:203], v228 offset:512\n\t" \
    "v_mfma_f32_16x16x32_bf16 v[28:31], v[220:223], v[164:167], v[28:31]\n\t" \
    "ds_read_b64_tr_b16 v[204:205], v229 offset:0\n\t" \
    "v_mfma_f32_16x16x32_bf16 v[32:35], v[208:211], v[168:171], v[32:35]\n\t" \
    "ds_read_b64_tr_b16 v[206:207], v229 offset:512\n\t" \
    "v_mfma_f32_16x16x32_bf16 v[36:39], v[212:215], v[168:171], v[36:39]\n\t" \
    "ds_read_b128 v[132:135], v224 offset:2048\n\t" \
    "v_mfma_f32_16x16x32_bf16 v[40:43], v[216:219], v[168:171], v[40:43]\n\t" \
    "ds_read_b128 v[136:139], v224 offset:4096\n\t" \
    "v_mfma_f32_16x16x32_bf16 v[44:47], v[220:223], v[168:171], v[44:47]\n\t" \
    "ds_read_b128 v[140:143], v224 offset:6144\n\t" \
    "v_mfma_f32_16x16x32_bf16 v[48:51], v[208:211], v[172:175], v[48:51]\n\t" \
    "ds_read_b128 v[144:147], v224 offset:8192\n\t" \
    "v_mfma_f32_16x16x32_bf16 v[52:55], v[212:215], v[172:175], v[52:55]\n\t" \
    "ds_read_b128 v[148:151], v224 offset:10240\n\t" \
    "v_mfma_f32_16x16x32_bf16 v[56:59], v[216:219], v[172:175], v[56:59]\n\t" \
    "ds_read_b128 v[152:155], v224 offset:12288\n\t" \
    "v_mfma_f32_16x16x32_bf16 v[60:63], v[220:223], v[172:175], v[60:63]\n\t" \
    "ds_read_b128 v[156:159], v224 offset:14336\n\t" \
    "v_mfma_f32_16x16x32_bf16 v[64:67], v[208:211], v[176:179], v[64:67]\n\t" \
    "s_add_u32 s70, s74, 6292480\n\t" \
    "s_addc_u32 s71, s75, 0\n\t" \
    "global_load_dwordx4 v[252:255], %[vl16], s[70:71]\n\t" \
    "v_mfma_f32_16x16x32_bf16 v[68:71], v[212:215], v[176:179], v[68:71]\n\t" \
    "s_add_u32 s70, s72, 6293504\n\t" \
    "s_addc_u32 s71, s73, 0\n\t" \
    "global_load_dwordx4 v[252:255], %[vl16], s[70:71]\n\t" \
    "v_mfma_f32_16x16x32_bf16 v[72:75], v[216:219], v[176:179], v[72:75]\n\t" \
    "v_mfma_f32_16x16x32_bf16 v[76:79], v[220:223], v[176:179], v[76:79]\n\t" \
    "v_mfma_f32_16x16x32_bf16 v[80:83], v[208:211], v[180:183], v[80:83]\n\t" \
    "v_mfma_f32_16x16x32_bf16 v[84:87], v[212:215], v[180:183], v[84:87]\n\t" \
    "v_mfma_f32_16x16x32_bf16 v[88:91], v[216:219], v[180:183], v[88:91]\n\t" \
    "v_mfma_f32_16x16x32_bf16 v[92:95], v[220:223], v[180:183], v[92:95]\n\t" \
    "v_mfma_f32_16x16x32_bf16 v[96:99], v[208:211], v[184:187], v[96:99]\n\t" \
    "v_mfma_f32_16x16x32_bf16 v[100:103], v[212:215], v[184:187], v[100:103]\n\t" \
    "v_mfma_f32_16x16x32_bf16 v[104:107], v[216:219], v[184:187], v[104:107]\n\t" \
    "v_mfma_f32_16x16x32_bf16 v[108:111], v[220:223], v[184:187], v[108:111]\n\t" \
    "v_mfma_f32_16x16x32_bf16 v[112:115], v[208:211], v[188:191], v[112:115]\n\t" \
    "v_mfma_f32_16x16x32_bf16 v[116:119], v[212:215], v[188:191], v[116:119]\n\t" \
    "v_mfma_f32_16x16x32_bf16 v[120:123], v[216:219], v[188:191], v[120:123]\n\t" \
    "v_mfma_f32_16x16x32_bf16 v[124:127], v[220:223], v[188:191], v[124:127]\n\t" \
    "s_waitcnt lgkmcnt(13)\n\t" \
    "v_mfma_f32_16x16x32_bf16 v[0:3], v[192:195], v[128:131], v[0:3]\n\t" \
    "ds_read_b64_tr_b16 v[208:209], v226 offset:4096\n\t" \
    "s_waitcnt lgkmcnt(12)\n\t" \
    "v_mfma_f32_16x16x32_bf16 v[4:7], v[196:199], v[128:131], v[4:7]\n\t" \
    "ds_read_b64_tr_b16 v[210:211], v226 offset:4608\n\t" \
    "s_waitcnt lgkmcnt(11)\n\t" \
    "v_mfma_f32_16x16x32_bf16 v[8:11], v[200:203], v[128:131], v[8:11]\n\t" \
    "ds_read_b128 v[160:163], v225 offset:0\n\t" \
    "s_waitcnt lgkmcnt(10)\n\t" \
    "v_mfma_f32_16x16x32_bf16 v[12:15], v[204:207], v[128:131], v[12:15]\n\t" \
    "ds_read_b64_tr_b16 v[212:213], v227 offset:4096\n\t" \
    "s_waitcnt lgkmcnt(10)\n\t" \
    "v_mfma_f32_16x16x32_bf16 v[16:19], v[192:195], v[132:135], v[16:19]\n\t" \
    "ds_read_b64_tr_b16 v[214:215], v227 offset:4608\n\t" \
    "v_mfma_f32_16x16x32_bf16 v[20:23], v[196:199], v[132:135], v[20:23]\n\t" \
    "ds_read_b64_tr_b16 v[216:217], v228 offset:4096\n\t" \
    "v_mfma_f32_16x16x32_bf16 v[24:27], v[200:203], v[132:135], v[24:27]\n\t" \
    "ds_read_b64_tr_b16 v[218:219], v228 offset:4608\n\t" \
    "v_mfma_f32_16x16x32_bf16 v[28:31], v[204:207], v[132:135], v[28:31]\n\t" \
    "ds_read_b64_tr_b16 v[220:221], v229 offset:4096\n\t" \
    "s_waitcnt lgkmcnt(13)\n\t" \
    "v_mfma_f32_16x16x32_bf16 v[32:35], v[192:195], v[136:139], v[32:35]\n\t" \
    "ds_read_b64_tr_b16 v[222:223], v229 offset:4608\n\t" \
    "v_mfma_f32_16x16x32_bf16 v[36:39], v[196:199], v[136:139], v[36:39]\n\t" \
    "ds_read_b128 v[164:167], v225 offset:2048\n\t" \
    "v_mfma_f32_16x16x32_bf16 v[40:43], v[200:203], v[136:139], v[40:43]\n\t" \
    "ds_read_b128 v[168:171], v225 offset:4096\n\t" \
    "v_mfma_f32_16x16x32_bf16 v[44:47], v[204:207], v[136:139], v[44:47]\n\t" \
    "ds_read_b128 v[172:175], v225 offset:6144\n\t" \
    "s_waitcnt lgkmcnt(15)\n\t" \
    "v_mfma_f32_16x16x32_bf16 v[48:51], v[192:195], v[140:143], v[48:51]\n\t" \
    "ds_read_b128 v[176:179], v225 offset:8192\n\t" \
    "v_mfma_f32_16x16x32_bf16 v[52:55], v[196:199], v[140:143], v[52:55]\n\t" \
    "ds_read_b128 v[180:183], v225 offset:10240\n\t" \
    "v_mfma_f32_16x16x32_bf16 v[56:59], v[200:203], v[140:143], v[56:59]\n\t" \
    "ds_read_b128 v[184:187], v225 offset:12288\n\t" \
    "v_mfma_f32_16x16x32_bf16 v[60:63], v[204:207], v[140:143], v[60:63]\n\t" \
    "ds_read_b128 v[188:191], v225 offset:14336\n\t" \
    "v_mfma_f32_16x16x32_bf16 v[64:67], v[192:195], v[144:147], v[64:67]\n\t" \
    "s_add_u32 s51, s51, 49152\n\t" \
    "v_mfma_f32_16x16x32_bf16 v[68:71], v[196:199], v[144:147], v[68:71]\n\t" \
    "s_cmp_eq_u32 s51, 147456\n\t" \
    "v_mfma_f32_16x16x32_bf16 v[72:75], v[200:203], v[144:147], v[72:75]\n\t" \
    "s_cselect_b32 s50, s53, s54\n\t" \
    "v_mfma_f32_16x16x32_bf16 v[76:79], v[204:207], v[144:147], v[76:79]\n\t" \
    "s_cselect_b32 s51, 0, s51\n\t" \
    "s_waitcnt lgkmcnt(15)\n\t" \
    "v_mfma_f32_16x16x32_bf16 v[80:83], v[192:195], v[148:151], v[80:83]\n\t" \
    "v_add_u32 v224, s50, v224\n\t" \
    "v_mfma_f32_16x16x32_bf16 v[84:87], v[196:199], v[148:151], v[84:87]\n\t" \
    "v_add_u32 v225, s50, v225\n\t" \
    "v_mfma_f32_16x16x32_bf16 v[88:91], v[200:203], v[148:151], v[88:91]\n\t" \
    "v_add_u32 v226, s50, v226\n\t" \
    "v_mfma_f32_16x16x32_bf16 v[92:95], v[204:207], v[148:151], v[92:95]\n\t" \
    "v_add_u32 v227, s50, v227\n\t" \
    "v_mfma_f32_16x16x32_bf16 v[96:99], v[192:195], v[152:155], v[96:99]\n\t" \
    "v_add_u32 v228, s50, v228\n\t" \
    "v_mfma_f32_16x16x32_bf16 v[100:103], v[196:199], v[152:155], v[100:103]\n\t" \
    "v_add_u32 v229, s50, v229\n\t" \
    "v_mfma_f32_16x16x32_bf16 v[104:107], v[200:203], v[152:155], v[104:107]\n\t" \
    "v_mfma_f32_16x16x32_bf16 v[108:111], v[204:207], v[152:155], v[108:111]\n\t" \
    "v_mfma_f32_16x16x32_bf16 v[112:115], v[192:195], v[156:159], v[112:115]\n\t" \
    "v_mfma_f32_16x16x32_bf16 v[116:119], v[196:199], v[156:159], v[116:119]\n\t" \
    "v_mfma_f32_16x16x32_bf16 v[120:123], v[200:203], v[156:159], v[120:123]\n\t" \
    "v_mfma_f32_16x16x32_bf16 v[124:127], v[204:207], v[156:159], v[124:127]\n\t" \
    "s_waitcnt lgkmcnt(0)\n\t" \
    "s_barrier\n\t" \
    "v_mfma_f32_16x16x32_bf16 v[0:3], v[208:211], v[160:163], v[0:3]\n\t" \
    "ds_read_b64_tr_b16 v[192:193], v226 offset:0\n\t" \
    "v_mfma_f32_16x16x32_bf16 v[4:7], v[212:215], v[160:163], v[4:7]\n\t" \
    "ds_read_b64_tr_b16 v[194:195], v226 offset:512\n\t" \
    "v_mfma_f32_16x16x32_bf16 v[8:11], v[216:219], v[160:163], v[8:11]\n\t" \
    "ds_read_b128 v[128:131], v224 offset:0\n\t" \
    "v_mfma_f32_16x16x32_bf16 v[12:15], v[220:223], v[160:163], v[12:15]\n\t" \
    "ds_read_b64_tr_b16 v[196:197], v227 offset:0\n\t" \
    "v_mfma_f32_16x16x32_bf16 v[16:19], v[208:211], v[164:167], v[16:19]\n\t" \
    "ds_read_b64_tr_b16 v[198:199], v227 offset:512\n\t" \
    "v_mfma_f32_16x16x32_bf16 v[20:23], v[212:215], v[164:167], v[20:23]\n\t" \
    "ds_read_b64_tr_b16 v[200:201], v228 offset:0\n\t" \
    "v_mfma_f32_16x16x32_bf16 v[24:27], v[216:219], v[164:167], v[24:27]\n\t" \
    "ds_read_b64_tr_b16 v[202:203], v228 offset:512\n\t" \
    "v_mfma_f32_16x16x32_bf16 v[28:31], v[220:223], v[164:167], v[28:31]\n\t" \
    "ds_read_b64_tr_b16 v[204:205], v229 offset:0\n\t" \
    "v_mfma_f32_16x16x32_bf16 v[32:35], v[208:211], v[168:171], v[32:35]\n\t" \
    "ds_read_b64_tr_b16 v[206:207], v229 offset:512\n\t" \
    "v_mfma_f32_16x16x32_bf16 v[36:39], v[212:215], v[168:171], v[36:39]\n\t" \
    "ds_read_b128 v[132:135], v224 offset:2048\n\t" \
    "v_mfma_f32_16x16x32_bf16 v[40:43], v[216:219], v[168:171], v[40:43]\n\t" \
    "ds_read_b128 v[136:139], v224 offset:4096\n\t" \
    "v_mfma_f32_16x16x32_bf16 v[44:47], v[220:223], v[168:171], v[44:47]\n\t" \
    "ds_read_b128 v[140:143], v224 offset:6144\n\t" \
    "v_mfma_f32_16x16x32_bf16 v[48:51], v[208:211], v[172:175], v[48:51]\n\t" \
    "ds_read_b128 v[144:147], v224 offset:8192\n\t" \
    "v_mfma_f32_16x16x32_bf16 v[52:55], v[212:215], v[172:175], v[52:55]\n\t" \
    "ds_read_b128 v[148:151], v224 offset:10240\n\t" \
    "v_mfma_f32_16x16x32_bf16 v[56:59], v[216:219], v[172:175], v[56:59]\n\t" \
    "ds_read_b128 v[152:155], v224 offset:12288\n\t" \
    "v_mfma_f32_16x16x32_bf16 v[60:63], v[220:223], v[172:175], v[60:63]\n\t" \
    "ds_read_b128 v[156:159], v224 offset:14336\n\t" \
    "v_mfma_f32_16x16x32_bf16 v[64:67], v[208:211], v[176:179], v[64:67]\n\t" \
    "s_add_u32 s70, s76, 1048576\n\t" \
    "s_addc_u32 s71, s77, 0\n\t" \
    "global_load_dwordx4 v[252:255], %[vl16], s[70:71]\n\t" \
    "v_mfma_f32_16x16x32_bf16 v[68:71], v[212:215], v[176:179], v[68:71]\n\t" \
    "s_add_u32 s70, s74, 2048\n\t" \
    "s_addc_u32 s71, s75, 0\n\t" \
    "global_load_dwordx4 v[252:255], %[vl16], s[70:71]\n\t" \
    "v_mfma_f32_16x16x32_bf16 v[72:75], v[216:219], v[176:179], v[72:75]\n\t" \
    "v_mfma_f32_16x16x32_bf16 v[76:79], v[220:223], v[176:179], v[76:79]\n\t" \
    "v_mfma_f32_16x16x32_bf16 v[80:83], v[208:211], v[180:183], v[80:83]\n\t" \
    "v_mfma_f32_16x16x32_bf16 v[84:87], v[212:215], v[180:183], v[84:87]\n\t" \
    "v_mfma_f32_16x16x32_bf16 v[88:91], v[216:219], v[180:183], v[88:91]\n\t" \
    "v_mfma_f32_16x16x32_bf16 v[92:95], v[220:223], v[180:183], v[92:95]\n\t" \
    "v_mfma_f32_16x16x32_bf16 v[96:99], v[208:211], v[184:187], v[96:99]\n\t" \
    "v_mfma_f32_16x16x32_bf16 v[100:103], v[212:215], v[184:187], v[100:103]\n\t" \
    "v_mfma_f32_16x16x32_bf16 v[104:107], v[216:219], v[184:187], v[104:107]\n\t" \
    "v_mfma_f32_16x16x32_bf16 v[108:111], v[220:223], v[184:187], v[108:111]\n\t" \
    "v_mfma_f32_16x16x32_bf16 v[112:115], v[208:211], v[188:191], v[112:115]\n\t" \
    "v_mfma_f32_16x16x32_bf16 v[116:119], v[212:215], v[188:191], v[116:119]\n\t" \
    "v_mfma_f32_16x16x32_bf16 v[120:123], v[216:219], v[188:191], v[120:123]\n\t" \
    "v_mfma_f32_16x16x32_bf16 v[124:127], v[220:223], v[188:191], v[124:127]\n\t" \
    "s_waitcnt lgkmcnt(13)\n\t" \
    "v_mfma_f32_16x16x32_bf16 v[0:3], v[192:195], v[128:131], v[0:3]\n\t" \
    "ds_read_b64_tr_b16 v[208:209], v226 offset:4096\n\t" \
    "s_waitcnt lgkmcnt(12)\n\t" \
    "v_mfma_f32_16x16x32_bf16 v[4:7], v[196:199], v[128:131], v[4:7]\n\t" \
    "ds_read_b64_tr_b16 v[210:211], v226 offset:4608\n\t" \
    "s_waitcnt lgkmcnt(11)\n\t" \
    "v_mfma_f32_16x16x32_bf16 v[8:11], v[200:203], v[128:131], v[8:11]\n\t" \
    "ds_read_b128 v[160:163], v225 offset:0\n\t" \
    "s_waitcnt lgkmcnt(10)\n\t" \
    "v_mfma_f32_16x16x32_bf16 v[12:15], v[204:207], v[128:131], v[12:15]\n\t" \
    "ds_read_b64_tr_b16 v[212:213], v227 offset:4096\n\t" \
    "s_waitcnt lgkmcnt(10)\n\t" \
    "v_mfma_f32_16x16x32_bf16 v[16:19], v[192:195], v[132:135], v[16:19]\n\t" \
    "ds_read_b64_tr_b16 v[214:215], v227 offset:4608\n\t" \
    "v_mfma_f32_16x16x32_bf16 v[20:23], v[196:199], v[132:135], v[20:23]\n\t" \
    "ds_read_b64_tr_b16 v[216:217], v228 offset:4096\n\t" \
    "v_mfma_f32_16x16x32_bf16 v[24:27], v[200:203], v[132:135], v[24:27]\n\t" \
    "ds_read_b64_tr_b16 v[218:219], v228 offset:4608\n\t" \
    "v_mfma_f32_16x16x32_bf16 v[28:31], v[204:207], v[132:135], v[28:31]\n\t" \
    "ds_read_b64_tr_b16 v[220:221], v229 offset:4096\n\t" \
    "s_waitcnt lgkmcnt(13)\n\t" \
    "v_mfma_f32_16x16x32_bf16 v[32:35], v[192:195], v[136:139], v[32:35]\n\t" \
    "ds_read_b64_tr_b16 v[222:223], v229 offset:4608\n\t" \
    "v_mfma_f32_16x16x32_bf16 v[36:39], v[196:199], v[136:139], v[36:39]\n\t" \
    "ds_read_b128 v[164:167], v225 offset:2048\n\t" \
    "v_mfma_f32_16x16x32_bf16 v[40:43], v[200:203], v[136:139], v[40:43]\n\t" \
    "ds_read_b128 v[168:171], v225 offset:4096\n\t" \
    "v_mfma_f32_16x16x32_bf16 v[44:47], v[204:207], v[136:139], v[44:47]\n\t" \
    "ds_read_b128 v[172:175], v225 offset:6144\n\t" \
    "s_waitcnt lgkmcnt(15)\n\t" \
    "v_mfma_f32_16x16x32_bf16 v[48:51], v[192:195], v[140:143], v[48:51]\n\t" \
    "ds_read_b128 v[176:179], v225 offset:8192\n\t" \
    "v_mfma_f32_16x16x32_bf16 v[52:55], v[196:199], v[140:143], v[52:55]\n\t" \
    "ds_read_b128 v[180:183], v225 offset:10240\n\t" \
    "v_mfma_f32_16x16x32_bf16 v[56:59], v[200:203], v[140:143], v[56:59]\n\t" \
    "ds_read_b128 v[184:187], v225 offset:12288\n\t" \
    "v_mfma_f32_16x16x32_bf16 v[60:63], v[204:207], v[140:143], v[60:63]\n\t" \
    "ds_read_b128 v[188:191], v225 offset:14336\n\t" \
    "v_mfma_f32_16x16x32_bf16 v[64:67], v[192:195], v[144:147], v[64:67]\n\t" \
    "s_add_u32 s51, s51, 49152\n\t" \
    "v_mfma_f32_16x16x32_bf16 v[68:71], v[196:199], v[144:147], v[68:71]\n\t" \
    "s_cmp_eq_u32 s51, 147456\n\t" \
    "v_mfma_f32_16x16x32_bf16 v[72:75], v[200:203], v[144:147], v[72:75]\n\t" \
    "s_cselect_b32 s50, s53, s54\n\t" \
    "v_mfma_f32_16x16x32_bf16 v[76:79], v[204:207], v[144:147], v[76:79]\n\t" \
    "s_cselect_b32 s51, 0, s51\n\t" \
    "s_waitcnt lgkmcnt(15)\n\t" \
    "v_mfma_f32_16x16x32_bf16 v[80:83], v[192:195], v[148:151], v[80:83]\n\t" \
    "v_add_u32 v224, s50, v224\n\t" \
    "v_mfma_f32_16x16x32_bf16 v[84:87], v[196:199], v[148:151], v[84:87]\n\t" \
    "v_add_u32 v225, s50, v225\n\t" \
    "v_mfma_f32_16x16x32_bf16 v[88:91], v[200:203], v[148:151], v[88:91]\n\t" \
    "v_add_u32 v226, s50, v226\n\t" \
    "v_mfma_f32_16x16x32_bf16 v[92:95], v[204:207], v[148:151], v[92:95]\n\t" \
    "v_add_u32 v227, s50, v227\n\t" \
    "v_mfma_f32_16x16x32_bf16 v[96:99], v[192:195], v[152:155], v[96:99]\n\t" \
    "v_add_u32 v228, s50, v228\n\t" \
    "v_mfma_f32_16x16x32_bf16 v[100:103], v[196:199], v[152:155], v[100:103]\n\t" \
    "v_add_u32 v229, s50, v229\n\t" \
    "v_mfma_f32_16x16x32_bf16 v[104:107], v[200:203], v[152:155], v[104:107]\n\t" \
    "v_mfma_f32_16x16x32_bf16 v[108:111], v[204:207], v[152:155], v[108:111]\n\t" \
    "v_mfma_f32_16x16x32_bf16 v[112:115], v[192:195], v[156:159], v[112:115]\n\t" \
    "v_mfma_f32_16x16x32_bf16 v[116:119], v[196:199], v[156:159], v[116:119]\n\t" \
    "v_mfma_f32_16x16x32_bf16 v[120:123], v[200:203], v[156:159], v[120:123]\n\t" \
    "v_mfma_f32_16x16x32_bf16 v[124:127], v[204:207], v[156:159], v[124:127]\n\t" \
    "s_waitcnt lgkmcnt(0)\n\t" \
    "s_barrier\n\t" \
    "v_mfma_f32_16x16x32_bf16 v[0:3], v[208:211], v[160:163], v[0:3]\n\t" \
    "ds_read_b64_tr_b16 v[192:193], v226 offset:0\n\t" \
    "v_mfma_f32_16x16x32_bf16 v[4:7], v[212:215], v[160:163], v[4:7]\n\t" \
    "ds_read_b64_tr_b16 v[194:195], v226 offset:512\n\t" \
    "v_mfma_f32_16x16x32_bf16 v[8:11], v[216:219], v[160:163], v[8:11]\n\t" \
    "ds_read_b128 v[128:131], v224 offset:0\n\t" \
    "v_mfma_f32_16x16x32_bf16 v[12:15], v[220:223], v[160:163], v[12:15]\n\t" \
    "ds_read_b64_tr_b16 v[196:197], v227 offset:0\n\t" \
    "v_mfma_f32_16x16x32_bf16 v[16:19], v[208:211], v[164:167], v[16:19]\n\t" \
    "ds_read_b64_tr_b16 v[198:199], v227 offset:512\n\t" \
    "v_mfma_f32_16x16x32_bf16 v[20:23], v[212:215], v[164:167], v[20:23]\n\t" \
    "ds_read_b64_tr_b16 v[200:201], v228 offset:0\n\t" \
    "v_mfma_f32_16x16x32_bf16 v[24:27], v[216:219], v[164:167], v[24:27]\n\t" \
    "ds_read_b64_tr_b16 v[202:203], v228 offset:512\n\t" \
    "v_mfma_f32_16x16x32_bf16 v[28:31], v[220:223], v[164:167], v[28:31]\n\t" \
    "ds_read_b64_tr_b16 v[204:205], v229 offset:0\n\t" \
    "v_mfma_f32_16x16x32_bf16 v[32:35], v[208:211], v[168:171], v[32:35]\n\t" \
    "ds_read_b64_tr_b16 v[206:207], v229 offset:512\n\t" \
    "v_mfma_f32_16x16x32_bf16 v[36:39], v[212:215], v[168:171], v[36:39]\n\t" \
    "ds_read_b128 v[132:135], v224 offset:2048\n\t" \
    "v_mfma_f32_16x16x32_bf16 v[40:43], v[216:219], v[168:171], v[40:43]\n\t" \
    "ds_read_b128 v[136:139], v224 offset:4096\n\t" \
    "v_mfma_f32_16x16x32_bf16 v[44:47], v[220:223], v[168:171], v[44:47]\n\t" \
    "ds_read_b128 v[140:143], v224 offset:6144\n\t" \
    "v_mfma_f32_16x16x32_bf16 v[48:51], v[208:211], v[172:175], v[48:51]\n\t" \
    "ds_read_b128 v[144:147], v224 offset:8192\n\t" \
    "v_mfma_f32_16x16x32_bf16 v[52:55], v[212:215], v[172:175], v[52:55]\n\t" \
    "ds_read_b128 v[148:151], v224 offset:10240\n\t" \
    "v_mfma_f32_16x16x32_bf16 v[56:59], v[216:219], v[172:175], v[56:59]\n\t" \
    "ds_read_b128 v[152:155], v224 offset:12288\n\t" \
    "v_mfma_f32_16x16x32_bf16 v[60:63], v[220:223], v[172:175], v[60:63]\n\t" \
    "ds_read_b128 v[156:159], v224 offset:14336\n\t" \
    "v_mfma_f32_16x16x32_bf16 v[64:67], v[208:211], v[176:179], v[64:67]\n\t" \
    "s_add_u32 s70, s76, 1572864\n\t" \
    "s_addc_u32 s71, s77, 0\n\t" \
    "global_load_dwordx4 v[252:255], %[vl16], s[70:71]\n\t" \
    "v_mfma_f32_16x16x32_bf16 v[68:71], v[212:215], v[176:179], v[68:71]\n\t" \
    "s_add_u32 s70, s74, 2099200\n\t" \
    "s_addc_u32 s71, s75, 0\n\t" \
    "global_load_dwordx4 v[252:255], %[vl16], s[70:71]\n\t" \
    "v_mfma_f32_16x16x32_bf16 v[72:75], v[216:219], v[176:179], v[72:75]\n\t" \
    "v_mfma_f32_16x16x32_bf16 v[76:79], v[220:223], v[176:179], v[76:79]\n\t" \
    "v_mfma_f32_16x16x32_bf16 v[80:83], v[208:211], v[180:183], v[80:83]\n\t" \
    "v_mfma_f32_16x16x32_bf16 v[84:87], v[212:215], v[180:183], v[84:87]\n\t" \
    "v_mfma_f32_16x16x32_bf16 v[88:91], v[216:219], v[180:183], v[88:91]\n\t" \
    "v_mfma_f32_16x16x32_bf16 v[92:95], v[220:223], v[180:183], v[92:95]\n\t" \
    "v_mfma_f32_16x16x32_bf16 v[96:99], v[208:211], v[184:187], v[96:99]\n\t" \
    "v_mfma_f32_16x16x32_bf16 v[100:103], v[212:215], v[184:187], v[100:103]\n\t" \
    "v_mfma_f32_16x16x32_bf16 v[104:107], v[216:219], v[184:187], v[104:107]\n\t" \
    "v_mfma_f32_16x16x32_bf16 v[108:111], v[220:223], v[184:187], v[108:111]\n\t" \
    "v_mfma_f32_16x16x32_bf16 v[112:115], v[208:211], v[188:191], v[112:115]\n\t" \
    "v_mfma_f32_16x16x32_bf16 v[116:119], v[212:215], v[188:191], v[116:119]\n\t" \
    "v_mfma_f32_16x16x32_bf16 v[120:123], v[216:219], v[188:191], v[120:123]\n\t" \
    "v_mfma_f32_16x16x32_bf16 v[124:127], v[220:223], v[188:191], v[124:127]\n\t" \
    "s_waitcnt lgkmcnt(13)\n\t" \
    "v_mfma_f32_16x16x32_bf16 v[0:3], v[192:195], v[128:131], v[0:3]\n\t" \
    "ds_read_b64_tr_b16 v[208:209], v226 offset:4096\n\t" \
    "s_waitcnt lgkmcnt(12)\n\t" \
    "v_mfma_f32_16x16x32_bf16 v[4:7], v[196:199], v[128:131], v[4:7]\n\t" \
    "ds_read_b64_tr_b16 v[210:211], v226 offset:4608\n\t" \
    "s_waitcnt lgkmcnt(11)\n\t" \
    "v_mfma_f32_16x16x32_bf16 v[8:11], v[200:203], v[128:131], v[8:11]\n\t" \
    "ds_read_b128 v[160:163], v225 offset:0\n\t" \
    "s_waitcnt lgkmcnt(10)\n\t" \
    "v_mfma_f32_16x16x32_bf16 v[12:15], v[204:207], v[128:131], v[12:15]\n\t" \
    "ds_read_b64_tr_b16 v[212:213], v227 offset:4096\n\t" \
    "s_waitcnt lgkmcnt(10)\n\t" \
    "v_mfma_f32_16x16x32_bf16 v[16:19], v[192:195], v[132:135], v[16:19]\n\t" \
    "ds_read_b64_tr_b16 v[214:215], v227 offset:4608\n\t" \
    "v_mfma_f32_16x16x32_bf16 v[20:23], v[196:199], v[132:135], v[20:23]\n\t" \
    "ds_read_b64_tr_b16 v[216:217], v228 offset:4096\n\t" \
    "v_mfma_f32_16x16x32_bf16 v[24:27], v[200:203], v[132:135], v[24:27]\n\t" \
    "ds_read_b64_tr_b16 v[218:219], v228 offset:4608\n\t" \
    "v_mfma_f32_16x16x32_bf16 v[28:31], v[204:207], v[132:135], v[28:31]\n\t" \
    "ds_read_b64_tr_b16 v[220:221], v229 offset:4096\n\t" \
    "s_waitcnt lgkmcnt(13)\n\t" \
    "v_mfma_f32_16x16x32_bf16 v[32:35], v[192:195], v[136:139], v[32:35]\n\t" \
    "ds_read_b64_tr_b16 v[222:223], v229 offset:4608\n\t" \
    "v_mfma_f32_16x16x32_bf16 v[36:39], v[196:199], v[136:139], v[36:39]\n\t" \
    "ds_read_b128 v[164:167], v225 offset:2048\n\t" \
    "v_mfma_f32_16x16x32_bf16 v[40:43], v[200:203], v[136:139], v[40:43]\n\t" \
    "ds_read_b128 v[168:171], v225 offset:4096\n\t" \
    "v_mfma_f32_16x16x32_bf16 v[44:47], v[204:207], v[136:139], v[44:47]\n\t" \
    "ds_read_b128 v[172:175], v225 offset:6144\n\t" \
    "s_waitcnt lgkmcnt(15)\n\t" \
    "v_mfma_f32_16x16x32_bf16 v[48:51], v[192:195], v[140:143], v[48:51]\n\t" \
    "ds_read_b128 v[176:179], v225 offset:8192\n\t" \
    "v_mfma_f32_16x16x32_bf16 v[52:55], v[196:199], v[140:143], v[52:55]\n\t" \
    "ds_read_b128 v[180:183], v225 offset:10240\n\t" \
    "v_mfma_f32_16x16x32_bf16 v[56:59], v[200:203], v[140:143], v[56:59]\n\t" \
    "ds_read_b128 v[184:187], v225 offset:12288\n\t" \
    "v_mfma_f32_16x16x32_bf16 v[60:63], v[204:207], v[140:143], v[60:63]\n\t" \
    "ds_read_b128 v[188:191], v225 offset:14336\n\t" \
    "v_mfma_f32_16x16x32_bf16 v[64:67], v[192:195], v[144:147], v[64:67]\n\t" \
    "s_add_u32 s51, s51, 49152\n\t" \
    "v_mfma_f32_16x16x32_bf16 v[68:71], v[196:199], v[144:147], v[68:71]\n\t" \
    "s_cmp_eq_u32 s51, 147456\n\t" \
    "v_mfma_f32_16x16x32_bf16 v[72:75], v[200:203], v[144:147], v[72:75]\n\t" \
    "s_cselect_b32 s50, s53, s54\n\t" \
    "v_mfma_f32_16x16x32_bf16 v[76:79], v[204:207], v[144:147], v[76:79]\n\t" \
    "s_cselect_b32 s51, 0, s51\n\t" \
    "s_waitcnt lgkmcnt(15)\n\t" \
    "v_mfma_f32_16x16x32_bf16 v[80:83], v[192:195], v[148:151], v[80:83]\n\t" \
    "v_add_u32 v224, s50, v224\n\t" \
    "v_mfma_f32_16x16x32_bf16 v[84:87], v[196:199], v[148:151], v[84:87]\n\t" \
    "v_add_u32 v225, s50, v225\n\t" \
    "v_mfma_f32_16x16x32_bf16 v[88:91], v[200:203], v[148:151], v[88:91]\n\t" \
    "v_add_u32 v226, s50, v226\n\t" \
    "v_mfma_f32_16x16x32_bf16 v[92:95], v[204:207], v[148:151], v[92:95]\n\t" \
    "v_add_u32 v227, s50, v227\n\t" \
    "v_mfma_f32_16x16x32_bf16 v[96:99], v[192:195], v[152:155], v[96:99]\n\t" \
    "v_add_u32 v228, s50, v228\n\t" \
    "v_mfma_f32_16x16x32_bf16 v[100:103], v[196:199], v[152:155], v[100:103]\n\t" \
    "v_add_u32 v229, s50, v229\n\t" \
    "v_mfma_f32_16x16x32_bf16 v[104:107], v[200:203], v[152:155], v[104:107]\n\t" \
    "v_mfma_f32_16x16x32_bf16 v[108:111], v[204:207], v[152:155], v[108:111]\n\t" \
    "v_mfma_f32_16x16x32_bf16 v[112:115], v[192:195], v[156:159], v[112:115]\n\t" \
    "v_mfma_f32_16x16x32_bf16 v[116:119], v[196:199], v[156:159], v[116:119]\n\t" \
    "v_mfma_f32_16x16x32_bf16 v[120:123], v[200:203], v[156:159], v[120:123]\n\t" \
    "v_mfma_f32_16x16x32_bf16 v[124:127], v[204:207], v[156:159], v[124:127]\n\t" \
    "s_waitcnt lgkmcnt(0)\n\t" \
    "s_barrier\n\t" \
    "v_mfma_f32_16x16x32_bf16 v[0:3], v[208:211], v[160:163], v[0:3]\n\t" \
    "ds_read_b64_tr_b16 v[192:193], v226 offset:0\n\t" \
    "v_mfma_f32_16x16x32_bf16 v[4:7], v[212:215], v[160:163], v[4:7]\n\t" \
    "ds_read_b64_tr_b16 v[194:195], v226 offset:512\n\t" \
    "v_mfma_f32_16x16x32_bf16 v[8:11], v[216:219], v[160:163], v[8:11]\n\t" \
    "ds_read_b128 v[128:131], v224 offset:0\n\t" \
    "v_mfma_f32_16x16x32_bf16 v[12:15], v[220:223], v[160:163], v[12:15]\n\t" \
    "ds_read_b64_tr_b16 v[196:197], v227 offset:0\n\t" \
    "v_mfma_f32_16x16x32_bf16 v[16:19], v[208:211], v[164:167], v[16:19]\n\t" \
    "ds_read_b64_tr_b16 v[198:199], v227 offset:512\n\t" \
    "v_mfma_f32_16x16x32_bf16 v[20:23], v[212:215], v[164:167], v[20:23]\n\t" \
    "ds_read_b64_tr_b16 v[200:201], v228 offset:0\n\t" \
    "v_mfma_f32_16x16x32_bf16 v[24:27], v[216:219], v[164:167], v[24:27]\n\t" \
    "ds_read_b64_tr_b16 v[202:203], v228 offset:512\n\t" \
    "v_mfma_f32_16x16x32_bf16 v[28:31], v[220:223], v[164:167], v[28:31]\n\t" \
    "ds_read_b64_tr_b16 v[204:205], v229 offset:0\n\t" \
    "v_mfma_f32_16x16x32_bf16 v[32:35], v[208:211], v[168:171], v[32:35]\n\t" \
    "ds_read_b64_tr_b16 v[206:207], v229 offset:512\n\t" \
    "v_mfma_f32_16x16x32_bf16 v[36:39], v[212:215], v[168:171], v[36:39]\n\t" \
    "ds_read_b128 v[132:135], v224 offset:2048\n\t" \
    "v_mfma_f32_16x16x32_bf16 v[40:43], v[216:219], v[168:171], v[40:43]\n\t" \
    "ds_read_b128 v[136:139], v224 offset:4096\n\t" \
    "v_mfma_f32_16x16x32_bf16 v[44:47], v[220:223], v[168:171], v[44:47]\n\t" \
    "ds_read_b128 v[140:143], v224 offset:6144\n\t" \
    "v_mfma_f32_16x16x32_bf16 v[48:51], v[208:211], v[172:175], v[48:51]\n\t" \
    "ds_read_b128 v[144:147], v224 offset:8192\n\t" \
    "v_mfma_f32_16x16x32_bf16 v[52:55], v[212:215], v[172:175], v[52:55]\n\t" \
    "ds_read_b128 v[148:151], v224 offset:10240\n\t" \
    "v_mfma_f32_16x16x32_bf16 v[56:59], v[216:219], v[172:175], v[56:59]\n\t" \
    "ds_read_b128 v[152:155], v224 offset:12288\n\t" \
    "v_mfma_f32_16x16x32_bf16 v[60:63], v[220:223], v[172:175], v[60:63]\n\t" \
    "ds_read_b128 v[156:159], v224 offset:14336\n\t" \
    "v_mfma_f32_16x16x32_bf16 v[64:67], v[208:211], v[176:179], v[64:67]\n\t" \
    "s_add_u32 s70, s74, 4196352\n\t" \
    "s_addc_u32 s71, s75, 0\n\t" \
    "global_load_dwordx4 v[252:255], %[vl16], s[70:71]\n\t" \
    "v_mfma_f32_16x16x32_bf16 v[68:71], v[212:215], v[176:179], v[68:71]\n\t" \
    "v_mfma_f32_16x16x32_bf16 v[72:75], v[216:219], v[176:179], v[72:75]\n\t" \
    "v_mfma_f32_16x16x32_bf16 v[76:79], v[220:223], v[176:179], v[76:79]\n\t" \
    "v_mfma_f32_16x16x32_bf16 v[80:83], v[208:211], v[180:183], v[80:83]\n\t" \
    "v_mfma_f32_16x16x32_bf16 v[84:87], v[212:215], v[180:183], v[84:87]\n\t" \
    "v_mfma_f32_16x16x32_bf16 v[88:91], v[216:219], v[180:183], v[88:91]\n\t" \
    "v_mfma_f32_16x16x32_bf16 v[92:95], v[220:223], v[180:183], v[92:95]\n\t" \
    "v_mfma_f32_16x16x32_bf16 v[96:99], v[208:211], v[184:187], v[96:99]\n\t" \
    "v_mfma_f32_16x16x32_bf16 v[100:103], v[212:215], v[184:187], v[100:103]\n\t" \
    "v_mfma_f32_16x16x32_bf16 v[104:107], v[216:219], v[184:187], v[104:107]\n\t" \
    "v_mfma_f32_16x16x32_bf16 v[108:111], v[220:223], v[184:187], v[108:111]\n\t" \
    "v_mfma_f32_16x16x32_bf16 v[112:115], v[208:211], v[188:191], v[112:115]\n\t" \
    "v_mfma_f32_16x16x32_bf16 v[116:119], v[212:215], v[188:191], v[116:119]\n\t" \
    "v_mfma_f32_16x16x32_bf16 v[120:123], v[216:219], v[188:191], v[120:123]\n\t" \
    "v_mfma_f32_16x16x32_bf16 v[124:127], v[220:223], v[188:191], v[124:127]\n\t" \
    "s_waitcnt lgkmcnt(13)\n\t" \
    "v_mfma_f32_16x16x32_bf16 v[0:3], v[192:195], v[128:131], v[0:3]\n\t" \
    "ds_read_b64_tr_b16 v[208:209], v226 offset:4096\n\t" \
    "s_waitcnt lgkmcnt(12)\n\t" \
    "v_mfma_f32_16x16x32_bf16 v[4:7], v[196:199], v[128:131], v[4:7]\n\t" \
    "ds_read_b64_tr_b16 v[210:211], v226 offset:4608\n\t" \
    "s_waitcnt lgkmcnt(11)\n\t" \
    "v_mfma_f32_16x16x32_bf16 v[8:11], v[200:203], v[128:131], v[8:11]\n\t" \
    "ds_read_b128 v[160:163], v225 offset:0\n\t" \
    "s_waitcnt lgkmcnt(10)\n\t" \
    "v_mfma_f32_16x16x32_bf16 v[12:15], v[204:207], v[128:131], v[12:15]\n\t" \
    "ds_read_b64_tr_b16 v[212:213], v227 offset:4096\n\t" \
    "s_waitcnt lgkmcnt(10)\n\t" \
    "v_mfma_f32_16x16x32_bf16 v[16:19], v[192:195], v[132:135], v[16:19]\n\t" \
    "ds_read_b64_tr_b16 v[214:215], v227 offset:4608\n\t" \
    "v_mfma_f32_16x16x32_bf16 v[20:23], v[196:199], v[132:135], v[20:23]\n\t" \
    "ds_read_b64_tr_b16 v[216:217], v228 offset:4096\n\t" \
    "v_mfma_f32_16x16x32_bf16 v[24:27], v[200:203], v[132:135], v[24:27]\n\t" \
    "ds_read_b64_tr_b16 v[218:219], v228 offset:4608\n\t" \
    "v_mfma_f32_16x16x32_bf16 v[28:31], v[204:207], v[132:135], v[28:31]\n\t" \
    "ds_read_b64_tr_b16 v[220:221], v229 offset:4096\n\t" \
    "s_waitcnt lgkmcnt(13)\n\t" \
    "v_mfma_f32_16x16x32_bf16 v[32:35], v[192:195], v[136:139], v[32:35]\n\t" \
    "ds_read_b64_tr_b16 v[222:223], v229 offset:4608\n\t" \
    "v_mfma_f32_16x16x32_bf16 v[36:39], v[196:199], v[136:139], v[36:39]\n\t" \
    "ds_read_b128 v[164:167], v225 offset:2048\n\t" \
    "v_mfma_f32_16x16x32_bf16 v[40:43], v[200:203], v[136:139], v[40:43]\n\t" \
    "ds_read_b128 v[168:171], v225 offset:4096\n\t" \
    "v_mfma_f32_16x16x32_bf16 v[44:47], v[204:207], v[136:139], v[44:47]\n\t" \
    "ds_read_b128 v[172:175], v225 offset:6144\n\t" \
    "s_waitcnt lgkmcnt(15)\n\t" \
    "v_mfma_f32_16x16x32_bf16 v[48:51], v[192:195], v[140:143], v[48:51]\n\t" \
    "ds_read_b128 v[176:179], v225 offset:8192\n\t" \
    "v_mfma_f32_16x16x32_bf16 v[52:55], v[196:199], v[140:143], v[52:55]\n\t" \
    "ds_read_b128 v[180:183], v225 offset:10240\n\t" \
    "v_mfma_f32_16x16x32_bf16 v[56:59], v[200:203], v[140:143], v[56:59]\n\t" \
    "ds_read_b128 v[184:187], v225 offset:12288\n\t" \
    "v_mfma_f32_16x16x32_bf16 v[60:63], v[204:207], v[140:143], v[60:63]\n\t" \
    "ds_read_b128 v[188:191], v225 offset:14336\n\t" \
    "v_mfma_f32_16x16x32_bf16 v[64:67], v[192:195], v[144:147], v[64:67]\n\t" \
    "s_add_u32 s51, s51, 49152\n\t" \
    "v_mfma_f32_16x16x32_bf16 v[68:71], v[196:199], v[144:147], v[68:71]\n\t" \
    "s_cmp_eq_u32 s51, 147456\n\t" \
    "v_mfma_f32_16x16x32_bf16 v[72:75], v[200:203], v[144:147], v[72:75]\n\t" \
    "s_cselect_b32 s50, s53, s54\n\t" \
    "v_mfma_f32_16x16x32_bf16 v[76:79], v[204:207], v[144:147], v[76:79]\n\t" \
    "s_cselect_b32 s51, 0, s51\n\t" \
    "s_waitcnt lgkmcnt(15)\n\t" \
    "v_mfma_f32_16x16x32_bf16 v[80:83], v[192:195], v[148:151], v[80:83]\n\t" \
    "v_add_u32 v224, s50, v224\n\t" \
    "v_mfma_f32_16x16x32_bf16 v[84:87], v[196:199], v[148:151], v[84:87]\n\t" \
    "v_add_u32 v225, s50, v225\n\t" \
    "v_mfma_f32_16x16x32_bf16 v[88:91], v[200:203], v[148:151], v[88:91]\n\t" \
    "v_add_u32 v226, s50, v226\n\t" \
    "v_mfma_f32_16x16x32_bf16 v[92:95], v[204:207], v[148:151], v[92:95]\n\t" \
    "v_add_u32 v227, s50, v227\n\t" \
    "v_mfma_f32_16x16x32_bf16 v[96:99], v[192:195], v[152:155], v[96:99]\n\t" \
    "v_add_u32 v228, s50, v228\n\t" \
    "v_mfma_f32_16x16x32_bf16 v[100:103], v[196:199], v[152:155], v[100:103]\n\t" \
    "v_add_u32 v229, s50, v229\n\t" \
    "v_mfma_f32_16x16x32_bf16 v[104:107], v[200:203], v[152:155], v[104:107]\n\t" \
    "v_mfma_f32_16x16x32_bf16 v[108:111], v[204:207], v[152:155], v[108:111]\n\t" \
    "v_mfma_f32_16x16x32_bf16 v[112:115], v[192:195], v[156:159], v[112:115]\n\t" \
    "v_mfma_f32_16x16x32_bf16 v[116:119], v[196:199], v[156:159], v[116:119]\n\t" \
    "v_mfma_f32_16x16x32_bf16 v[120:123], v[200:203], v[156:159], v[120:123]\n\t" \
    "v_mfma_f32_16x16x32_bf16 v[124:127], v[204:207], v[156:159], v[124:127]\n\t" \
    "s_waitcnt lgkmcnt(0)\n\t" \
    "s_barrier\n\t" \
    "v_mfma_f32_16x16x32_bf16 v[0:3], v[208:211], v[160:163], v[0:3]\n\t" \
    "ds_read_b64_tr_b16 v[192:193], v226 offset:0\n\t" \
    "v_mfma_f32_16x16x32_bf16 v[4:7], v[212:215], v[160:163], v[4:7]\n\t" \
    "ds_read_b64_tr_b16 v[194:195], v226 offset:512\n\t" \
    "v_mfma_f32_16x16x32_bf16 v[8:11], v[216:219], v[160:163], v[8:11]\n\t" \
    "ds_read_b128 v[128:131], v224 offset:0\n\t" \
    "v_mfma_f32_16x16x32_bf16 v[12:15], v[220:223], v[160:163], v[12:15]\n\t" \
    "ds_read_b64_tr_b16 v[196:197], v227 offset:0\n\t" \
    "v_mfma_f32_16x16x32_bf16 v[16:19], v[208:211], v[164:167], v[16:19]\n\t" \
    "ds_read_b64_tr_b16 v[198:199], v227 offset:512\n\t" \
    "v_mfma_f32_16x16x32_bf16 v[20:23], v[212:215], v[164:167], v[20:23]\n\t" \
    "ds_read_b64_tr_b16 v[200:201], v228 offset:0\n\t" \
    "v_mfma_f32_16x16x32_bf16 v[24:27], v[216:219], v[164:167], v[24:27]\n\t" \
    "ds_read_b64_tr_b16 v[202:203], v228 offset:512\n\t" \
    "v_mfma_f32_16x16x32_bf16 v[28:31], v[220:223], v[164:167], v[28:31]\n\t" \
    "ds_read_b64_tr_b16 v[204:205], v229 offset:0\n\t" \
    "v_mfma_f32_16x16x32_bf16 v[32:35], v[208:211], v[168:171], v[32:35]\n\t" \
    "ds_read_b64_tr_b16 v[206:207], v229 offset:512\n\t" \
    "v_mfma_f32_16x16x32_bf16 v[36:39], v[212:215], v[168:171], v[36:39]\n\t" \
    "ds_read_b128 v[132:135], v224 offset:2048\n\t" \
    "v_mfma_f32_16x16x32_bf16 v[40:43], v[216:219], v[168:171], v[40:43]\n\t" \
    "ds_read_b128 v[136:139], v224 offset:4096\n\t" \
    "v_mfma_f32_16x16x32_bf16 v[44:47], v[220:223], v[168:171], v[44:47]\n\t" \
    "ds_read_b128 v[140:143], v224 offset:6144\n\t" \
    "v_mfma_f32_16x16x32_bf16 v[48:51], v[208:211], v[172:175], v[48:51]\n\t" \
    "ds_read_b128 v[144:147], v224 offset:8192\n\t" \
    "v_mfma_f32_16x16x32_bf16 v[52:55], v[212:215], v[172:175], v[52:55]\n\t" \
    "ds_read_b128 v[148:151], v224 offset:10240\n\t" \
    "v_mfma_f32_16x16x32_bf16 v[56:59], v[216:219], v[172:175], v[56:59]\n\t" \
    "ds_read_b128 v[152:155], v224 offset:12288\n\t" \
    "v_mfma_f32_16x16x32_bf16 v[60:63], v[220:223], v[172:175], v[60:63]\n\t" \
    "ds_read_b128 v[156:159], v224 offset:14336\n\t" \
    "v_mfma_f32_16x16x32_bf16 v[64:67], v[208:211], v[176:179], v[64:67]\n\t" \
    "s_add_u32 s70, s74, 6293504\n\t" \
    "s_addc_u32 s71, s75, 0\n\t" \
    "global_load_dwordx4 v[252:255], %[vl16], s[70:71]\n\t" \
    "v_mfma_f32_16x16x32_bf16 v[68:71], v[212:215], v[176:179], v[68:71]\n\t" \
    "v_mfma_f32_16x16x32_bf16 v[72:75], v[216:219], v[176:179], v[72:75]\n\t" \
    "v_mfma_f32_16x16x32_bf16 v[76:79], v[220:223], v[176:179], v[76:79]\n\t" \
    "v_mfma_f32_16x16x32_bf16 v[80:83], v[208:211], v[180:183], v[80:83]\n\t" \
    "v_mfma_f32_16x16x32_bf16 v[84:87], v[212:215], v[180:183], v[84:87]\n\t" \
    "v_mfma_f32_16x16x32_bf16 v[88:91], v[216:219], v[180:183], v[88:91]\n\t" \
    "v_mfma_f32_16x16x32_bf16 v[92:95], v[220:223], v[180:183], v[92:95]\n\t" \
    "v_mfma_f32_16x16x32_bf16 v[96:99], v[208:211], v[184:187], v[96:99]\n\t" \
    "v_mfma_f32_16x16x32_bf16 v[100:103], v[212:215], v[184:187], v[100:103]\n\t" \
    "v_mfma_f32_16x16x32_bf16 v[104:107], v[216:219], v[184:187], v[104:107]\n\t" \
    "v_mfma_f32_16x16x32_bf16 v[108:111], v[220:223], v[184:187], v[108:111]\n\t" \
    "v_mfma_f32_16x16x32_bf16 v[112:115], v[208:211], v[188:191], v[112:115]\n\t" \
    "v_mfma_f32_16x16x32_bf16 v[116:119], v[212:215], v[188:191], v[116:119]\n\t" \
    "v_mfma_f32_16x16x32_bf16 v[120:123], v[216:219], v[188:191], v[120:123]\n\t" \
    "v_mfma_f32_16x16x32_bf16 v[124:127], v[220:223], v[188:191], v[124:127]\n\t" \
    "s_waitcnt lgkmcnt(13)\n\t" \
    "v_mfma_f32_16x16x32_bf16 v[0:3], v[192:195], v[128:131], v[0:3]\n\t" \
    "ds_read_b64_tr_b16 v[208:209], v226 offset:4096\n\t" \
    "s_waitcnt lgkmcnt(12)\n\t" \
    "v_mfma_f32_16x16x32_bf16 v[4:7], v[196:199], v[128:131], v[4:7]\n\t" \
    "ds_read_b64_tr_b16 v[210:211], v226 offset:4608\n\t" \
    "s_waitcnt lgkmcnt(11)\n\t" \
    "v_mfma_f32_16x16x32_bf16 v[8:11], v[200:203], v[128:131], v[8:11]\n\t" \
    "ds_read_b128 v[160:163], v225 offset:0\n\t" \
    "s_waitcnt lgkmcnt(10)\n\t" \
    "v_mfma_f32_16x16x32_bf16 v[12:15], v[204:207], v[128:131], v[12:15]\n\t" \
    "ds_read_b64_tr_b16 v[212:213], v227 offset:4096\n\t" \
    "s_waitcnt lgkmcnt(10)\n\t" \
    "v_mfma_f32_16x16x32_bf16 v[16:19], v[192:195], v[132:135], v[16:19]\n\t" \
    "ds_read_b64_tr_b16 v[214:215], v227 offset:4608\n\t" \
    "v_mfma_f32_16x16x32_bf16 v[20:23], v[196:199], v[132:135], v[20:23]\n\t" \
    "ds_read_b64_tr_b16 v[216:217], v228 offset:4096\n\t" \
    "v_mfma_f32_16x16x32_bf16 v[24:27], v[200:203], v[132:135], v[24:27]\n\t" \
    "ds_read_b64_tr_b16 v[218:219], v228 offset:4608\n\t" \
    "v_mfma_f32_16x16x32_bf16 v[28:31], v[204:207], v[132:135], v[28:31]\n\t" \
    "ds_read_b64_tr_b16 v[220:221], v229 offset:4096\n\t" \
    "s_waitcnt lgkmcnt(13)\n\t" \
    "v_mfma_f32_16x16x32_bf16 v[32:35], v[192:195], v[136:139], v[32:35]\n\t" \
    "ds_read_b64_tr_b16 v[222:223], v229 offset:4608\n\t" \
    "v_mfma_f32_16x16x32_bf16 v[36:39], v[196:199], v[136:139], v[36:39]\n\t" \
    "ds_read_b128 v[164:167], v225 offset:2048\n\t" \
    "v_mfma_f32_16x16x32_bf16 v[40:43], v[200:203], v[136:139], v[40:43]\n\t" \
    "ds_read_b128 v[168:171], v225 offset:4096\n\t" \
    "v_mfma_f32_16x16x32_bf16 v[44:47], v[204:207], v[136:139], v[44:47]\n\t" \
    "ds_read_b128 v[172:175], v225 offset:6144\n\t" \
    "s_waitcnt lgkmcnt(15)\n\t" \
    "v_mfma_f32_16x16x32_bf16 v[48:51], v[192:195], v[140:143], v[48:51]\n\t" \
    "ds_read_b128 v[176:179], v225 offset:8192\n\t" \
    "v_mfma_f32_16x16x32_bf16 v[52:55], v[196:199], v[140:143], v[52:55]\n\t" \
    "ds_read_b128 v[180:183], v225 offset:10240\n\t" \
    "v_mfma_f32_16x16x32_bf16 v[56:59], v[200:203], v[140:143], v[56:59]\n\t" \
    "ds_read_b128 v[184:187], v225 offset:12288\n\t" \
    "v_mfma_f32_16x16x32_bf16 v[60:63], v[204:207], v[140:143], v[60:63]\n\t" \
    "ds_read_b128 v[188:191], v225 offset:14336\n\t" \
    "v_mfma_f32_16x16x32_bf16 v[64:67], v[192:195], v[144:147], v[64:67]\n\t" \
    "s_add_u32 s51, s51, 49152\n\t" \
    "v_mfma_f32_16x16x32_bf16 v[68:71], v[196:199], v[144:147], v[68:71]\n\t" \
    "s_cmp_eq_u32 s51, 147456\n\t" \
    "v_mfma_f32_16x16x32_bf16 v[72:75], v[200:203], v[144:147], v[72:75]\n\t" \
    "s_cselect_b32 s50, s53, s54\n\t" \
    "v_mfma_f32_16x16x32_bf16 v[76:79], v[204:207], v[144:147], v[76:79]\n\t" \
    "s_cselect_b32 s51, 0, s51\n\t" \
    "s_waitcnt lgkmcnt(15)\n\t" \
    "v_mfma_f32_16x16x32_bf16 v[80:83], v[192:195], v[148:151], v[80:83]\n\t" \
    "v_add_u32 v224, s50, v224\n\t" \
    "v_mfma_f32_16x16x32_bf16 v[84:87], v[196:199], v[148:151], v[84:87]\n\t" \
    "v_add_u32 v225, s50, v225\n\t" \
    "v_mfma_f32_16x16x32_bf16 v[88:91], v[200:203], v[148:151], v[88:91]\n\t" \
    "v_add_u32 v226, s50, v226\n\t" \
    "v_mfma_f32_16x16x32_bf16 v[92:95], v[204:207], v[148:151], v[92:95]\n\t" \
    "v_add_u32 v227, s50, v227\n\t" \
    "v_mfma_f32_16x16x32_bf16 v[96:99], v[192:195], v[152:155], v[96:99]\n\t" \
    "v_add_u32 v228, s50, v228\n\t" \
    "v_mfma_f32_16x16x32_bf16 v[100:103], v[196:199], v[152:155], v[100:103]\n\t" \
    "v_add_u32 v229, s50, v229\n\t" \
    "v_mfma_f32_16x16x32_bf16 v[104:107], v[200:203], v[152:155], v[104:107]\n\t" \
    "v_mfma_f32_16x16x32_bf16 v[108:111], v[204:207], v[152:155], v[108:111]\n\t" \
    "v_mfma_f32_16x16x32_bf16 v[112:115], v[192:195], v[156:159], v[112:115]\n\t" \
    "v_mfma_f32_16x16x32_bf16 v[116:119], v[196:199], v[156:159], v[116:119]\n\t" \
    "v_mfma_f32_16x16x32_bf16 v[120:123], v[200:203], v[156:159], v[120:123]\n\t" \
    "v_mfma_f32_16x16x32_bf16 v[124:127], v[204:207], v[156:159], v[124:127]\n\t" \
    "s_waitcnt lgkmcnt(0)\n\t" \
    "s_barrier\n\t" \
    "v_mfma_f32_16x16x32_bf16 v[0:3], v[208:211], v[160:163], v[0:3]\n\t" \
    "ds_read_b64_tr_b16 v[192:193], v226 offset:0\n\t" \
    "v_mfma_f32_16x16x32_bf16 v[4:7], v[212:215], v[160:163], v[4:7]\n\t" \
    "ds_read_b64_tr_b16 v[194:195], v226 offset:512\n\t" \
    "v_mfma_f32_16x16x32_bf16 v[8:11], v[216:219], v[160:163], v[8:11]\n\t" \
    "ds_read_b128 v[128:131], v224 offset:0\n\t" \
    "v_mfma_f32_16x16x32_bf16 v[12:15], v[220:223], v[160:163], v[12:15]\n\t" \
    "ds_read_b64_tr_b16 v[196:197], v227 offset:0\n\t" \
    "v_mfma_f32_16x16x32_bf16 v[16:19], v[208:211], v[164:167], v[16:19]\n\t" \
    "ds_read_b64_tr_b16 v[198:199], v227 offset:512\n\t" \
    "v_mfma_f32_16x16x32_bf16 v[20:23], v[212:215], v[164:167], v[20:23]\n\t" \
    "ds_read_b64_tr_b16 v[200:201], v228 offset:0\n\t" \
    "v_mfma_f32_16x16x32_bf16 v[24:27], v[216:219], v[164:167], v[24:27]\n\t" \
    "ds_read_b64_tr_b16 v[202:203], v228 offset:512\n\t" \
    "v_mfma_f32_16x16x32_bf16 v[28:31], v[220:223], v[164:167], v[28:31]\n\t" \
    "ds_read_b64_tr_b16 v[204:205], v229 offset:0\n\t" \
    "v_mfma_f32_16x16x32_bf16 v[32:35], v[208:211], v[168:171], v[32:35]\n\t" \
    "ds_read_b64_tr_b16 v[206:207], v229 offset:512\n\t" \
    "v_mfma_f32_16x16x32_bf16 v[36:39], v[212:215], v[168:171], v[36:39]\n\t" \
    "ds_read_b128 v[132:135], v224 offset:2048\n\t" \
    "v_mfma_f32_16x16x32_bf16 v[40:43], v[216:219], v[168:171], v[40:43]\n\t" \
    "ds_read_b128 v[136:139], v224 offset:4096\n\t" \
    "v_mfma_f32_16x16x32_bf16 v[44:47], v[220:223], v[168:171], v[44:47]\n\t" \
    "ds_read_b128 v[140:143], v224 offset:6144\n\t" \
    "v_mfma_f32_16x16x32_bf16 v[48:51], v[208:211], v[172:175], v[48:51]\n\t" \
    "ds_read_b128 v[144:147], v224 offset:8192\n\t" \
    "v_mfma_f32_16x16x32_bf16 v[52:55], v[212:215], v[172:175], v[52:55]\n\t" \
    "ds_read_b128 v[148:151], v224 offset:10240\n\t" \
    "v_mfma_f32_16x16x32_bf16 v[56:59], v[216:219], v[172:175], v[56:59]\n\t" \
    "ds_read_b128 v[152:155], v224 offset:12288\n\t" \
    "v_mfma_f32_16x16x32_bf16 v[60:63], v[220:223], v[172:175], v[60:63]\n\t" \
    "ds_read_b128 v[156:159], v224 offset:14336\n\t" \
    "v_mfma_f32_16x16x32_bf16 v[64:67], v[208:211], v[176:179], v[64:67]\n\t" \
    "s_add_u32 s70, s76, 2097152\n\t" \
    "s_addc_u32 s71, s77, 0\n\t" \
    "global_load_dwordx4 v[252:255], %[vl16], s[70:71]\n\t" \
    "v_mfma_f32_16x16x32_bf16 v[68:71], v[212:215], v[176:179], v[68:71]\n\t" \
    "s_add_u32 s70, s72, 3072\n\t" \
    "s_addc_u32 s71, s73, 0\n\t" \
    "global_load_dwordx4 v[252:255], %[vl16], s[70:71]\n\t" \
    "v_mfma_f32_16x16x32_bf16 v[72:75], v[216:219], v[176:179], v[72:75]\n\t" \
    "v_mfma_f32_16x16x32_bf16 v[76:79], v[220:223], v[176:179], v[76:79]\n\t" \
    "v_mfma_f32_16x16x32_bf16 v[80:83], v[208:211], v[180:183], v[80:83]\n\t" \
    "v_mfma_f32_16x16x32_bf16 v[84:87], v[212:215], v[180:183], v[84:87]\n\t" \
    "v_mfma_f32_16x16x32_bf16 v[88:91], v[216:219], v[180:183], v[88:91]\n\t" \
    "v_mfma_f32_16x16x32_bf16 v[92:95], v[220:223], v[180:183], v[92:95]\n\t" \
    "v_mfma_f32_16x16x32_bf16 v[96:99], v[208:211], v[184:187], v[96:99]\n\t" \
    "v_mfma_f32_16x16x32_bf16 v[100:103], v[212:215], v[184:187], v[100:103]\n\t" \
    "v_mfma_f32_16x16x32_bf16 v[104:107], v[216:219], v[184:187], v[104:107]\n\t" \
    "v_mfma_f32_16x16x32_bf16 v[108:111], v[220:223], v[184:187], v[108:111]\n\t" \
    "v_mfma_f32_16x16x32_bf16 v[112:115], v[208:211], v[188:191], v[112:115]\n\t" \
    "v_mfma_f32_16x16x32_bf16 v[116:119], v[212:215], v[188:191], v[116:119]\n\t" \
    "v_mfma_f32_16x16x32_bf16 v[120:123], v[216:219], v[188:191], v[120:123]\n\t" \
    "v_mfma_f32_16x16x32_bf16 v[124:127], v[220:223], v[188:191], v[124:127]\n\t" \
    "s_waitcnt lgkmcnt(13)\n\t" \
    "v_mfma_f32_16x16x32_bf16 v[0:3], v[192:195], v[128:131], v[0:3]\n\t" \
    "ds_read_b64_tr_b16 v[208:209], v226 offset:4096\n\t" \
    "s_waitcnt lgkmcnt(12)\n\t" \
    "v_mfma_f32_16x16x32_bf16 v[4:7], v[196:199], v[128:131], v[4:7]\n\t" \
    "ds_read_b64_tr_b16 v[210:211], v226 offset:4608\n\t" \
    "s_waitcnt lgkmcnt(11)\n\t" \
    "v_mfma_f32_16x16x32_bf16 v[8:11], v[200:203], v[128:131], v[8:11]\n\t" \
    "ds_read_b128 v[160:163], v225 offset:0\n\t" \
    "s_waitcnt lgkmcnt(10)\n\t" \
    "v_mfma_f32_16x16x32_bf16 v[12:15], v[204:207], v[128:131], v[12:15]\n\t" \
    "ds_read_b64_tr_b16 v[212:213], v227 offset:4096\n\t" \
    "s_waitcnt lgkmcnt(10)\n\t" \
    "v_mfma_f32_16x16x32_bf16 v[16:19], v[192:195], v[132:135], v[16:19]\n\t" \
    "ds_read_b64_tr_b16 v[214:215], v227 offset:4608\n\t" \
    "v_mfma_f32_16x16x32_bf16 v[20:23], v[196:199], v[132:135], v[20:23]\n\t" \
    "ds_read_b64_tr_b16 v[216:217], v228 offset:4096\n\t" \
    "v_mfma_f32_16x16x32_bf16 v[24:27], v[200:203], v[132:135], v[24:27]\n\t" \
    "ds_read_b64_tr_b16 v[218:219], v228 offset:4608\n\t" \
    "v_mfma_f32_16x16x32_bf16 v[28:31], v[204:207], v[132:135], v[28:31]\n\t" \
    "ds_read_b64_tr_b16 v[220:221], v229 offset:4096\n\t" \
    "s_waitcnt lgkmcnt(13)\n\t" \
    "v_mfma_f32_16x16x32_bf16 v[32:35], v[192:195], v[136:139], v[32:35]\n\t" \
    "ds_read_b64_tr_b16 v[222:223], v229 offset:4608\n\t" \
    "v_mfma_f32_16x16x32_bf16 v[36:39], v[196:199], v[136:139], v[36:39]\n\t" \
    "ds_read_b128 v[164:167], v225 offset:2048\n\t" \
    "v_mfma_f32_16x16x32_bf16 v[40:43], v[200:203], v[136:139], v[40:43]\n\t" \
    "ds_read_b128 v[168:171], v225 offset:4096\n\t" \
    "v_mfma_f32_16x16x32_bf16 v[44:47], v[204:207], v[136:139], v[44:47]\n\t" \
    "ds_read_b128 v[172:175], v225 offset:6144\n\t" \
    "s_waitcnt lgkmcnt(15)\n\t" \
    "v_mfma_f32_16x16x32_bf16 v[48:51], v[192:195], v[140:143], v[48:51]\n\t" \
    "ds_read_b128 v[176:179], v225 offset:8192\n\t" \
    "v_mfma_f32_16x16x32_bf16 v[52:55], v[196:199], v[140:143], v[52:55]\n\t" \
    "ds_read_b128 v[180:183], v225 offset:10240\n\t" \
    "v_mfma_f32_16x16x32_bf16 v[56:59], v[200:203], v[140:143], v[56:59]\n\t" \
    "ds_read_b128 v[184:187], v225 offset:12288\n\t" \
    "v_mfma_f32_16x16x32_bf16 v[60:63], v[204:207], v[140:143], v[60:63]\n\t" \
    "ds_read_b128 v[188:191], v225 offset:14336\n\t" \
    "v_mfma_f32_16x16x32_bf16 v[64:67], v[192:195], v[144:147], v[64:67]\n\t" \
    "s_add_u32 s51, s51, 49152\n\t" \
    "v_mfma_f32_16x16x32_bf16 v[68:71], v[196:199], v[144:147], v[68:71]\n\t" \
    "s_cmp_eq_u32 s51, 147456\n\t" \
    "v_mfma_f32_16x16x32_bf16 v[72:75], v[200:203], v[144:147], v[72:75]\n\t" \
    "s_cselect_b32 s50, s53, s54\n\t" \
    "v_mfma_f32_16x16x32_bf16 v[76:79], v[204:207], v[144:147], v[76:79]\n\t" \
    "s_cselect_b32 s51, 0, s51\n\t" \
    "s_waitcnt lgkmcnt(15)\n\t" \
    "v_mfma_f32_16x16x32_bf16 v[80:83], v[192:195], v[148:151], v[80:83]\n\t" \
    "v_add_u32 v224, s50, v224\n\t" \
    "v_mfma_f32_16x16x32_bf16 v[84:87], v[196:199], v[148:151], v[84:87]\n\t" \
    "v_add_u32 v225, s50, v225\n\t" \
    "v_mfma_f32_16x16x32_bf16 v[88:91], v[200:203], v[148:151], v[88:91]\n\t" \
    "v_add_u32 v226, s50, v226\n\t" \
    "v_mfma_f32_16x16x32_bf16 v[92:95], v[204:207], v[148:151], v[92:95]\n\t" \
    "v_add_u32 v227, s50, v227\n\t" \
    "v_mfma_f32_16x16x32_bf16 v[96:99], v[192:195], v[152:155], v[96:99]\n\t" \
    "v_add_u32 v228, s50, v228\n\t" \
    "v_mfma_f32_16x16x32_bf16 v[100:103], v[196:199], v[152:155], v[100:103]\n\t" \
    "v_add_u32 v229, s50, v229\n\t" \
    "v_mfma_f32_16x16x32_bf16 v[104:107], v[200:203], v[152:155], v[104:107]\n\t" \
    "v_mfma_f32_16x16x32_bf16 v[108:111], v[204:207], v[152:155], v[108:111]\n\t" \
    "v_mfma_f32_16x16x32_bf16 v[112:115], v[192:195], v[156:159], v[112:115]\n\t" \
    "v_mfma_f32_16x16x32_bf16 v[116:119], v[196:199], v[156:159], v[116:119]\n\t" \
    "v_mfma_f32_16x16x32_bf16 v[120:123], v[200:203], v[156:159], v[120:123]\n\t" \
    "v_mfma_f32_16x16x32_bf16 v[124:127], v[204:207], v[156:159], v[124:127]\n\t" \
    "s_waitcnt lgkmcnt(0)\n\t" \
    "s_barrier\n\t" \
    "v_mfma_f32_16x16x32_bf16 v[0:3], v[208:211], v[160:163], v[0:3]\n\t" \
    "ds_read_b64_tr_b16 v[192:193], v226 offset:0\n\t" \
    "v_mfma_f32_16x16x32_bf16 v[4:7], v[212:215], v[160:163], v[4:7]\n\t" \
    "ds_read_b64_tr_b16 v[194:195], v226 offset:512\n\t" \
    "v_mfma_f32_16x16x32_bf16 v[8:11], v[216:219], v[160:163], v[8:11]\n\t" \
    "ds_read_b128 v[128:131], v224 offset:0\n\t" \
    "v_mfma_f32_16x16x32_bf16 v[12:15], v[220:223], v[160:163], v[12:15]\n\t" \
    "ds_read_b64_tr_b16 v[196:197], v227 offset:0\n\t" \
    "v_mfma_f32_16x16x32_bf16 v[16:19], v[208:211], v[164:167], v[16:19]\n\t" \
    "ds_read_b64_tr_b16 v[198:199], v227 offset:512\n\t" \
    "v_mfma_f32_16x16x32_bf16 v[20:23], v[212:215], v[164:167], v[20:23]\n\t" \
    "ds_read_b64_tr_b16 v[200:201], v228 offset:0\n\t" \
    "v_mfma_f32_16x16x32_bf16 v[24:27], v[216:219], v[164:167], v[24:27]\n\t" \
    "ds_read_b64_tr_b16 v[202:203], v228 offset:512\n\t" \
    "v_mfma_f32_16x16x32_bf16 v[28:31], v[220:223], v[164:167], v[28:31]\n\t" \
    "ds_read_b64_tr_b16 v[204:205], v229 offset:0\n\t" \
    "v_mfma_f32_16x16x32_bf16 v[32:35], v[208:211], v[168:171], v[32:35]\n\t" \
    "ds_read_b64_tr_b16 v[206:207], v229 offset:512\n\t" \
    "v_mfma_f32_16x16x32_bf16 v[36:39], v[212:215], v[168:171], v[36:39]\n\t" \
    "ds_read_b128 v[132:135], v224 offset:2048\n\t" \
    "v_mfma_f32_16x16x32_bf16 v[40:43], v[216:219], v[168:171], v[40:43]\n\t" \
    "ds_read_b128 v[136:139], v224 offset:4096\n\t" \
    "v_mfma_f32_16x16x32_bf16 v[44:47], v[220:223], v[168:171], v[44:47]\n\t" \
    "ds_read_b128 v[140:143], v224 offset:6144\n\t" \
    "v_mfma_f32_16x16x32_bf16 v[48:51], v[208:211], v[172:175], v[48:51]\n\t" \
    "ds_read_b128 v[144:147], v224 offset:8192\n\t" \
    "v_mfma_f32_16x16x32_bf16 v[52:55], v[212:215], v[172:175], v[52:55]\n\t" \
    "ds_read_b128 v[148:151], v224 offset:10240\n\t" \
    "v_mfma_f32_16x16x32_bf16 v[56:59], v[216:219], v[172:175], v[56:59]\n\t" \
    "ds_read_b128 v[152:155], v224 offset:12288\n\t" \
    "v_mfma_f32_16x16x32_bf16 v[60:63], v[220:223], v[172:175], v[60:63]\n\t" \
    "ds_read_b128 v[156:159], v224 offset:14336\n\t" \
    "v_mfma_f32_16x16x32_bf16 v[64:67], v[208:211], v[176:179], v[64:67]\n\t" \
    "s_add_u32 s70, s76, 2621440\n\t" \
    "s_addc_u32 s71, s77, 0\n\t" \
    "global_load_dwordx4 v[252:255], %[vl16], s[70:71]\n\t" \
    "v_mfma_f32_16x16x32_bf16 v[68:71], v[212:215], v[176:179], v[68:71]\n\t" \
    "s_add_u32 s70, s72, 2100224\n\t" \
    "s_addc_u32 s71, s73, 0\n\t" \
    "global_load_dwordx4 v[252:255], %[vl16], s[70:71]\n\t" \
    "v_mfma_f32_16x16x32_bf16 v[72:75], v[216:219], v[176:179], v[72:75]\n\t" \
    "v_mfma_f32_16x16x32_bf16 v[76:79], v[220:223], v[176:179], v[76:79]\n\t" \
    "v_mfma_f32_16x16x32_bf16 v[80:83], v[208:211], v[180:183], v[80:83]\n\t" \
    "v_mfma_f32_16x16x32_bf16 v[84:87], v[212:215], v[180:183], v[84:87]\n\t" \
    "v_mfma_f32_16x16x32_bf16 v[88:91], v[216:219], v[180:183], v[88:91]\n\t" \
    "v_mfma_f32_16x16x32_bf16 v[92:95], v[220:223], v[180:183], v[92:95]\n\t" \
    "v_mfma_f32_16x16x32_bf16 v[96:99], v[208:211], v[184:187], v[96:99]\n\t" \
    "v_mfma_f32_16x16x32_bf16 v[100:103], v[212:215], v[184:187], v[100:103]\n\t" \
    "v_mfma_f32_16x16x32_bf16 v[104:107], v[216:219], v[184:187], v[104:107]\n\t" \
    "v_mfma_f32_16x16x32_bf16 v[108:111], v[220:223], v[184:187], v[108:111]\n\t" \
    "v_mfma_f32_16x16x32_bf16 v[112:115], v[208:211], v[188:191], v[112:115]\n\t" \
    "v_mfma_f32_16x16x32_bf16 v[116:119], v[212:215], v[188:191], v[116:119]\n\t" \
    "v_mfma_f32_16x16x32_bf16 v[120:123], v[216:219], v[188:191], v[120:123]\n\t" \
    "v_mfma_f32_16x16x32_bf16 v[124:127], v[220:223], v[188:191], v[124:127]\n\t" \
    "s_waitcnt lgkmcnt(13)\n\t" \
    "v_mfma_f32_16x16x32_bf16 v[0:3], v[192:195], v[128:131], v[0:3]\n\t" \
    "ds_read_b64_tr_b16 v[208:209], v226 offset:4096\n\t" \
    "s_waitcnt lgkmcnt(12)\n\t" \
    "v_mfma_f32_16x16x32_bf16 v[4:7], v[196:199], v[128:131], v[4:7]\n\t" \
    "ds_read_b64_tr_b16 v[210:211], v226 offset:4608\n\t" \
    "s_waitcnt lgkmcnt(11)\n\t" \
    "v_mfma_f32_16x16x32_bf16 v[8:11], v[200:203], v[128:131], v[8:11]\n\t" \
    "ds_read_b128 v[160:163], v225 offset:0\n\t" \
    "s_waitcnt lgkmcnt(10)\n\t" \
    "v_mfma_f32_16x16x32_bf16 v[12:15], v[204:207], v[128:131], v[12:15]\n\t" \
    "ds_read_b64_tr_b16 v[212:213], v227 offset:4096\n\t" \
    "s_waitcnt lgkmcnt(10)\n\t" \
    "v_mfma_f32_16x16x32_bf16 v[16:19], v[192:195], v[132:135], v[16:19]\n\t" \
    "ds_read_b64_tr_b16 v[214:215], v227 offset:4608\n\t" \
    "v_mfma_f32_16x16x32_bf16 v[20:23], v[196:199], v[132:135], v[20:23]\n\t" \
    "ds_read_b64_tr_b16 v[216:217], v228 offset:4096\n\t" \
    "v_mfma_f32_16x16x32_bf16 v[24:27], v[200:203], v[132:135], v[24:27]\n\t" \
    "ds_read_b64_tr_b16 v[218:219], v228 offset:4608\n\t" \
    "v_mfma_f32_16x16x32_bf16 v[28:31], v[204:207], v[132:135], v[28:31]\n\t" \
    "ds_read_b64_tr_b16 v[220:221], v229 offset:4096\n\t" \
    "s_waitcnt lgkmcnt(13)\n\t" \
    "v_mfma_f32_16x16x32_bf16 v[32:35], v[192:195], v[136:139], v[32:35]\n\t" \
    "ds_read_b64_tr_b16 v[222:223], v229 offset:4608\n\t" \
    "v_mfma_f32_16x16x32_bf16 v[36:39], v[196:199], v[136:139], v[36:39]\n\t" \
    "ds_read_b128 v[164:167], v225 offset:2048\n\t" \
    "v_mfma_f32_16x16x32_bf16 v[40:43], v[200:203], v[136:139], v[40:43]\n\t" \
    "ds_read_b128 v[168:171], v225 offset:4096\n\t" \
    "v_mfma_f32_16x16x32_bf16 v[44:47], v[204:207], v[136:139], v[44:47]\n\t" \
    "ds_read_b128 v[172:175], v225 offset:6144\n\t" \
    "s_waitcnt lgkmcnt(15)\n\t" \
    "v_mfma_f32_16x16x32_bf16 v[48:51], v[192:195], v[140:143], v[48:51]\n\t" \
    "ds_read_b128 v[176:179], v225 offset:8192\n\t" \
    "v_mfma_f32_16x16x32_bf16 v[52:55], v[196:199], v[140:143], v[52:55]\n\t" \
    "ds_read_b128 v[180:183], v225 offset:10240\n\t" \
    "v_mfma_f32_16x16x32_bf16 v[56:59], v[200:203], v[140:143], v[56:59]\n\t" \
    "ds_read_b128 v[184:187], v225 offset:12288\n\t" \
    "v_mfma_f32_16x16x32_bf16 v[60:63], v[204:207], v[140:143], v[60:63]\n\t" \
    "ds_read_b128 v[188:191], v225 offset:14336\n\t" \
    "v_mfma_f32_16x16x32_bf16 v[64:67], v[192:195], v[144:147], v[64:67]\n\t" \
    "s_add_u32 s51, s51, 49152\n\t" \
    "v_mfma_f32_16x16x32_bf16 v[68:71], v[196:199], v[144:147], v[68:71]\n\t" \
    "s_cmp_eq_u32 s51, 147456\n\t" \
    "v_mfma_f32_16x16x32_bf16 v[72:75], v[200:203], v[144:147], v[72:75]\n\t" \
    "s_cselect_b32 s50, s53, s54\n\t" \
    "v_mfma_f32_16x16x32_bf16 v[76:79], v[204:207], v[144:147], v[76:79]\n\t" \
    "s_cselect_b32 s51, 0, s51\n\t" \
    "s_waitcnt lgkmcnt(15)\n\t" \
    "v_mfma_f32_16x16x32_bf16 v[80:83], v[192:195], v[148:151], v[80:83]\n\t" \
    "v_add_u32 v224, s50, v224\n\t" \
    "v_mfma_f32_16x16x32_bf16 v[84:87], v[196:199], v[148:151], v[84:87]\n\t" \
    "v_add_u32 v225, s50, v225\n\t" \
    "v_mfma_f32_16x16x32_bf16 v[88:91], v[200:203], v[148:151], v[88:91]\n\t" \
    "v_add_u32 v226, s50, v226\n\t" \
    "v_mfma_f32_16x16x32_bf16 v[92:95], v[204:207], v[148:151], v[92:95]\n\t" \
    "v_add_u32 v227, s50, v227\n\t" \
    "v_mfma_f32_16x16x32_bf16 v[96:99], v[192:195], v[152:155], v[96:99]\n\t" \
    "v_add_u32 v228, s50, v228\n\t" \
    "v_mfma_f32_16x16x32_bf16 v[100:103], v[196:199], v[152:155], v[100:103]\n\t" \
    "v_add_u32 v229, s50, v229\n\t" \
    "v_mfma_f32_16x16x32_bf16 v[104:107], v[200:203], v[152:155], v[104:107]\n\t" \
    "v_mfma_f32_16x16x32_bf16 v[108:111], v[204:207], v[152:155], v[108:111]\n\t" \
    "v_mfma_f32_16x16x32_bf16 v[112:115], v[192:195], v[156:159], v[112:115]\n\t" \
    "v_mfma_f32_16x16x32_bf16 v[116:119], v[196:199], v[156:159], v[116:119]\n\t" \
    "v_mfma_f32_16x16x32_bf16 v[120:123], v[200:203], v[156:159], v[120:123]\n\t" \
    "v_mfma_f32_16x16x32_bf16 v[124:127], v[204:207], v[156:159], v[124:127]\n\t" \
    "s_waitcnt lgkmcnt(0)\n\t" \
    "s_barrier\n\t" \
    "v_mfma_f32_16x16x32_bf16 v[0:3], v[208:211], v[160:163], v[0:3]\n\t" \
    "ds_read_b64_tr_b16 v[192:193], v226 offset:0\n\t" \
    "v_mfma_f32_16x16x32_bf16 v[4:7], v[212:215], v[160:163], v[4:7]\n\t" \
    "ds_read_b64_tr_b16 v[194:195], v226 offset:512\n\t" \
    "v_mfma_f32_16x16x32_bf16 v[8:11], v[216:219], v[160:163], v[8:11]\n\t" \
    "ds_read_b128 v[128:131], v224 offset:0\n\t" \
    "v_mfma_f32_16x16x32_bf16 v[12:15], v[220:223], v[160:163], v[12:15]\n\t" \
    "ds_read_b64_tr_b16 v[196:197], v227 offset:0\n\t" \
    "v_mfma_f32_16x16x32_bf16 v[16:19], v[208:211], v[164:167], v[16:19]\n\t" \
    "ds_read_b64_tr_b16 v[198:199], v227 offset:512\n\t" \
    "v_mfma_f32_16x16x32_bf16 v[20:23], v[212:215], v[164:167], v[20:23]\n\t" \
    "ds_read_b64_tr_b16 v[200:201], v228 offset:0\n\t" \
    "v_mfma_f32_16x16x32_bf16 v[24:27], v[216:219], v[164:167], v[24:27]\n\t" \
    "ds_read_b64_tr_b16 v[202:203], v228 offset:512\n\t" \
    "v_mfma_f32_16x16x32_bf16 v[28:31], v[220:223], v[164:167], v[28:31]\n\t" \
    "ds_read_b64_tr_b16 v[204:205], v229 offset:0\n\t" \
    "v_mfma_f32_16x16x32_bf16 v[32:35], v[208:211], v[168:171], v[32:35]\n\t" \
    "ds_read_b64_tr_b16 v[206:207], v229 offset:512\n\t" \
    "v_mfma_f32_16x16x32_bf16 v[36:39], v[212:215], v[168:171], v[36:39]\n\t" \
    "ds_read_b128 v[132:135], v224 offset:2048\n\t" \
    "v_mfma_f32_16x16x32_bf16 v[40:43], v[216:219], v[168:171], v[40:43]\n\t" \
    "ds_read_b128 v[136:139], v224 offset:4096\n\t" \
    "v_mfma_f32_16x16x32_bf16 v[44:47], v[220:223], v[168:171], v[44:47]\n\t" \
    "ds_read_b128 v[140:143], v224 offset:6144\n\t" \
    "v_mfma_f32_16x16x32_bf16 v[48:51], v[208:211], v[172:175], v[48:51]\n\t" \
    "ds_read_b128 v[144:147], v224 offset:8192\n\t" \
    "v_mfma_f32_16x16x32_bf16 v[52:55], v[212:215], v[172:175], v[52:55]\n\t" \
    "ds_read_b128 v[148:151], v224 offset:10240\n\t" \
    "v_mfma_f32_16x16x32_bf16 v[56:59], v[216:219], v[172:175], v[56:59]\n\t" \
    "ds_read_b128 v[152:155], v224 offset:12288\n\t" \
    "v_mfma_f32_16x16x32_bf16 v[60:63], v[220:223], v[172:175], v[60:63]\n\t" \
    "ds_read_b128 v[156:159], v224 offset:14336\n\t" \
    "v_mfma_f32_16x16x32_bf16 v[64:67], v[208:211], v[176:179], v[64:67]\n\t" \
    "s_add_u32 s70, s72, 4197376\n\t" \
    "s_addc_u32 s71, s73, 0\n\t" \
    "global_load_dwordx4 v[252:255], %[vl16], s[70:71]\n\t" \
    "v_mfma_f32_16x16x32_bf16 v[68:71], v[212:215], v[176:179], v[68:71]\n\t" \
    "v_mfma_f32_16x16x32_bf16 v[72:75], v[216:219], v[176:179], v[72:75]\n\t" \
    "v_mfma_f32_16x16x32_bf16 v[76:79], v[220:223], v[176:179], v[76:79]\n\t" \
    "v_mfma_f32_16x16x32_bf16 v[80:83], v[208:211], v[180:183], v[80:83]\n\t" \
    "v_mfma_f32_16x16x32_bf16 v[84:87], v[212:215], v[180:183], v[84:87]\n\t" \
    "v_mfma_f32_16x16x32_bf16 v[88:91], v[216:219], v[180:183], v[88:91]\n\t" \
    "v_mfma_f32_16x16x32_bf16 v[92:95], v[220:223], v[180:183], v[92:95]\n\t" \
    "v_mfma_f32_16x16x32_bf16 v[96:99], v[208:211], v[184:187], v[96:99]\n\t" \
    "v_mfma_f32_16x16x32_bf16 v[100:103], v[212:215], v[184:187], v[100:103]\n\t" \
    "v_mfma_f32_16x16x32_bf16 v[104:107], v[216:219], v[184:187], v[104:107]\n\t" \
    "v_mfma_f32_16x16x32_bf16 v[108:111], v[220:223], v[184:187], v[108:111]\n\t" \
    "v_mfma_f32_16x16x32_bf16 v[112:115], v[208:211], v[188:191], v[112:115]\n\t" \
    "v_mfma_f32_16x16x32_bf16 v[116:119], v[212:215], v[188:191], v[116:119]\n\t" \
    "v_mfma_f32_16x16x32_bf16 v[120:123], v[216:219], v[188:191], v[120:123]\n\t" \
    "v_mfma_f32_16x16x32_bf16 v[124:127], v[220:223], v[188:191], v[124:127]\n\t" \
    "s_waitcnt lgkmcnt(13)\n\t" \
    "v_mfma_f32_16x16x32_bf16 v[0:3], v[192:195], v[128:131], v[0:3]\n\t" \
    "ds_read_b64_tr_b16 v[208:209], v226 offset:4096\n\t" \
    "s_waitcnt lgkmcnt(12)\n\t" \
    "v_mfma_f32_16x16x32_bf16 v[4:7], v[196:199], v[128:131], v[4:7]\n\t" \
    "ds_read_b64_tr_b16 v[210:211], v226 offset:4608\n\t" \
    "s_waitcnt lgkmcnt(11)\n\t" \
    "v_mfma_f32_16x16x32_bf16 v[8:11], v[200:203], v[128:131], v[8:11]\n\t" \
    "ds_read_b128 v[160:163], v225 offset:0\n\t" \
    "s_waitcnt lgkmcnt(10)\n\t" \
    "v_mfma_f32_16x16x32_bf16 v[12:15], v[204:207], v[128:131], v[12:15]\n\t" \
    "ds_read_b64_tr_b16 v[212:213], v227 offset:4096\n\t" \
    "s_waitcnt lgkmcnt(10)\n\t" \
    "v_mfma_f32_16x16x32_bf16 v[16:19], v[192:195], v[132:135], v[16:19]\n\t" \
    "ds_read_b64_tr_b16 v[214:215], v227 offset:4608\n\t" \
    "v_mfma_f32_16x16x32_bf16 v[20:23], v[196:199], v[132:135], v[20:23]\n\t" \
    "ds_read_b64_tr_b16 v[216:217], v228 offset:4096\n\t" \
    "v_mfma_f32_16x16x32_bf16 v[24:27], v[200:203], v[132:135], v[24:27]\n\t" \
    "ds_read_b64_tr_b16 v[218:219], v228 offset:4608\n\t" \
    "v_mfma_f32_16x16x32_bf16 v[28:31], v[204:207], v[132:135], v[28:31]\n\t" \
    "ds_read_b64_tr_b16 v[220:221], v229 offset:4096\n\t" \
    "s_waitcnt lgkmcnt(13)\n\t" \
    "v_mfma_f32_16x16x32_bf16 v[32:35], v[192:195], v[136:139], v[32:35]\n\t" \
    "ds_read_b64_tr_b16 v[222:223], v229 offset:4608\n\t" \
    "v_mfma_f32_16x16x32_bf16 v[36:39], v[196:199], v[136:139], v[36:39]\n\t" \
    "ds_read_b128 v[164:167], v225 offset:2048\n\t" \
    "v_mfma_f32_16x16x32_bf16 v[40:43], v[200:203], v[136:139], v[40:43]\n\t" \
    "ds_read_b128 v[168:171], v225 offset:4096\n\t" \
    "v_mfma_f32_16x16x32_bf16 v[44:47], v[204:207], v[136:139], v[44:47]\n\t" \
    "ds_read_b128 v[172:175], v225 offset:6144\n\t" \
    "s_waitcnt lgkmcnt(15)\n\t" \
    "v_mfma_f32_16x16x32_bf16 v[48:51], v[192:195], v[140:143], v[48:51]\n\t" \
    "ds_read_b128 v[176:179], v225 offset:8192\n\t" \
    "v_mfma_f32_16x16x32_bf16 v[52:55], v[196:199], v[140:143], v[52:55]\n\t" \
    "ds_read_b128 v[180:183], v225 offset:10240\n\t" \
    "v_mfma_f32_16x16x32_bf16 v[56:59], v[200:203], v[140:143], v[56:59]\n\t" \
    "ds_read_b128 v[184:187], v225 offset:12288\n\t" \
    "v_mfma_f32_16x16x32_bf16 v[60:63], v[204:207], v[140:143], v[60:63]\n\t" \
    "ds_read_b128 v[188:191], v225 offset:14336\n\t" \
    "v_mfma_f32_16x16x32_bf16 v[64:67], v[192:195], v[144:147], v[64:67]\n\t" \
    "s_add_u32 s51, s51, 49152\n\t" \
    "v_mfma_f32_16x16x32_bf16 v[68:71], v[196:199], v[144:147], v[68:71]\n\t" \
    "s_cmp_eq_u32 s51, 147456\n\t" \
    "v_mfma_f32_16x16x32_bf16 v[72:75], v[200:203], v[144:147], v[72:75]\n\t" \
    "s_cselect_b32 s50, s53, s54\n\t" \
    "v_mfma_f32_16x16x32_bf16 v[76:79], v[204:207], v[144:147], v[76:79]\n\t" \
    "s_cselect_b32 s51, 0, s51\n\t" \
    "s_waitcnt lgkmcnt(15)\n\t" \
    "v_mfma_f32_16x16x32_bf16 v[80:83], v[192:195], v[148:151], v[80:83]\n\t" \
    "v_add_u32 v224, s50, v224\n\t" \
    "v_mfma_f32_16x16x32_bf16 v[84:87], v[196:199], v[148:151], v[84:87]\n\t" \
    "v_add_u32 v225, s50, v225\n\t" \
    "v_mfma_f32_16x16x32_bf16 v[88:91], v[200:203], v[148:151], v[88:91]\n\t" \
    "v_add_u32 v226, s50, v226\n\t" \
    "v_mfma_f32_16x16x32_bf16 v[92:95], v[204:207], v[148:151], v[92:95]\n\t" \
    "v_add_u32 v227, s50, v227\n\t" \
    "v_mfma_f32_16x16x32_bf16 v[96:99], v[192:195], v[152:155], v[96:99]\n\t" \
    "v_add_u32 v228, s50, v228\n\t" \
    "v_mfma_f32_16x16x32_bf16 v[100:103], v[196:199], v[152:155], v[100:103]\n\t" \
    "v_add_u32 v229, s50, v229\n\t" \
    "v_mfma_f32_16x16x32_bf16 v[104:107], v[200:203], v[152:155], v[104:107]\n\t" \
    "v_mfma_f32_16x16x32_bf16 v[108:111], v[204:207], v[152:155], v[108:111]\n\t" \
    "v_mfma_f32_16x16x32_bf16 v[112:115], v[192:195], v[156:159], v[112:115]\n\t" \
    "v_mfma_f32_16x16x32_bf16 v[116:119], v[196:199], v[156:159], v[116:119]\n\t" \
    "v_mfma_f32_16x16x32_bf16 v[120:123], v[200:203], v[156:159], v[120:123]\n\t" \
    "v_mfma_f32_16x16x32_bf16 v[124:127], v[204:207], v[156:159], v[124:127]\n\t" \
    "s_waitcnt lgkmcnt(0)\n\t" \
    "s_barrier\n\t" \
    "v_mfma_f32_16x16x32_bf16 v[0:3], v[208:211], v[160:163], v[0:3]\n\t" \
    "ds_read_b64_tr_b16 v[192:193], v226 offset:0\n\t" \
    "v_mfma_f32_16x16x32_bf16 v[4:7], v[212:215], v[160:163], v[4:7]\n\t" \
    "ds_read_b64_tr_b16 v[194:195], v226 offset:512\n\t" \
    "v_mfma_f32_16x16x32_bf16 v[8:11], v[216:219], v[160:163], v[8:11]\n\t" \
    "ds_read_b128 v[128:131], v224 offset:0\n\t" \
    "v_mfma_f32_16x16x32_bf16 v[12:15], v[220:223], v[160:163], v[12:15]\n\t" \
    "ds_read_b64_tr_b16 v[196:197], v227 offset:0\n\t" \
    "v_mfma_f32_16x16x32_bf16 v[16:19], v[208:211], v[164:167], v[16:19]\n\t" \
    "ds_read_b64_tr_b16 v[198:199], v227 offset:512\n\t" \
    "v_mfma_f32_16x16x32_bf16 v[20:23], v[212:215], v[164:167], v[20:23]\n\t" \
    "ds_read_b64_tr_b16 v[200:201], v228 offset:0\n\t" \
    "v_mfma_f32_16x16x32_bf16 v[24:27], v[216:219], v[164:167], v[24:27]\n\t" \
    "ds_read_b64_tr_b16 v[202:203], v228 offset:512\n\t" \
    "v_mfma_f32_16x16x32_bf16 v[28:31], v[220:223], v[164:167], v[28:31]\n\t" \
    "ds_read_b64_tr_b16 v[204:205], v229 offset:0\n\t" \
    "v_mfma_f32_16x16x32_bf16 v[32:35], v[208:211], v[168:171], v[32:35]\n\t" \
    "ds_read_b64_tr_b16 v[206:207], v229 offset:512\n\t" \
    "v_mfma_f32_16x16x32_bf16 v[36:39], v[212:215], v[168:171], v[36:39]\n\t" \
    "ds_read_b128 v[132:135], v224 offset:2048\n\t" \
    "v_mfma_f32_16x16x32_bf16 v[40:43], v[216:219], v[168:171], v[40:43]\n\t" \
    "ds_read_b128 v[136:139], v224 offset:4096\n\t" \
    "v_mfma_f32_16x16x32_bf16 v[44:47], v[220:223], v[168:171], v[44:47]\n\t" \
    "ds_read_b128 v[140:143], v224 offset:6144\n\t" \
    "v_mfma_f32_16x16x32_bf16 v[48:51], v[208:211], v[172:175], v[48:51]\n\t" \
    "ds_read_b128 v[144:147], v224 offset:8192\n\t" \
    "v_mfma_f32_16x16x32_bf16 v[52:55], v[212:215], v[172:175], v[52:55]\n\t" \
    "ds_read_b128 v[148:151], v224 offset:10240\n\t" \
    "v_mfma_f32_16x16x32_bf16 v[56:59], v[216:219], v[172:175], v[56:59]\n\t" \
    "ds_read_b128 v[152:155], v224 offset:12288\n\t" \
    "v_mfma_f32_16x16x32_bf16 v[60:63], v[220:223], v[172:175], v[60:63]\n\t" \
    "ds_read_b128 v[156:159], v224 offset:14336\n\t" \
    "v_mfma_f32_16x16x32_bf16 v[64:67], v[208:211], v[176:179], v[64:67]\n\t" \
    "s_add_u32 s70, s72, 6294528\n\t" \
    "s_addc_u32 s71, s73, 0\n\t" \
    "global_load_dwordx4 v[252:255], %[vl16], s[70:71]\n\t" \
    "v_mfma_f32_16x16x32_bf16 v[68:71], v[212:215], v[176:179], v[68:71]\n\t" \
    "v_mfma_f32_16x16x32_bf16 v[72:75], v[216:219], v[176:179], v[72:75]\n\t" \
    "v_mfma_f32_16x16x32_bf16 v[76:79], v[220:223], v[176:179], v[76:79]\n\t" \
    "v_mfma_f32_16x16x32_bf16 v[80:83], v[208:211], v[180:183], v[80:83]\n\t" \
    "v_mfma_f32_16x16x32_bf16 v[84:87], v[212:215], v[180:183], v[84:87]\n\t" \
    "v_mfma_f32_16x16x32_bf16 v[88:91], v[216:219], v[180:183], v[88:91]\n\t" \
    "v_mfma_f32_16x16x32_bf16 v[92:95], v[220:223], v[180:183], v[92:95]\n\t" \
    "v_mfma_f32_16x16x32_bf16 v[96:99], v[208:211], v[184:187], v[96:99]\n\t" \
    "v_mfma_f32_16x16x32_bf16 v[100:103], v[212:215], v[184:187], v[100:103]\n\t" \
    "v_mfma_f32_16x16x32_bf16 v[104:107], v[216:219], v[184:187], v[104:107]\n\t" \
    "v_mfma_f32_16x16x32_bf16 v[108:111], v[220:223], v[184:187], v[108:111]\n\t" \
    "v_mfma_f32_16x16x32_bf16 v[112:115], v[208:211], v[188:191], v[112:115]\n\t" \
    "v_mfma_f32_16x16x32_bf16 v[116:119], v[212:215], v[188:191], v[116:119]\n\t" \
    "v_mfma_f32_16x16x32_bf16 v[120:123], v[216:219], v[188:191], v[120:123]\n\t" \
    "v_mfma_f32_16x16x32_bf16 v[124:127], v[220:223], v[188:191], v[124:127]\n\t" \
    "s_waitcnt lgkmcnt(13)\n\t" \
    "v_mfma_f32_16x16x32_bf16 v[0:3], v[192:195], v[128:131], v[0:3]\n\t" \
    "ds_read_b64_tr_b16 v[208:209], v226 offset:4096\n\t" \
    "s_waitcnt lgkmcnt(12)\n\t" \
    "v_mfma_f32_16x16x32_bf16 v[4:7], v[196:199], v[128:131], v[4:7]\n\t" \
    "ds_read_b64_tr_b16 v[210:211], v226 offset:4608\n\t" \
    "s_waitcnt lgkmcnt(11)\n\t" \
    "v_mfma_f32_16x16x32_bf16 v[8:11], v[200:203], v[128:131], v[8:11]\n\t" \
    "ds_read_b128 v[160:163], v225 offset:0\n\t" \
    "s_waitcnt lgkmcnt(10)\n\t" \
    "v_mfma_f32_16x16x32_bf16 v[12:15], v[204:207], v[128:131], v[12:15]\n\t" \
    "ds_read_b64_tr_b16 v[212:213], v227 offset:4096\n\t" \
    "s_waitcnt lgkmcnt(10)\n\t" \
    "v_mfma_f32_16x16x32_bf16 v[16:19], v[192:195], v[132:135], v[16:19]\n\t" \
    "ds_read_b64_tr_b16 v[214:215], v227 offset:4608\n\t" \
    "v_mfma_f32_16x16x32_bf16 v[20:23], v[196:199], v[132:135], v[20:23]\n\t" \
    "ds_read_b64_tr_b16 v[216:217], v228 offset:4096\n\t" \
    "v_mfma_f32_16x16x32_bf16 v[24:27], v[200:203], v[132:135], v[24:27]\n\t" \
    "ds_read_b64_tr_b16 v[218:219], v228 offset:4608\n\t" \
    "v_mfma_f32_16x16x32_bf16 v[28:31], v[204:207], v[132:135], v[28:31]\n\t" \
    "ds_read_b64_tr_b16 v[220:221], v229 offset:4096\n\t" \
    "s_waitcnt lgkmcnt(13)\n\t" \
    "v_mfma_f32_16x16x32_bf16 v[32:35], v[192:195], v[136:139], v[32:35]\n\t" \
    "ds_read_b64_tr_b16 v[222:223], v229 offset:4608\n\t" \
    "v_mfma_f32_16x16x32_bf16 v[36:39], v[196:199], v[136:139], v[36:39]\n\t" \
    "ds_read_b128 v[164:167], v225 offset:2048\n\t" \
    "v_mfma_f32_16x16x32_bf16 v[40:43], v[200:203], v[136:139], v[40:43]\n\t" \
    "ds_read_b128 v[168:171], v225 offset:4096\n\t" \
    "v_mfma_f32_16x16x32_bf16 v[44:47], v[204:207], v[136:139], v[44:47]\n\t" \
    "ds_read_b128 v[172:175], v225 offset:6144\n\t" \
    "s_waitcnt lgkmcnt(15)\n\t" \
    "v_mfma_f32_16x16x32_bf16 v[48:51], v[192:195], v[140:143], v[48:51]\n\t" \
    "ds_read_b128 v[176:179], v225 offset:8192\n\t" \
    "v_mfma_f32_16x16x32_bf16 v[52:55], v[196:199], v[140:143], v[52:55]\n\t" \
    "ds_read_b128 v[180:183], v225 offset:10240\n\t" \
    "v_mfma_f32_16x16x32_bf16 v[56:59], v[200:203], v[140:143], v[56:59]\n\t" \
    "ds_read_b128 v[184:187], v225 offset:12288\n\t" \
    "v_mfma_f32_16x16x32_bf16 v[60:63], v[204:207], v[140:143], v[60:63]\n\t" \
    "ds_read_b128 v[188:191], v225 offset:14336\n\t" \
    "v_mfma_f32_16x16x32_bf16 v[64:67], v[192:195], v[144:147], v[64:67]\n\t" \
    "s_add_u32 s51, s51, 49152\n\t" \
    "v_mfma_f32_16x16x32_bf16 v[68:71], v[196:199], v[144:147], v[68:71]\n\t" \
    "s_cmp_eq_u32 s51, 147456\n\t" \
    "v_mfma_f32_16x16x32_bf16 v[72:75], v[200:203], v[144:147], v[72:75]\n\t" \
    "s_cselect_b32 s50, s53, s54\n\t" \
    "v_mfma_f32_16x16x32_bf16 v[76:79], v[204:207], v[144:147], v[76:79]\n\t" \
    "s_cselect_b32 s51, 0, s51\n\t" \
    "s_waitcnt lgkmcnt(15)\n\t" \
    "v_mfma_f32_16x16x32_bf16 v[80:83], v[192:195], v[148:151], v[80:83]\n\t" \
    "v_add_u32 v224, s50, v224\n\t" \
    "v_mfma_f32_16x16x32_bf16 v[84:87], v[196:199], v[148:151], v[84:87]\n\t" \
    "v_add_u32 v225, s50, v225\n\t" \
    "v_mfma_f32_16x16x32_bf16 v[88:91], v[200:203], v[148:151], v[88:91]\n\t" \
    "v_add_u32 v226, s50, v226\n\t" \
    "v_mfma_f32_16x16x32_bf16 v[92:95], v[204:207], v[148:151], v[92:95]\n\t" \
    "v_add_u32 v227, s50, v227\n\t" \
    "v_mfma_f32_16x16x32_bf16 v[96:99], v[192:195], v[152:155], v[96:99]\n\t" \
    "v_add_u32 v228, s50, v228\n\t" \
    "v_mfma_f32_16x16x32_bf16 v[100:103], v[196:199], v[152:155], v[100:103]\n\t" \
    "v_add_u32 v229, s50, v229\n\t" \
    "v_mfma_f32_16x16x32_bf16 v[104:107], v[200:203], v[152:155], v[104:107]\n\t" \
    "v_mfma_f32_16x16x32_bf16 v[108:111], v[204:207], v[152:155], v[108:111]\n\t" \
    "v_mfma_f32_16x16x32_bf16 v[112:115], v[192:195], v[156:159], v[112:115]\n\t" \
    "v_mfma_f32_16x16x32_bf16 v[116:119], v[196:199], v[156:159], v[116:119]\n\t" \
    "v_mfma_f32_16x16x32_bf16 v[120:123], v[200:203], v[156:159], v[120:123]\n\t" \
    "v_mfma_f32_16x16x32_bf16 v[124:127], v[204:207], v[156:159], v[124:127]\n\t" \
    "s_waitcnt lgkmcnt(0)\n\t" \
    "s_barrier\n\t" \
    "v_mfma_f32_16x16x32_bf16 v[0:3], v[208:211], v[160:163], v[0:3]\n\t" \
    "ds_read_b64_tr_b16 v[192:193], v226 offset:0\n\t" \
    "v_mfma_f32_16x16x32_bf16 v[4:7], v[212:215], v[160:163], v[4:7]\n\t" \
    "ds_read_b64_tr_b16 v[194:195], v226 offset:512\n\t" \
    "v_mfma_f32_16x16x32_bf16 v[8:11], v[216:219], v[160:163], v[8:11]\n\t" \
    "ds_read_b128 v[128:131], v224 offset:0\n\t" \
    "v_mfma_f32_16x16x32_bf16 v[12:15], v[220:223], v[160:163], v[12:15]\n\t" \
    "ds_read_b64_tr_b16 v[196:197], v227 offset:0\n\t" \
    "v_mfma_f32_16x16x32_bf16 v[16:19], v[208:211], v[164:167], v[16:19]\n\t" \
    "ds_read_b64_tr_b16 v[198:199], v227 offset:512\n\t" \
    "v_mfma_f32_16x16x32_bf16 v[20:23], v[212:215], v[164:167], v[20:23]\n\t" \
    "ds_read_b64_tr_b16 v[200:201], v228 offset:0\n\t" \
    "v_mfma_f32_16x16x32_bf16 v[24:27], v[216:219], v[164:167], v[24:27]\n\t" \
    "ds_read_b64_tr_b16 v[202:203], v228 offset:512\n\t" \
    "v_mfma_f32_16x16x32_bf16 v[28:31], v[220:223], v[164:167], v[28:31]\n\t" \
    "ds_read_b64_tr_b16 v[204:205], v229 offset:0\n\t" \
    "v_mfma_f32_16x16x32_bf16 v[32:35], v[208:211], v[168:171], v[32:35]\n\t" \
    "ds_read_b64_tr_b16 v[206:207], v229 offset:512\n\t" \
    "v_mfma_f32_16x16x32_bf16 v[36:39], v[212:215], v[168:171], v[36:39]\n\t" \
    "ds_read_b128 v[132:135], v224 offset:2048\n\t" \
    "v_mfma_f32_16x16x32_bf16 v[40:43], v[216:219], v[168:171], v[40:43]\n\t" \
    "ds_read_b128 v[136:139], v224 offset:4096\n\t" \
    "v_mfma_f32_16x16x32_bf16 v[44:47], v[220:223], v[168:171], v[44:47]\n\t" \
    "ds_read_b128 v[140:143], v224 offset:6144\n\t" \
    "v_mfma_f32_16x16x32_bf16 v[48:51], v[208:211], v[172:175], v[48:51]\n\t" \
    "ds_read_b128 v[144:147], v224 offset:8192\n\t" \
    "v_mfma_f32_16x16x32_bf16 v[52:55], v[212:215], v[172:175], v[52:55]\n\t" \
    "ds_read_b128 v[148:151], v224 offset:10240\n\t" \
    "v_mfma_f32_16x16x32_bf16 v[56:59], v[216:219], v[172:175], v[56:59]\n\t" \
    "ds_read_b128 v[152:155], v224 offset:12288\n\t" \
    "v_mfma_f32_16x16x32_bf16 v[60:63], v[220:223], v[172:175], v[60:63]\n\t" \
    "ds_read_b128 v[156:159], v224 offset:14336\n\t" \
    "v_mfma_f32_16x16x32_bf16 v[64:67], v[208:211], v[176:179], v[64:67]\n\t" \
    "s_add_u32 s70, s74, 3072\n\t" \
    "s_addc_u32 s71, s75, 0\n\t" \
    "global_load_dwordx4 v[252:255], %[vl16], s[70:71]\n\t" \
    "v_mfma_f32_16x16x32_bf16 v[68:71], v[212:215], v[176:179], v[68:71]\n\t" \
    "v_mfma_f32_16x16x32_bf16 v[72:75], v[216:219], v[176:179], v[72:75]\n\t" \
    "v_mfma_f32_16x16x32_bf16 v[76:79], v[220:223], v[176:179], v[76:79]\n\t" \
    "v_mfma_f32_16x16x32_bf16 v[80:83], v[208:211], v[180:183], v[80:83]\n\t" \
    "v_mfma_f32_16x16x32_bf16 v[84:87], v[212:215], v[180:183], v[84:87]\n\t" \
    "v_mfma_f32_16x16x32_bf16 v[88:91], v[216:219], v[180:183], v[88:91]\n\t" \
    "v_mfma_f32_16x16x32_bf16 v[92:95], v[220:223], v[180:183], v[92:95]\n\t" \
    "v_mfma_f32_16x16x32_bf16 v[96:99], v[208:211], v[184:187], v[96:99]\n\t" \
    "v_mfma_f32_16x16x32_bf16 v[100:103], v[212:215], v[184:187], v[100:103]\n\t" \
    "v_mfma_f32_16x16x32_bf16 v[104:107], v[216:219], v[184:187], v[104:107]\n\t" \
    "v_mfma_f32_16x16x32_bf16 v[108:111], v[220:223], v[184:187], v[108:111]\n\t" \
    "v_mfma_f32_16x16x32_bf16 v[112:115], v[208:211], v[188:191], v[112:115]\n\t" \
    "v_mfma_f32_16x16x32_bf16 v[116:119], v[212:215], v[188:191], v[116:119]\n\t" \
    "v_mfma_f32_16x16x32_bf16 v[120:123], v[216:219], v[188:191], v[120:123]\n\t" \
    "v_mfma_f32_16x16x32_bf16 v[124:127], v[220:223], v[188:191], v[124:127]\n\t" \
    "s_waitcnt lgkmcnt(13)\n\t" \
    "v_mfma_f32_16x16x32_bf16 v[0:3], v[192:195], v[128:131], v[0:3]\n\t" \
    "ds_read_b64_tr_b16 v[208:209], v226 offset:4096\n\t" \
    "s_waitcnt lgkmcnt(12)\n\t" \
    "v_mfma_f32_16x16x32_bf16 v[4:7], v[196:199], v[128:131], v[4:7]\n\t" \
    "ds_read_b64_tr_b16 v[210:211], v226 offset:4608\n\t" \
    "s_waitcnt lgkmcnt(11)\n\t" \
    "v_mfma_f32_16x16x32_bf16 v[8:11], v[200:203], v[128:131], v[8:11]\n\t" \
    "ds_read_b128 v[160:163], v225 offset:0\n\t" \
    "s_waitcnt lgkmcnt(10)\n\t" \
    "v_mfma_f32_16x16x32_bf16 v[12:15], v[204:207], v[128:131], v[12:15]\n\t" \
    "ds_read_b64_tr_b16 v[212:213], v227 offset:4096\n\t" \
    "s_waitcnt lgkmcnt(10)\n\t" \
    "v_mfma_f32_16x16x32_bf16 v[16:19], v[192:195], v[132:135], v[16:19]\n\t" \
    "ds_read_b64_tr_b16 v[214:215], v227 offset:4608\n\t" \
    "v_mfma_f32_16x16x32_bf16 v[20:23], v[196:199], v[132:135], v[20:23]\n\t" \
    "ds_read_b64_tr_b16 v[216:217], v228 offset:4096\n\t" \
    "v_mfma_f32_16x16x32_bf16 v[24:27], v[200:203], v[132:135], v[24:27]\n\t" \
    "ds_read_b64_tr_b16 v[218:219], v228 offset:4608\n\t" \
    "v_mfma_f32_16x16x32_bf16 v[28:31], v[204:207], v[132:135], v[28:31]\n\t" \
    "ds_read_b64_tr_b16 v[220:221], v229 offset:4096\n\t" \
    "s_waitcnt lgkmcnt(13)\n\t" \
    "v_mfma_f32_16x16x32_bf16 v[32:35], v[192:195], v[136:139], v[32:35]\n\t" \
    "ds_read_b64_tr_b16 v[222:223], v229 offset:4608\n\t" \
    "v_mfma_f32_16x16x32_bf16 v[36:39], v[196:199], v[136:139], v[36:39]\n\t" \
    "ds_read_b128 v[164:167], v225 offset:2048\n\t" \
    "v_mfma_f32_16x16x32_bf16 v[40:43], v[200:203], v[136:139], v[40:43]\n\t" \
    "ds_read_b128 v[168:171], v225 offset:4096\n\t" \
    "v_mfma_f32_16x16x32_bf16 v[44:47], v[204:207], v[136:139], v[44:47]\n\t" \
    "ds_read_b128 v[172:175], v225 offset:6144\n\t" \
    "s_waitcnt lgkmcnt(15)\n\t" \
    "v_mfma_f32_16x16x32_bf16 v[48:51], v[192:195], v[140:143], v[48:51]\n\t" \
    "ds_read_b128 v[176:179], v225 offset:8192\n\t" \
    "v_mfma_f32_16x16x32_bf16 v[52:55], v[196:199], v[140:143], v[52:55]\n\t" \
    "ds_read_b128 v[180:183], v225 offset:10240\n\t" \
    "v_mfma_f32_16x16x32_bf16 v[56:59], v[200:203], v[140:143], v[56:59]\n\t" \
    "ds_read_b128 v[184:187], v225 offset:12288\n\t" \
    "v_mfma_f32_16x16x32_bf16 v[60:63], v[204:207], v[140:143], v[60:63]\n\t" \
    "ds_read_b128 v[188:191], v225 offset:14336\n\t" \
    "v_mfma_f32_16x16x32_bf16 v[64:67], v[192:195], v[144:147], v[64:67]\n\t" \
    "s_add_u32 s51, s51, 49152\n\t" \
    "v_mfma_f32_16x16x32_bf16 v[68:71], v[196:199], v[144:147], v[68:71]\n\t" \
    "s_cmp_eq_u32 s51, 147456\n\t" \
    "v_mfma_f32_16x16x32_bf16 v[72:75], v[200:203], v[144:147], v[72:75]\n\t" \
    "s_cselect_b32 s50, s53, s54\n\t" \
    "v_mfma_f32_16x16x32_bf16 v[76:79], v[204:207], v[144:147], v[76:79]\n\t" \
    "s_cselect_b32 s51, 0, s51\n\t" \
    "s_waitcnt lgkmcnt(15)\n\t" \
    "v_mfma_f32_16x16x32_bf16 v[80:83], v[192:195], v[148:151], v[80:83]\n\t" \
    "v_add_u32 v224, s50, v224\n\t" \
    "v_mfma_f32_16x16x32_bf16 v[84:87], v[196:199], v[148:151], v[84:87]\n\t" \
    "v_add_u32 v225, s50, v225\n\t" \
    "v_mfma_f32_16x16x32_bf16 v[88:91], v[200:203], v[148:151], v[88:91]\n\t" \
    "v_add_u32 v226, s50, v226\n\t" \
    "v_mfma_f32_16x16x32_bf16 v[92:95], v[204:207], v[148:151], v[92:95]\n\t" \
    "v_add_u32 v227, s50, v227\n\t" \
    "v_mfma_f32_16x16x32_bf16 v[96:99], v[192:195], v[152:155], v[96:99]\n\t" \
    "v_add_u32 v228, s50, v228\n\t" \
    "v_mfma_f32_16x16x32_bf16 v[100:103], v[196:199], v[152:155], v[100:103]\n\t" \
    "v_add_u32 v229, s50, v229\n\t" \
    "v_mfma_f32_16x16x32_bf16 v[104:107], v[200:203], v[152:155], v[104:107]\n\t" \
    "v_mfma_f32_16x16x32_bf16 v[108:111], v[204:207], v[152:155], v[108:111]\n\t" \
    "v_mfma_f32_16x16x32_bf16 v[112:115], v[192:195], v[156:159], v[112:115]\n\t" \
    "v_mfma_f32_16x16x32_bf16 v[116:119], v[196:199], v[156:159], v[116:119]\n\t" \
    "v_mfma_f32_16x16x32_bf16 v[120:123], v[200:203], v[156:159], v[120:123]\n\t" \
    "v_mfma_f32_16x16x32_bf16 v[124:127], v[204:207], v[156:159], v[124:127]\n\t" \
    "s_waitcnt lgkmcnt(0)\n\t" \
    "s_barrier\n\t" \
    "v_mfma_f32_16x16x32_bf16 v[0:3], v[208:211], v[160:163], v[0:3]\n\t" \
    "ds_read_b64_tr_b16 v[192:193], v226 offset:0\n\t" \
    "v_mfma_f32_16x16x32_bf16 v[4:7], v[212:215], v[160:163], v[4:7]\n\t" \
    "ds_read_b64_tr_b16 v[194:195], v226 offset:512\n\t" \
    "v_mfma_f32_16x16x32_bf16 v[8:11], v[216:219], v[160:163], v[8:11]\n\t" \
    "ds_read_b128 v[128:131], v224 offset:0\n\t" \
    "v_mfma_f32_16x16x32_bf16 v[12:15], v[220:223], v[160:163], v[12:15]\n\t" \
    "ds_read_b64_tr_b16 v[196:197], v227 offset:0\n\t" \
    "v_mfma_f32_16x16x32_bf16 v[16:19], v[208:211], v[164:167], v[16:19]\n\t" \
    "ds_read_b64_tr_b16 v[198:199], v227 offset:512\n\t" \
    "v_mfma_f32_16x16x32_bf16 v[20:23], v[212:215], v[164:167], v[20:23]\n\t" \
    "ds_read_b64_tr_b16 v[200:201], v228 offset:0\n\t" \
    "v_mfma_f32_16x16x32_bf16 v[24:27], v[216:219], v[164:167], v[24:27]\n\t" \
    "ds_read_b64_tr_b16 v[202:203], v228 offset:512\n\t" \
    "v_mfma_f32_16x16x32_bf16 v[28:31], v[220:223], v[164:167], v[28:31]\n\t" \
    "ds_read_b64_tr_b16 v[204:205], v229 offset:0\n\t" \
    "v_mfma_f32_16x16x32_bf16 v[32:35], v[208:211], v[168:171], v[32:35]\n\t" \
    "ds_read_b64_tr_b16 v[206:207], v229 offset:512\n\t" \
    "v_mfma_f32_16x16x32_bf16 v[36:39], v[212:215], v[168:171], v[36:39]\n\t" \
    "ds_read_b128 v[132:135], v224 offset:2048\n\t" \
    "v_mfma_f32_16x16x32_bf16 v[40:43], v[216:219], v[168:171], v[40:43]\n\t" \
    "ds_read_b128 v[136:139], v224 offset:4096\n\t" \
    "v_mfma_f32_16x16x32_bf16 v[44:47], v[220:223], v[168:171], v[44:47]\n\t" \
    "ds_read_b128 v[140:143], v224 offset:6144\n\t" \
    "v_mfma_f32_16x16x32_bf16 v[48:51], v[208:211], v[172:175], v[48:51]\n\t" \
    "ds_read_b128 v[144:147], v224 offset:8192\n\t" \
    "v_mfma_f32_16x16x32_bf16 v[52:55], v[212:215], v[172:175], v[52:55]\n\t" \
    "ds_read_b128 v[148:151], v224 offset:10240\n\t" \
    "v_mfma_f32_16x16x32_bf16 v[56:59], v[216:219], v[172:175], v[56:59]\n\t" \
    "ds_read_b128 v[152:155], v224 offset:12288\n\t" \
    "v_mfma_f32_16x16x32_bf16 v[60:63], v[220:223], v[172:175], v[60:63]\n\t" \
    "ds_read_b128 v[156:159], v224 offset:14336\n\t" \
    "v_mfma_f32_16x16x32_bf16 v[64:67], v[208:211], v[176:179], v[64:67]\n\t" \
    "s_add_u32 s70, s74, 2100224\n\t" \
    "s_addc_u32 s71, s75, 0\n\t" \
    "global_load_dwordx4 v[252:255], %[vl16], s[70:71]\n\t" \
    "v_mfma_f32_16x16x32_bf16 v[68:71], v[212:215], v[176:179], v[68:71]\n\t" \
    "v_mfma_f32_16x16x32_bf16 v[72:75], v[216:219], v[176:179], v[72:75]\n\t" \
    "v_mfma_f32_16x16x32_bf16 v[76:79], v[220:223], v[176:179], v[76:79]\n\t" \
    "v_mfma_f32_16x16x32_bf16 v[80:83], v[208:211], v[180:183], v[80:83]\n\t" \
    "v_mfma_f32_16x16x32_bf16 v[84:87], v[212:215], v[180:183], v[84:87]\n\t" \
    "v_mfma_f32_16x16x32_bf16 v[88:91], v[216:219], v[180:183], v[88:91]\n\t" \
    "v_mfma_f32_16x16x32_bf16 v[92:95], v[220:223], v[180:183], v[92:95]\n\t" \
    "v_mfma_f32_16x16x32_bf16 v[96:99], v[208:211], v[184:187], v[96:99]\n\t" \
    "v_mfma_f32_16x16x32_bf16 v[100:103], v[212:215], v[184:187], v[100:103]\n\t" \
    "v_mfma_f32_16x16x32_bf16 v[104:107], v[216:219], v[184:187], v[104:107]\n\t" \
    "v_mfma_f32_16x16x32_bf16 v[108:111], v[220:223], v[184:187], v[108:111]\n\t" \
    "v_mfma_f32_16x16x32_bf16 v[112:115], v[208:211], v[188:191], v[112:115]\n\t" \
    "v_mfma_f32_16x16x32_bf16 v[116:119], v[212:215], v[188:191], v[116:119]\n\t" \
    "v_mfma_f32_16x16x32_bf16 v[120:123], v[216:219], v[188:191], v[120:123]\n\t" \
    "v_mfma_f32_16x16x32_bf16 v[124:127], v[220:223], v[188:191], v[124:127]\n\t" \
    "s_waitcnt lgkmcnt(13)\n\t" \
    "v_mfma_f32_16x16x32_bf16 v[0:3], v[192:195], v[128:131], v[0:3]\n\t" \
    "ds_read_b64_tr_b16 v[208:209], v226 offset:4096\n\t" \
    "s_waitcnt lgkmcnt(12)\n\t" \
    "v_mfma_f32_16x16x32_bf16 v[4:7], v[196:199], v[128:131], v[4:7]\n\t" \
    "ds_read_b64_tr_b16 v[210:211], v226 offset:4608\n\t" \
    "s_waitcnt lgkmcnt(11)\n\t" \
    "v_mfma_f32_16x16x32_bf16 v[8:11], v[200:203], v[128:131], v[8:11]\n\t" \
    "ds_read_b128 v[160:163], v225 offset:0\n\t" \
    "s_waitcnt lgkmcnt(10)\n\t" \
    "v_mfma_f32_16x16x32_bf16 v[12:15], v[204:207], v[128:131], v[12:15]\n\t" \
    "ds_read_b64_tr_b16 v[212:213], v227 offset:4096\n\t" \
    "s_waitcnt lgkmcnt(10)\n\t" \
    "v_mfma_f32_16x16x32_bf16 v[16:19], v[192:195], v[132:135], v[16:19]\n\t" \
    "ds_read_b64_tr_b16 v[214:215], v227 offset:4608\n\t" \
    "v_mfma_f32_16x16x32_bf16 v[20:23], v[196:199], v[132:135], v[20:23]\n\t" \
    "ds_read_b64_tr_b16 v[216:217], v228 offset:4096\n\t" \
    "v_mfma_f32_16x16x32_bf16 v[24:27], v[200:203], v[132:135], v[24:27]\n\t" \
    "ds_read_b64_tr_b16 v[218:219], v228 offset:4608\n\t" \
    "v_mfma_f32_16x16x32_bf16 v[28:31], v[204:207], v[132:135], v[28:31]\n\t" \
    "ds_read_b64_tr_b16 v[220:221], v229 offset:4096\n\t" \
    "s_waitcnt lgkmcnt(13)\n\t" \
    "v_mfma_f32_16x16x32_bf16 v[32:35], v[192:195], v[136:139], v[32:35]\n\t" \
    "ds_read_b64_tr_b16 v[222:223], v229 offset:4608\n\t" \
    "v_mfma_f32_16x16x32_bf16 v[36:39], v[196:199], v[136:139], v[36:39]\n\t" \
    "ds_read_b128 v[164:167], v225 offset:2048\n\t" \
    "v_mfma_f32_16x16x32_bf16 v[40:43], v[200:203], v[136:139], v[40:43]\n\t" \
    "ds_read_b128 v[168:171], v225 offset:4096\n\t" \
    "v_mfma_f32_16x16x32_bf16 v[44:47], v[204:207], v[136:139], v[44:47]\n\t" \
    "ds_read_b128 v[172:175], v225 offset:6144\n\t" \
    "s_waitcnt lgkmcnt(15)\n\t" \
    "v_mfma_f32_16x16x32_bf16 v[48:51], v[192:195], v[140:143], v[48:51]\n\t" \
    "ds_read_b128 v[176:179], v225 offset:8192\n\t" \
    "v_mfma_f32_16x16x32_bf16 v[52:55], v[196:199], v[140:143], v[52:55]\n\t" \
    "ds_read_b128 v[180:183], v225 offset:10240\n\t" \
    "v_mfma_f32_16x16x32_bf16 v[56:59], v[200:203], v[140:143], v[56:59]\n\t" \
    "ds_read_b128 v[184:187], v225 offset:12288\n\t" \
    "v_mfma_f32_16x16x32_bf16 v[60:63], v[204:207], v[140:143], v[60:63]\n\t" \
    "ds_read_b128 v[188:191], v225 offset:14336\n\t" \
    "v_mfma_f32_16x16x32_bf16 v[64:67], v[192:195], v[144:147], v[64:67]\n\t" \
    "s_add_u32 s51, s51, 49152\n\t" \
    "v_mfma_f32_16x16x32_bf16 v[68:71], v[196:199], v[144:147], v[68:71]\n\t" \
    "s_cmp_eq_u32 s51, 147456\n\t" \
    "v_mfma_f32_16x16x32_bf16 v[72:75], v[200:203], v[144:147], v[72:75]\n\t" \
    "s_cselect_b32 s50, s53, s54\n\t" \
    "v_mfma_f32_16x16x32_bf16 v[76:79], v[204:207], v[144:147], v[76:79]\n\t" \
    "s_cselect_b32 s51, 0, s51\n\t" \
    "s_waitcnt lgkmcnt(15)\n\t" \
    "v_mfma_f32_16x16x32_bf16 v[80:83], v[192:195], v[148:151], v[80:83]\n\t" \
    "v_add_u32 v224, s50, v224\n\t" \
    "v_mfma_f32_16x16x32_bf16 v[84:87], v[196:199], v[148:151], v[84:87]\n\t" \
    "v_add_u32 v225, s50, v225\n\t" \
    "v_mfma_f32_16x16x32_bf16 v[88:91], v[200:203], v[148:151], v[88:91]\n\t" \
    "v_add_u32 v226, s50, v226\n\t" \
    "v_mfma_f32_16x16x32_bf16 v[92:95], v[204:207], v[148:151], v[92:95]\n\t" \
    "v_add_u32 v227, s50, v227\n\t" \
    "v_mfma_f32_16x16x32_bf16 v[96:99], v[192:195], v[152:155], v[96:99]\n\t" \
    "v_add_u32 v228, s50, v228\n\t" \
    "v_mfma_f32_16x16x32_bf16 v[100:103], v[196:199], v[152:155], v[100:103]\n\t" \
    "v_add_u32 v229, s50, v229\n\t" \
    "v_mfma_f32_16x16x32_bf16 v[104:107], v[200:203], v[152:155], v[104:107]\n\t" \
    "v_mfma_f32_16x16x32_bf16 v[108:111], v[204:207], v[152:155], v[108:111]\n\t" \
    "v_mfma_f32_16x16x32_bf16 v[112:115], v[192:195], v[156:159], v[112:115]\n\t" \
    "v_mfma_f32_16x16x32_bf16 v[116:119], v[196:199], v[156:159], v[116:119]\n\t" \
    "v_mfma_f32_16x16x32_bf16 v[120:123], v[200:203], v[156:159], v[120:123]\n\t" \
    "v_mfma_f32_16x16x32_bf16 v[124:127], v[204:207], v[156:159], v[124:127]\n\t" \
    "s_waitcnt lgkmcnt(0)\n\t" \
    "s_barrier\n\t" \
    "v_mfma_f32_16x16x32_bf16 v[0:3], v[208:211], v[160:163], v[0:3]\n\t" \
    "ds_read_b64_tr_b16 v[192:193], v226 offset:0\n\t" \
    "v_mfma_f32_16x16x32_bf16 v[4:7], v[212:215], v[160:163], v[4:7]\n\t" \
    "ds_read_b64_tr_b16 v[194:195], v226 offset:512\n\t" \
    "v_mfma_f32_16x16x32_bf16 v[8:11], v[216:219], v[160:163], v[8:11]\n\t" \
    "ds_read_b128 v[128:131], v224 offset:0\n\t" \
    "v_mfma_f32_16x16x32_bf16 v[12:15], v[220:223], v[160:163], v[12:15]\n\t" \
    "ds_read_b64_tr_b16 v[196:197], v227 offset:0\n\t" \
    "v_mfma_f32_16x16x32_bf16 v[16:19], v[208:211], v[164:167], v[16:19]\n\t" \
    "ds_read_b64_tr_b16 v[198:199], v227 offset:512\n\t" \
    "v_mfma_f32_16x16x32_bf16 v[20:23], v[212:215], v[164:167], v[20:23]\n\t" \
    "ds_read_b64_tr_b16 v[200:201], v228 offset:0\n\t" \
    "v_mfma_f32_16x16x32_bf16 v[24:27], v[216:219], v[164:167], v[24:27]\n\t" \
    "ds_read_b64_tr_b16 v[202:203], v228 offset:512\n\t" \
    "v_mfma_f32_16x16x32_bf16 v[28:31], v[220:223], v[164:167], v[28:31]\n\t" \
    "ds_read_b64_tr_b16 v[204:205], v229 offset:0\n\t" \
    "v_mfma_f32_16x16x32_bf16 v[32:35], v[208:211], v[168:171], v[32:35]\n\t" \
    "ds_read_b64_tr_b16 v[206:207], v229 offset:512\n\t" \
    "v_mfma_f32_16x16x32_bf16 v[36:39], v[212:215], v[168:171], v[36:39]\n\t" \
    "ds_read_b128 v[132:135], v224 offset:2048\n\t" \
    "v_mfma_f32_16x16x32_bf16 v[40:43], v[216:219], v[168:171], v[40:43]\n\t" \
    "ds_read_b128 v[136:139], v224 offset:4096\n\t" \
    "v_mfma_f32_16x16x32_bf16 v[44:47], v[220:223], v[168:171], v[44:47]\n\t" \
    "ds_read_b128 v[140:143], v224 offset:6144\n\t" \
    "v_mfma_f32_16x16x32_bf16 v[48:51], v[208:211], v[172:175], v[48:51]\n\t" \
    "ds_read_b128 v[144:147], v224 offset:8192\n\t" \
    "v_mfma_f32_16x16x32_bf16 v[52:55], v[212:215], v[172:175], v[52:55]\n\t" \
    "ds_read_b128 v[148:151], v224 offset:10240\n\t" \
    "v_mfma_f32_16x16x32_bf16 v[56:59], v[216:219], v[172:175], v[56:59]\n\t" \
    "ds_read_b128 v[152:155], v224 offset:12288\n\t" \
    "v_mfma_f32_16x16x32_bf16 v[60:63], v[220:223], v[172:175], v[60:63]\n\t" \
    "ds_read_b128 v[156:159], v224 offset:14336\n\t" \
    "v_mfma_f32_16x16x32_bf16 v[64:67], v[208:211], v[176:179], v[64:67]\n\t" \
    "s_add_u32 s70, s74, 4197376\n\t" \
    "s_addc_u32 s71, s75, 0\n\t" \
    "global_load_dwordx4 v[252:255], %[vl16], s[70:71]\n\t" \
    "v_mfma_f32_16x16x32_bf16 v[68:71], v[212:215], v[176:179], v[68:71]\n\t" \
    "v_mfma_f32_16x16x32_bf16 v[72:75], v[216:219], v[176:179], v[72:75]\n\t" \
    "v_mfma_f32_16x16x32_bf16 v[76:79], v[220:223], v[176:179], v[76:79]\n\t" \
    "v_mfma_f32_16x16x32_bf16 v[80:83], v[208:211], v[180:183], v[80:83]\n\t" \
    "v_mfma_f32_16x16x32_bf16 v[84:87], v[212:215], v[180:183], v[84:87]\n\t" \
    "v_mfma_f32_16x16x32_bf16 v[88:91], v[216:219], v[180:183], v[88:91]\n\t" \
    "v_mfma_f32_16x16x32_bf16 v[92:95], v[220:223], v[180:183], v[92:95]\n\t" \
    "v_mfma_f32_16x16x32_bf16 v[96:99], v[208:211], v[184:187], v[96:99]\n\t" \
    "v_mfma_f32_16x16x32_bf16 v[100:103], v[212:215], v[184:187], v[100:103]\n\t" \
    "v_mfma_f32_16x16x32_bf16 v[104:107], v[216:219], v[184:187], v[104:107]\n\t" \
    "v_mfma_f32_16x16x32_bf16 v[108:111], v[220:223], v[184:187], v[108:111]\n\t" \
    "v_mfma_f32_16x16x32_bf16 v[112:115], v[208:211], v[188:191], v[112:115]\n\t" \
    "v_mfma_f32_16x16x32_bf16 v[116:119], v[212:215], v[188:191], v[116:119]\n\t" \
    "v_mfma_f32_16x16x32_bf16 v[120:123], v[216:219], v[188:191], v[120:123]\n\t" \
    "v_mfma_f32_16x16x32_bf16 v[124:127], v[220:223], v[188:191], v[124:127]\n\t" \
    "s_waitcnt lgkmcnt(13)\n\t" \
    "v_mfma_f32_16x16x32_bf16 v[0:3], v[192:195], v[128:131], v[0:3]\n\t" \
    "ds_read_b64_tr_b16 v[208:209], v226 offset:4096\n\t" \
    "s_waitcnt lgkmcnt(12)\n\t" \
    "v_mfma_f32_16x16x32_bf16 v[4:7], v[196:199], v[128:131], v[4:7]\n\t" \
    "ds_read_b64_tr_b16 v[210:211], v226 offset:4608\n\t" \
    "s_waitcnt lgkmcnt(11)\n\t" \
    "v_mfma_f32_16x16x32_bf16 v[8:11], v[200:203], v[128:131], v[8:11]\n\t" \
    "ds_read_b128 v[160:163], v225 offset:0\n\t" \
    "s_waitcnt lgkmcnt(10)\n\t" \
    "v_mfma_f32_16x16x32_bf16 v[12:15], v[204:207], v[128:131], v[12:15]\n\t" \
    "ds_read_b64_tr_b16 v[212:213], v227 offset:4096\n\t" \
    "s_waitcnt lgkmcnt(10)\n\t" \
    "v_mfma_f32_16x16x32_bf16 v[16:19], v[192:195], v[132:135], v[16:19]\n\t" \
    "ds_read_b64_tr_b16 v[214:215], v227 offset:4608\n\t" \
    "v_mfma_f32_16x16x32_bf16 v[20:23], v[196:199], v[132:135], v[20:23]\n\t" \
    "ds_read_b64_tr_b16 v[216:217], v228 offset:4096\n\t" \
    "v_mfma_f32_16x16x32_bf16 v[24:27], v[200:203], v[132:135], v[24:27]\n\t" \
    "ds_read_b64_tr_b16 v[218:219], v228 offset:4608\n\t" \
    "v_mfma_f32_16x16x32_bf16 v[28:31], v[204:207], v[132:135], v[28:31]\n\t" \
    "ds_read_b64_tr_b16 v[220:221], v229 offset:4096\n\t" \
    "s_waitcnt lgkmcnt(13)\n\t" \
    "v_mfma_f32_16x16x32_bf16 v[32:35], v[192:195], v[136:139], v[32:35]\n\t" \
    "ds_read_b64_tr_b16 v[222:223], v229 offset:4608\n\t" \
    "v_mfma_f32_16x16x32_bf16 v[36:39], v[196:199], v[136:139], v[36:39]\n\t" \
    "ds_read_b128 v[164:167], v225 offset:2048\n\t" \
    "v_mfma_f32_16x16x32_bf16 v[40:43], v[200:203], v[136:139], v[40:43]\n\t" \
    "ds_read_b128 v[168:171], v225 offset:4096\n\t" \
    "v_mfma_f32_16x16x32_bf16 v[44:47], v[204:207], v[136:139], v[44:47]\n\t" \
    "ds_read_b128 v[172:175], v225 offset:6144\n\t" \
    "s_waitcnt lgkmcnt(15)\n\t" \
    "v_mfma_f32_16x16x32_bf16 v[48:51], v[192:195], v[140:143], v[48:51]\n\t" \
    "ds_read_b128 v[176:179], v225 offset:8192\n\t" \
    "v_mfma_f32_16x16x32_bf16 v[52:55], v[196:199], v[140:143], v[52:55]\n\t" \
    "ds_read_b128 v[180:183], v225 offset:10240\n\t" \
    "v_mfma_f32_16x16x32_bf16 v[56:59], v[200:203], v[140:143], v[56:59]\n\t" \
    "ds_read_b128 v[184:187], v225 offset:12288\n\t" \
    "v_mfma_f32_16x16x32_bf16 v[60:63], v[204:207], v[140:143], v[60:63]\n\t" \
    "ds_read_b128 v[188:191], v225 offset:14336\n\t" \
    "v_mfma_f32_16x16x32_bf16 v[64:67], v[192:195], v[144:147], v[64:67]\n\t" \
    "s_add_u32 s51, s51, 49152\n\t" \
    "v_mfma_f32_16x16x32_bf16 v[68:71], v[196:199], v[144:147], v[68:71]\n\t" \
    "s_cmp_eq_u32 s51, 147456\n\t" \
    "v_mfma_f32_16x16x32_bf16 v[72:75], v[200:203], v[144:147], v[72:75]\n\t" \
    "s_cselect_b32 s50, s53, s54\n\t" \
    "v_mfma_f32_16x16x32_bf16 v[76:79], v[204:207], v[144:147], v[76:79]\n\t" \
    "s_cselect_b32 s51, 0, s51\n\t" \
    "s_waitcnt lgkmcnt(15)\n\t" \
    "v_mfma_f32_16x16x32_bf16 v[80:83], v[192:195], v[148:151], v[80:83]\n\t" \
    "v_add_u32 v224, s50, v224\n\t" \
    "v_mfma_f32_16x16x32_bf16 v[84:87], v[196:199], v[148:151], v[84:87]\n\t" \
    "v_add_u32 v225, s50, v225\n\t" \
    "v_mfma_f32_16x16x32_bf16 v[88:91], v[200:203], v[148:151], v[88:91]\n\t" \
    "v_add_u32 v226, s50, v226\n\t" \
    "v_mfma_f32_16x16x32_bf16 v[92:95], v[204:207], v[148:151], v[92:95]\n\t" \
    "v_add_u32 v227, s50, v227\n\t" \
    "v_mfma_f32_16x16x32_bf16 v[96:99], v[192:195], v[152:155], v[96:99]\n\t" \
    "v_add_u32 v228, s50, v228\n\t" \
    "v_mfma_f32_16x16x32_bf16 v[100:103], v[196:199], v[152:155], v[100:103]\n\t" \
    "v_add_u32 v229, s50, v229\n\t" \
    "v_mfma_f32_16x16x32_bf16 v[104:107], v[200:203], v[152:155], v[104:107]\n\t" \
    "v_mfma_f32_16x16x32_bf16 v[108:111], v[204:207], v[152:155], v[108:111]\n\t" \
    "v_mfma_f32_16x16x32_bf16 v[112:115], v[192:195], v[156:159], v[112:115]\n\t" \
    "v_mfma_f32_16x16x32_bf16 v[116:119], v[196:199], v[156:159], v[116:119]\n\t" \
    "v_mfma_f32_16x16x32_bf16 v[120:123], v[200:203], v[156:159], v[120:123]\n\t" \
    "v_mfma_f32_16x16x32_bf16 v[124:127], v[204:207], v[156:159], v[124:127]\n\t" \
    "s_waitcnt lgkmcnt(0)\n\t" \
    "s_barrier\n\t" \
    "v_mfma_f32_16x16x32_bf16 v[0:3], v[208:211], v[160:163], v[0:3]\n\t" \
    "ds_read_b64_tr_b16 v[192:193], v226 offset:0\n\t" \
    "v_mfma_f32_16x16x32_bf16 v[4:7], v[212:215], v[160:163], v[4:7]\n\t" \
    "ds_read_b64_tr_b16 v[194:195], v226 offset:512\n\t" \
    "v_mfma_f32_16x16x32_bf16 v[8:11], v[216:219], v[160:163], v[8:11]\n\t" \
    "ds_read_b128 v[128:131], v224 offset:0\n\t" \
    "v_mfma_f32_16x16x32_bf16 v[12:15], v[220:223], v[160:163], v[12:15]\n\t" \
    "ds_read_b64_tr_b16 v[196:197], v227 offset:0\n\t" \
    "v_mfma_f32_16x16x32_bf16 v[16:19], v[208:211], v[164:167], v[16:19]\n\t" \
    "ds_read_b64_tr_b16 v[198:199], v227 offset:512\n\t" \
    "v_mfma_f32_16x16x32_bf16 v[20:23], v[212:215], v[164:167], v[20:23]\n\t" \
    "ds_read_b64_tr_b16 v[200:201], v228 offset:0\n\t" \
    "v_mfma_f32_16x16x32_bf16 v[24:27], v[216:219], v[164:167], v[24:27]\n\t" \
    "ds_read_b64_tr_b16 v[202:203], v228 offset:512\n\t" \
    "v_mfma_f32_16x16x32_bf16 v[28:31], v[220:223], v[164:167], v[28:31]\n\t" \
    "ds_read_b64_tr_b16 v[204:205], v229 offset:0\n\t" \
    "v_mfma_f32_16x16x32_bf16 v[32:35], v[208:211], v[168:171], v[32:35]\n\t" \
    "ds_read_b64_tr_b16 v[206:207], v229 offset:512\n\t" \
    "v_mfma_f32_16x16x32_bf16 v[36:39], v[212:215], v[168:171], v[36:39]\n\t" \
    "ds_read_b128 v[132:135], v224 offset:2048\n\t" \
    "v_mfma_f32_16x16x32_bf16 v[40:43], v[216:219], v[168:171], v[40:43]\n\t" \
    "ds_read_b128 v[136:139], v224 offset:4096\n\t" \
    "v_mfma_f32_16x16x32_bf16 v[44:47], v[220:223], v[168:171], v[44:47]\n\t" \
    "ds_read_b128 v[140:143], v224 offset:6144\n\t" \
    "v_mfma_f32_16x16x32_bf16 v[48:51], v[208:211], v[172:175], v[48:51]\n\t" \
    "ds_read_b128 v[144:147], v224 offset:8192\n\t" \
    "v_mfma_f32_16x16x32_bf16 v[52:55], v[212:215], v[172:175], v[52:55]\n\t" \
    "ds_read_b128 v[148:151], v224 offset:10240\n\t" \
    "v_mfma_f32_16x16x32_bf16 v[56:59], v[216:219], v[172:175], v[56:59]\n\t" \
    "ds_read_b128 v[152:155], v224 offset:12288\n\t" \
    "v_mfma_f32_16x16x32_bf16 v[60:63], v[220:223], v[172:175], v[60:63]\n\t" \
    "ds_read_b128 v[156:159], v224 offset:14336\n\t" \
    "v_mfma_f32_16x16x32_bf16 v[64:67], v[208:211], v[176:179], v[64:67]\n\t" \
    "s_add_u32 s70, s74, 6294528\n\t" \
    "s_addc_u32 s71, s75, 0\n\t" \
    "global_load_dwordx4 v[252:255], %[vl16], s[70:71]\n\t" \
    "v_mfma_f32_16x16x32_bf16 v[68:71], v[212:215], v[176:179], v[68:71]\n\t" \
    "v_mfma_f32_16x16x32_bf16 v[72:75], v[216:219], v[176:179], v[72:75]\n\t" \
    "v_mfma_f32_16x16x32_bf16 v[76:79], v[220:223], v[176:179], v[76:79]\n\t" \
    "v_mfma_f32_16x16x32_bf16 v[80:83], v[208:211], v[180:183], v[80:83]\n\t" \
    "v_mfma_f32_16x16x32_bf16 v[84:87], v[212:215], v[180:183], v[84:87]\n\t" \
    "v_mfma_f32_16x16x32_bf16 v[88:91], v[216:219], v[180:183], v[88:91]\n\t" \
    "v_mfma_f32_16x16x32_bf16 v[92:95], v[220:223], v[180:183], v[92:95]\n\t" \
    "v_mfma_f32_16x16x32_bf16 v[96:99], v[208:211], v[184:187], v[96:99]\n\t" \
    "v_mfma_f32_16x16x32_bf16 v[100:103], v[212:215], v[184:187], v[100:103]\n\t" \
    "v_mfma_f32_16x16x32_bf16 v[104:107], v[216:219], v[184:187], v[104:107]\n\t" \
    "v_mfma_f32_16x16x32_bf16 v[108:111], v[220:223], v[184:187], v[108:111]\n\t" \
    "v_mfma_f32_16x16x32_bf16 v[112:115], v[208:211], v[188:191], v[112:115]\n\t" \
    "v_mfma_f32_16x16x32_bf16 v[116:119], v[212:215], v[188:191], v[116:119]\n\t" \
    "v_mfma_f32_16x16x32_bf16 v[120:123], v[216:219], v[188:191], v[120:123]\n\t" \
    "v_mfma_f32_16x16x32_bf16 v[124:127], v[220:223], v[188:191], v[124:127]\n\t" \
    "s_waitcnt lgkmcnt(13)\n\t" \
    "v_mfma_f32_16x16x32_bf16 v[0:3], v[192:195], v[128:131], v[0:3]\n\t" \
    "ds_read_b64_tr_b16 v[208:209], v226 offset:4096\n\t" \
    "s_waitcnt lgkmcnt(12)\n\t" \
    "v_mfma_f32_16x16x32_bf16 v[4:7], v[196:199], v[128:131], v[4:7]\n\t" \
    "ds_read_b64_tr_b16 v[210:211], v226 offset:4608\n\t" \
    "s_waitcnt lgkmcnt(11)\n\t" \
    "v_mfma_f32_16x16x32_bf16 v[8:11], v[200:203], v[128:131], v[8:11]\n\t" \
    "ds_read_b128 v[160:163], v225 offset:0\n\t" \
    "s_waitcnt lgkmcnt(10)\n\t" \
    "v_mfma_f32_16x16x32_bf16 v[12:15], v[204:207], v[128:131], v[12:15]\n\t" \
    "ds_read_b64_tr_b16 v[212:213], v227 offset:4096\n\t" \
    "s_waitcnt lgkmcnt(10)\n\t" \
    "v_mfma_f32_16x16x32_bf16 v[16:19], v[192:195], v[132:135], v[16:19]\n\t" \
    "ds_read_b64_tr_b16 v[214:215], v227 offset:4608\n\t" \
    "v_mfma_f32_16x16x32_bf16 v[20:23], v[196:199], v[132:135], v[20:23]\n\t" \
    "ds_read_b64_tr_b16 v[216:217], v228 offset:4096\n\t" \
    "v_mfma_f32_16x16x32_bf16 v[24:27], v[200:203], v[132:135], v[24:27]\n\t" \
    "ds_read_b64_tr_b16 v[218:219], v228 offset:4608\n\t" \
    "v_mfma_f32_16x16x32_bf16 v[28:31], v[204:207], v[132:135], v[28:31]\n\t" \
    "ds_read_b64_tr_b16 v[220:221], v229 offset:4096\n\t" \
    "s_waitcnt lgkmcnt(13)\n\t" \
    "v_mfma_f32_16x16x32_bf16 v[32:35], v[192:195], v[136:139], v[32:35]\n\t" \
    "ds_read_b64_tr_b16 v[222:223], v229 offset:4608\n\t" \
    "v_mfma_f32_16x16x32_bf16 v[36:39], v[196:199], v[136:139], v[36:39]\n\t" \
    "ds_read_b128 v[164:167], v225 offset:2048\n\t" \
    "v_mfma_f32_16x16x32_bf16 v[40:43], v[200:203], v[136:139], v[40:43]\n\t" \
    "ds_read_b128 v[168:171], v225 offset:4096\n\t" \
    "v_mfma_f32_16x16x32_bf16 v[44:47], v[204:207], v[136:139], v[44:47]\n\t" \
    "ds_read_b128 v[172:175], v225 offset:6144\n\t" \
    "s_waitcnt lgkmcnt(15)\n\t" \
    "v_mfma_f32_16x16x32_bf16 v[48:51], v[192:195], v[140:143], v[48:51]\n\t" \
    "ds_read_b128 v[176:179], v225 offset:8192\n\t" \
    "v_mfma_f32_16x16x32_bf16 v[52:55], v[196:199], v[140:143], v[52:55]\n\t" \
    "ds_read_b128 v[180:183], v225 offset:10240\n\t" \
    "v_mfma_f32_16x16x32_bf16 v[56:59], v[200:203], v[140:143], v[56:59]\n\t" \
    "ds_read_b128 v[184:187], v225 offset:12288\n\t" \
    "v_mfma_f32_16x16x32_bf16 v[60:63], v[204:207], v[140:143], v[60:63]\n\t" \
    "ds_read_b128 v[188:191], v225 offset:14336\n\t" \
    "v_mfma_f32_16x16x32_bf16 v[64:67], v[192:195], v[144:147], v[64:67]\n\t" \
    "s_add_u32 s51, s51, 49152\n\t" \
    "v_mfma_f32_16x16x32_bf16 v[68:71], v[196:199], v[144:147], v[68:71]\n\t" \
    "s_cmp_eq_u32 s51, 147456\n\t" \
    "v_mfma_f32_16x16x32_bf16 v[72:75], v[200:203], v[144:147], v[72:75]\n\t" \
    "s_cselect_b32 s50, s53, s54\n\t" \
    "v_mfma_f32_16x16x32_bf16 v[76:79], v[204:207], v[144:147], v[76:79]\n\t" \
    "s_cselect_b32 s51, 0, s51\n\t" \
    "s_waitcnt lgkmcnt(15)\n\t" \
    "v_mfma_f32_16x16x32_bf16 v[80:83], v[192:195], v[148:151], v[80:83]\n\t" \
    "v_add_u32 v224, s50, v224\n\t" \
    "v_mfma_f32_16x16x32_bf16 v[84:87], v[196:199], v[148:151], v[84:87]\n\t" \
    "v_add_u32 v225, s50, v225\n\t" \
    "v_mfma_f32_16x16x32_bf16 v[88:91], v[200:203], v[148:151], v[88:91]\n\t" \
    "v_add_u32 v226, s50, v226\n\t" \
    "v_mfma_f32_16x16x32_bf16 v[92:95], v[204:207], v[148:151], v[92:95]\n\t" \
    "v_add_u32 v227, s50, v227\n\t" \
    "v_mfma_f32_16x16x32_bf16 v[96:99], v[192:195], v[152:155], v[96:99]\n\t" \
    "v_add_u32 v228, s50, v228\n\t" \
    "v_mfma_f32_16x16x32_bf16 v[100:103], v[196:199], v[152:155], v[100:103]\n\t" \
    "v_add_u32 v229, s50, v229\n\t" \
    "v_mfma_f32_16x16x32_bf16 v[104:107], v[200:203], v[152:155], v[104:107]\n\t" \
    "v_mfma_f32_16x16x32_bf16 v[108:111], v[204:207], v[152:155], v[108:111]\n\t" \
    "v_mfma_f32_16x16x32_bf16 v[112:115], v[192:195], v[156:159], v[112:115]\n\t" \
    "v_mfma_f32_16x16x32_bf16 v[116:119], v[196:199], v[156:159], v[116:119]\n\t" \
    "v_mfma_f32_16x16x32_bf16 v[120:123], v[200:203], v[156:159], v[120:123]\n\t" \
    "v_mfma_f32_16x16x32_bf16 v[124:127], v[204:207], v[156:159], v[124:127]\n\t" \
    "s_waitcnt lgkmcnt(0)\n\t" \
    "s_barrier\n\t" \
    "v_mfma_f32_16x16x32_bf16 v[0:3], v[208:211], v[160:163], v[0:3]\n\t" \
    "ds_read_b64_tr_b16 v[192:193], v226 offset:0\n\t" \
    "v_mfma_f32_16x16x32_bf16 v[4:7], v[212:215], v[160:163], v[4:7]\n\t" \
    "ds_read_b64_tr_b16 v[194:195], v226 offset:512\n\t" \
    "v_mfma_f32_16x16x32_bf16 v[8:11], v[216:219], v[160:163], v[8:11]\n\t" \
    "ds_read_b128 v[128:131], v224 offset:0\n\t" \
    "v_mfma_f32_16x16x32_bf16 v[12:15], v[220:223], v[160:163], v[12:15]\n\t" \
    "ds_read_b64_tr_b16 v[196:197], v227 offset:0\n\t" \
    "v_mfma_f32_16x16x32_bf16 v[16:19], v[208:211], v[164:167], v[16:19]\n\t" \
    "ds_read_b64_tr_b16 v[198:199], v227 offset:512\n\t" \
    "v_mfma_f32_16x16x32_bf16 v[20:23], v[212:215], v[164:167], v[20:23]\n\t" \
    "ds_read_b64_tr_b16 v[200:201], v228 offset:0\n\t" \
    "v_mfma_f32_16x16x32_bf16 v[24:27], v[216:219], v[164:167], v[24:27]\n\t" \
    "ds_read_b64_tr_b16 v[202:203], v228 offset:512\n\t" \
    "v_mfma_f32_16x16x32_bf16 v[28:31], v[220:223], v[164:167], v[28:31]\n\t" \
    "ds_read_b64_tr_b16 v[204:205], v229 offset:0\n\t" \
    "v_mfma_f32_16x16x32_bf16 v[32:35], v[208:211], v[168:171], v[32:35]\n\t" \
    "ds_read_b64_tr_b16 v[206:207], v229 offset:512\n\t" \
    "v_mfma_f32_16x16x32_bf16 v[36:39], v[212:215], v[168:171], v[36:39]\n\t" \
    "ds_read_b128 v[132:135], v224 offset:2048\n\t" \
    "v_mfma_f32_16x16x32_bf16 v[40:43], v[216:219], v[168:171], v[40:43]\n\t" \
    "ds_read_b128 v[136:139], v224 offset:4096\n\t" \
    "v_mfma_f32_16x16x32_bf16 v[44:47], v[220:223], v[168:171], v[44:47]\n\t" \
    "ds_read_b128 v[140:143], v224 offset:6144\n\t" \
    "v_mfma_f32_16x16x32_bf16 v[48:51], v[208:211], v[172:175], v[48:51]\n\t" \
    "ds_read_b128 v[144:147], v224 offset:8192\n\t" \
    "v_mfma_f32_16x16x32_bf16 v[52:55], v[212:215], v[172:175], v[52:55]\n\t" \
    "ds_read_b128 v[148:151], v224 offset:10240\n\t" \
    "v_mfma_f32_16x16x32_bf16 v[56:59], v[216:219], v[172:175], v[56:59]\n\t" \
    "ds_read_b128 v[152:155], v224 offset:12288\n\t" \
    "v_mfma_f32_16x16x32_bf16 v[60:63], v[220:223], v[172:175], v[60:63]\n\t" \
    "ds_read_b128 v[156:159], v224 offset:14336\n\t" \
    "v_mfma_f32_16x16x32_bf16 v[64:67], v[208:211], v[176:179], v[64:67]\n\t" \
    "s_add_u32 s70, s76, 3145728\n\t" \
    "s_addc_u32 s71, s77, 0\n\t" \
    "global_load_dwordx4 v[252:255], %[vl16], s[70:71]\n\t" \
    "v_mfma_f32_16x16x32_bf16 v[68:71], v[212:215], v[176:179], v[68:71]\n\t" \
    "v_mfma_f32_16x16x32_bf16 v[72:75], v[216:219], v[176:179], v[72:75]\n\t" \
    "v_mfma_f32_16x16x32_bf16 v[76:79], v[220:223], v[176:179], v[76:79]\n\t" \
    "v_mfma_f32_16x16x32_bf16 v[80:83], v[208:211], v[180:183], v[80:83]\n\t" \
    "v_mfma_f32_16x16x32_bf16 v[84:87], v[212:215], v[180:183], v[84:87]\n\t" \
    "v_mfma_f32_16x16x32_bf16 v[88:91], v[216:219], v[180:183], v[88:91]\n\t" \
    "v_mfma_f32_16x16x32_bf16 v[92:95], v[220:223], v[180:183], v[92:95]\n\t" \
    "v_mfma_f32_16x16x32_bf16 v[96:99], v[208:211], v[184:187], v[96:99]\n\t" \
    "v_mfma_f32_16x16x32_bf16 v[100:103], v[212:215], v[184:187], v[100:103]\n\t" \
    "v_mfma_f32_16x16x32_bf16 v[104:107], v[216:219], v[184:187], v[104:107]\n\t" \
    "v_mfma_f32_16x16x32_bf16 v[108:111], v[220:223], v[184:187], v[108:111]\n\t" \
    "v_mfma_f32_16x16x32_bf16 v[112:115], v[208:211], v[188:191], v[112:115]\n\t" \
    "v_mfma_f32_16x16x32_bf16 v[116:119], v[212:215], v[188:191], v[116:119]\n\t" \
    "v_mfma_f32_16x16x32_bf16 v[120:123], v[216:219], v[188:191], v[120:123]\n\t" \
    "v_mfma_f32_16x16x32_bf16 v[124:127], v[220:223], v[188:191], v[124:127]\n\t" \
    "s_waitcnt lgkmcnt(13)\n\t" \
    "v_mfma_f32_16x16x32_bf16 v[0:3], v[192:195], v[128:131], v[0:3]\n\t" \
    "ds_read_b64_tr_b16 v[208:209], v226 offset:4096\n\t" \
    "s_waitcnt lgkmcnt(12)\n\t" \
    "v_mfma_f32_16x16x32_bf16 v[4:7], v[196:199], v[128:131], v[4:7]\n\t" \
    "ds_read_b64_tr_b16 v[210:211], v226 offset:4608\n\t" \
    "s_waitcnt lgkmcnt(11)\n\t" \
    "v_mfma_f32_16x16x32_bf16 v[8:11], v[200:203], v[128:131], v[8:11]\n\t" \
    "ds_read_b128 v[160:163], v225 offset:0\n\t" \
    "s_waitcnt lgkmcnt(10)\n\t" \
    "v_mfma_f32_16x16x32_bf16 v[12:15], v[204:207], v[128:131], v[12:15]\n\t" \
    "ds_read_b64_tr_b16 v[212:213], v227 offset:4096\n\t" \
    "s_waitcnt lgkmcnt(10)\n\t" \
    "v_mfma_f32_16x16x32_bf16 v[16:19], v[192:195], v[132:135], v[16:19]\n\t" \
    "ds_read_b64_tr_b16 v[214:215], v227 offset:4608\n\t" \
    "v_mfma_f32_16x16x32_bf16 v[20:23], v[196:199], v[132:135], v[20:23]\n\t" \
    "ds_read_b64_tr_b16 v[216:217], v228 offset:4096\n\t" \
    "v_mfma_f32_16x16x32_bf16 v[24:27], v[200:203], v[132:135], v[24:27]\n\t" \
    "ds_read_b64_tr_b16 v[218:219], v228 offset:4608\n\t" \
    "v_mfma_f32_16x16x32_bf16 v[28:31], v[204:207], v[132:135], v[28:31]\n\t" \
    "ds_read_b64_tr_b16 v[220:221], v229 offset:4096\n\t" \
    "s_waitcnt lgkmcnt(13)\n\t" \
    "v_mfma_f32_16x16x32_bf16 v[32:35], v[192:195], v[136:139], v[32:35]\n\t" \
    "ds_read_b64_tr_b16 v[222:223], v229 offset:4608\n\t" \
    "v_mfma_f32_16x16x32_bf16 v[36:39], v[196:199], v[136:139], v[36:39]\n\t" \
    "ds_read_b128 v[164:167], v225 offset:2048\n\t" \
    "v_mfma_f32_16x16x32_bf16 v[40:43], v[200:203], v[136:139], v[40:43]\n\t" \
    "ds_read_b128 v[168:171], v225 offset:4096\n\t" \
    "v_mfma_f32_16x16x32_bf16 v[44:47], v[204:207], v[136:139], v[44:47]\n\t" \
    "ds_read_b128 v[172:175], v225 offset:6144\n\t" \
    "s_waitcnt lgkmcnt(15)\n\t" \
    "v_mfma_f32_16x16x32_bf16 v[48:51], v[192:195], v[140:143], v[48:51]\n\t" \
    "ds_read_b128 v[176:179], v225 offset:8192\n\t" \
    "v_mfma_f32_16x16x32_bf16 v[52:55], v[196:199], v[140:143], v[52:55]\n\t" \
    "ds_read_b128 v[180:183], v225 offset:10240\n\t" \
    "v_mfma_f32_16x16x32_bf16 v[56:59], v[200:203], v[140:143], v[56:59]\n\t" \
    "ds_read_b128 v[184:187], v225 offset:12288\n\t" \
    "v_mfma_f32_16x16x32_bf16 v[60:63], v[204:207], v[140:143], v[60:63]\n\t" \
    "ds_read_b128 v[188:191], v225 offset:14336\n\t" \
    "v_mfma_f32_16x16x32_bf16 v[64:67], v[192:195], v[144:147], v[64:67]\n\t" \
    "s_add_u32 s51, s51, 49152\n\t" \
    "v_mfma_f32_16x16x32_bf16 v[68:71], v[196:199], v[144:147], v[68:71]\n\t" \
    "s_cmp_eq_u32 s51, 147456\n\t" \
    "v_mfma_f32_16x16x32_bf16 v[72:75], v[200:203], v[144:147], v[72:75]\n\t" \
    "s_cselect_b32 s50, s53, s54\n\t" \
    "v_mfma_f32_16x16x32_bf16 v[76:79], v[204:207], v[144:147], v[76:79]\n\t" \
    "s_cselect_b32 s51, 0, s51\n\t" \
    "s_waitcnt lgkmcnt(15)\n\t" \
    "v_mfma_f32_16x16x32_bf16 v[80:83], v[192:195], v[148:151], v[80:83]\n\t" \
    "v_add_u32 v224, s50, v224\n\t" \
    "v_mfma_f32_16x16x32_bf16 v[84:87], v[196:199], v[148:151], v[84:87]\n\t" \
    "v_add_u32 v225, s50, v225\n\t" \
    "v_mfma_f32_16x16x32_bf16 v[88:91], v[200:203], v[148:151], v[88:91]\n\t" \
    "v_add_u32 v226, s50, v226\n\t" \
    "v_mfma_f32_16x16x32_bf16 v[92:95], v[204:207], v[148:151], v[92:95]\n\t" \
    "v_add_u32 v227, s50, v227\n\t" \
    "v_mfma_f32_16x16x32_bf16 v[96:99], v[192:195], v[152:155], v[96:99]\n\t" \
    "v_add_u32 v228, s50, v228\n\t" \
    "v_mfma_f32_16x16x32_bf16 v[100:103], v[196:199], v[152:155], v[100:103]\n\t" \
    "v_add_u32 v229, s50, v229\n\t" \
    "v_mfma_f32_16x16x32_bf16 v[104:107], v[200:203], v[152:155], v[104:107]\n\t" \
    "v_mfma_f32_16x16x32_bf16 v[108:111], v[204:207], v[152:155], v[108:111]\n\t" \
    "v_mfma_f32_16x16x32_bf16 v[112:115], v[192:195], v[156:159], v[112:115]\n\t" \
    "v_mfma_f32_16x16x32_bf16 v[116:119], v[196:199], v[156:159], v[116:119]\n\t" \
    "v_mfma_f32_16x16x32_bf16 v[120:123], v[200:203], v[156:159], v[120:123]\n\t" \
    "v_mfma_f32_16x16x32_bf16 v[124:127], v[204:207], v[156:159], v[124:127]\n\t" \
    "s_waitcnt lgkmcnt(0)\n\t" \
    "s_barrier\n\t" \
    "v_mfma_f32_16x16x32_bf16 v[0:3], v[208:211], v[160:163], v[0:3]\n\t" \
    "ds_read_b64_tr_b16 v[192:193], v226 offset:0\n\t" \
    "v_mfma_f32_16x16x32_bf16 v[4:7], v[212:215], v[160:163], v[4:7]\n\t" \
    "ds_read_b64_tr_b16 v[194:195], v226 offset:512\n\t" \
    "v_mfma_f32_16x16x32_bf16 v[8:11], v[216:219], v[160:163], v[8:11]\n\t" \
    "ds_read_b128 v[128:131], v224 offset:0\n\t" \
    "v_mfma_f32_16x16x32_bf16 v[12:15], v[220:223], v[160:163], v[12:15]\n\t" \
    "ds_read_b64_tr_b16 v[196:197], v227 offset:0\n\t" \
    "v_mfma_f32_16x16x32_bf16 v[16:19], v[208:211], v[164:167], v[16:19]\n\t" \
    "ds_read_b64_tr_b16 v[198:199], v227 offset:512\n\t" \
    "v_mfma_f32_16x16x32_bf16 v[20:23], v[212:215], v[164:167], v[20:23]\n\t" \
    "ds_read_b64_tr_b16 v[200:201], v228 offset:0\n\t" \
    "v_mfma_f32_16x16x32_bf16 v[24:27], v[216:219], v[164:167], v[24:27]\n\t" \
    "ds_read_b64_tr_b16 v[202:203], v228 offset:512\n\t" \
    "v_mfma_f32_16x16x32_bf16 v[28:31], v[220:223], v[164:167], v[28:31]\n\t" \
    "ds_read_b64_tr_b16 v[204:205], v229 offset:0\n\t" \
    "v_mfma_f32_16x16x32_bf16 v[32:35], v[208:211], v[168:171], v[32:35]\n\t" \
    "ds_read_b64_tr_b16 v[206:207], v229 offset:512\n\t" \
    "v_mfma_f32_16x16x32_bf16 v[36:39], v[212:215], v[168:171], v[36:39]\n\t" \
    "ds_read_b128 v[132:135], v224 offset:2048\n\t" \
    "v_mfma_f32_16x16x32_bf16 v[40:43], v[216:219], v[168:171], v[40:43]\n\t" \
    "ds_read_b128 v[136:139], v224 offset:4096\n\t" \
    "v_mfma_f32_16x16x32_bf16 v[44:47], v[220:223], v[168:171], v[44:47]\n\t" \
    "ds_read_b128 v[140:143], v224 offset:6144\n\t" \
    "v_mfma_f32_16x16x32_bf16 v[48:51], v[208:211], v[172:175], v[48:51]\n\t" \
    "ds_read_b128 v[144:147], v224 offset:8192\n\t" \
    "v_mfma_f32_16x16x32_bf16 v[52:55], v[212:215], v[172:175], v[52:55]\n\t" \
    "ds_read_b128 v[148:151], v224 offset:10240\n\t" \
    "v_mfma_f32_16x16x32_bf16 v[56:59], v[216:219], v[172:175], v[56:59]\n\t" \
    "ds_read_b128 v[152:155], v224 offset:12288\n\t" \
    "v_mfma_f32_16x16x32_bf16 v[60:63], v[220:223], v[172:175], v[60:63]\n\t" \
    "ds_read_b128 v[156:159], v224 offset:14336\n\t" \
    "v_mfma_f32_16x16x32_bf16 v[64:67], v[208:211], v[176:179], v[64:67]\n\t" \
    "s_add_u32 s70, s76, 3670016\n\t" \
    "s_addc_u32 s71, s77, 0\n\t" \
    "global_load_dwordx4 v[252:255], %[vl16], s[70:71]\n\t" \
    "v_mfma_f32_16x16x32_bf16 v[68:71], v[212:215], v[176:179], v[68:71]\n\t" \
    "v_mfma_f32_16x16x32_bf16 v[72:75], v[216:219], v[176:179], v[72:75]\n\t" \
    "v_mfma_f32_16x16x32_bf16 v[76:79], v[220:223], v[176:179], v[76:79]\n\t" \
    "v_mfma_f32_16x16x32_bf16 v[80:83], v[208:211], v[180:183], v[80:83]\n\t" \
    "v_mfma_f32_16x16x32_bf16 v[84:87], v[212:215], v[180:183], v[84:87]\n\t" \
    "v_mfma_f32_16x16x32_bf16 v[88:91], v[216:219], v[180:183], v[88:91]\n\t" \
    "v_mfma_f32_16x16x32_bf16 v[92:95], v[220:223], v[180:183], v[92:95]\n\t" \
    "v_mfma_f32_16x16x32_bf16 v[96:99], v[208:211], v[184:187], v[96:99]\n\t" \
    "v_mfma_f32_16x16x32_bf16 v[100:103], v[212:215], v[184:187], v[100:103]\n\t" \
    "v_mfma_f32_16x16x32_bf16 v[104:107], v[216:219], v[184:187], v[104:107]\n\t" \
    "v_mfma_f32_16x16x32_bf16 v[108:111], v[220:223], v[184:187], v[108:111]\n\t" \
    "v_mfma_f32_16x16x32_bf16 v[112:115], v[208:211], v[188:191], v[112:115]\n\t" \
    "v_mfma_f32_16x16x32_bf16 v[116:119], v[212:215], v[188:191], v[116:119]\n\t" \
    "v_mfma_f32_16x16x32_bf16 v[120:123], v[216:219], v[188:191], v[120:123]\n\t" \
    "v_mfma_f32_16x16x32_bf16 v[124:127], v[220:223], v[188:191], v[124:127]\n\t" \
    "s_waitcnt lgkmcnt(13)\n\t" \
    "v_mfma_f32_16x16x32_bf16 v[0:3], v[192:195], v[128:131], v[0:3]\n\t" \
    "ds_read_b64_tr_b16 v[208:209], v226 offset:4096\n\t" \
    "s_waitcnt lgkmcnt(12)\n\t" \
    "v_mfma_f32_16x16x32_bf16 v[4:7], v[196:199], v[128:131], v[4:7]\n\t" \
    "ds_read_b64_tr_b16 v[210:211], v226 offset:4608\n\t" \
    "s_waitcnt lgkmcnt(11)\n\t" \
    "v_mfma_f32_16x16x32_bf16 v[8:11], v[200:203], v[128:131], v[8:11]\n\t" \
    "ds_read_b128 v[160:163], v225 offset:0\n\t" \
    "s_waitcnt lgkmcnt(10)\n\t" \
    "v_mfma_f32_16x16x32_bf16 v[12:15], v[204:207], v[128:131], v[12:15]\n\t" \
    "ds_read_b64_tr_b16 v[212:213], v227 offset:4096\n\t" \
    "s_waitcnt lgkmcnt(10)\n\t" \
    "v_mfma_f32_16x16x32_bf16 v[16:19], v[192:195], v[132:135], v[16:19]\n\t" \
    "ds_read_b64_tr_b16 v[214:215], v227 offset:4608\n\t" \
    "v_mfma_f32_16x16x32_bf16 v[20:23], v[196:199], v[132:135], v[20:23]\n\t" \
    "ds_read_b64_tr_b16 v[216:217], v228 offset:4096\n\t" \
    "v_mfma_f32_16x16x32_bf16 v[24:27], v[200:203], v[132:135], v[24:27]\n\t" \
    "ds_read_b64_tr_b16 v[218:219], v228 offset:4608\n\t" \
    "v_mfma_f32_16x16x32_bf16 v[28:31], v[204:207], v[132:135], v[28:31]\n\t" \
    "ds_read_b64_tr_b16 v[220:221], v229 offset:4096\n\t" \
    "s_waitcnt lgkmcnt(13)\n\t" \
    "v_mfma_f32_16x16x32_bf16 v[32:35], v[192:195], v[136:139], v[32:35]\n\t" \
    "ds_read_b64_tr_b16 v[222:223], v229 offset:4608\n\t" \
    "v_mfma_f32_16x16x32_bf16 v[36:39], v[196:199], v[136:139], v[36:39]\n\t" \
    "ds_read_b128 v[164:167], v225 offset:2048\n\t" \
    "v_mfma_f32_16x16x32_bf16 v[40:43], v[200:203], v[136:139], v[40:43]\n\t" \
    "ds_read_b128 v[168:171], v225 offset:4096\n\t" \
    "v_mfma_f32_16x16x32_bf16 v[44:47], v[204:207], v[136:139], v[44:47]\n\t" \
    "ds_read_b128 v[172:175], v225 offset:6144\n\t" \
    "s_waitcnt lgkmcnt(15)\n\t" \
    "v_mfma_f32_16x16x32_bf16 v[48:51], v[192:195], v[140:143], v[48:51]\n\t" \
    "ds_read_b128 v[176:179], v225 offset:8192\n\t" \
    "v_mfma_f32_16x16x32_bf16 v[52:55], v[196:199], v[140:143], v[52:55]\n\t" \
    "ds_read_b128 v[180:183], v225 offset:10240\n\t" \
    "v_mfma_f32_16x16x32_bf16 v[56:59], v[200:203], v[140:143], v[56:59]\n\t" \
    "ds_read_b128 v[184:187], v225 offset:12288\n\t" \
    "v_mfma_f32_16x16x32_bf16 v[60:63], v[204:207], v[140:143], v[60:63]\n\t" \
    "ds_read_b128 v[188:191], v225 offset:14336\n\t" \
    "v_mfma_f32_16x16x32_bf16 v[64:67], v[192:195], v[144:147], v[64:67]\n\t" \
    "s_add_u32 s51, s51, 49152\n\t" \
    "v_mfma_f32_16x16x32_bf16 v[68:71], v[196:199], v[144:147], v[68:71]\n\t" \
    "s_cmp_eq_u32 s51, 147456\n\t" \
    "v_mfma_f32_16x16x32_bf16 v[72:75], v[200:203], v[144:147], v[72:75]\n\t" \
    "s_cselect_b32 s50, s53, s54\n\t" \
    "v_mfma_f32_16x16x32_bf16 v[76:79], v[204:207], v[144:147], v[76:79]\n\t" \
    "s_cselect_b32 s51, 0, s51\n\t" \
    "s_waitcnt lgkmcnt(15)\n\t" \
    "v_mfma_f32_16x16x32_bf16 v[80:83], v[192:195], v[148:151], v[80:83]\n\t" \
    "v_add_u32 v224, s50, v224\n\t" \
    "v_mfma_f32_16x16x32_bf16 v[84:87], v[196:199], v[148:151], v[84:87]\n\t" \
    "v_add_u32 v225, s50, v225\n\t" \
    "v_mfma_f32_16x16x32_bf16 v[88:91], v[200:203], v[148:151], v[88:91]\n\t" \
    "v_add_u32 v226, s50, v226\n\t" \
    "v_mfma_f32_16x16x32_bf16 v[92:95], v[204:207], v[148:151], v[92:95]\n\t" \
    "v_add_u32 v227, s50, v227\n\t" \
    "v_mfma_f32_16x16x32_bf16 v[96:99], v[192:195], v[152:155], v[96:99]\n\t" \
    "v_add_u32 v228, s50, v228\n\t" \
    "v_mfma_f32_16x16x32_bf16 v[100:103], v[196:199], v[152:155], v[100:103]\n\t" \
    "v_add_u32 v229, s50, v229\n\t" \
    "v_mfma_f32_16x16x32_bf16 v[104:107], v[200:203], v[152:155], v[104:107]\n\t" \
    "v_mfma_f32_16x16x32_bf16 v[108:111], v[204:207], v[152:155], v[108:111]\n\t" \
    "v_mfma_f32_16x16x32_bf16 v[112:115], v[192:195], v[156:159], v[112:115]\n\t" \
    "v_mfma_f32_16x16x32_bf16 v[116:119], v[196:199], v[156:159], v[116:119]\n\t" \
    "v_mfma_f32_16x16x32_bf16 v[120:123], v[200:203], v[156:159], v[120:123]\n\t" \
    "v_mfma_f32_16x16x32_bf16 v[124:127], v[204:207], v[156:159], v[124:127]\n\t" \
    "s_waitcnt lgkmcnt(0)\n\t" \
    "s_barrier\n\t" \
    "v_mfma_f32_16x16x32_bf16 v[0:3], v[208:211], v[160:163], v[0:3]\n\t" \
    "ds_read_b64_tr_b16 v[192:193], v226 offset:0\n\t" \
    "v_mfma_f32_16x16x32_bf16 v[4:7], v[212:215], v[160:163], v[4:7]\n\t" \
    "ds_read_b64_tr_b16 v[194:195], v226 offset:512\n\t" \
    "v_mfma_f32_16x16x32_bf16 v[8:11], v[216:219], v[160:163], v[8:11]\n\t" \
    "ds_read_b128 v[128:131], v224 offset:0\n\t" \
    "v_mfma_f32_16x16x32_bf16 v[12:15], v[220:223], v[160:163], v[12:15]\n\t" \
    "ds_read_b64_tr_b16 v[196:197], v227 offset:0\n\t" \
    "v_mfma_f32_16x16x32_bf16 v[16:19], v[208:211], v[164:167], v[16:19]\n\t" \
    "ds_read_b64_tr_b16 v[198:199], v227 offset:512\n\t" \
    "v_mfma_f32_16x16x32_bf16 v[20:23], v[212:215], v[164:167], v[20:23]\n\t" \
    "ds_read_b64_tr_b16 v[200:201], v228 offset:0\n\t" \
    "v_mfma_f32_16x16x32_bf16 v[24:27], v[216:219], v[164:167], v[24:27]\n\t" \
    "ds_read_b64_tr_b16 v[202:203], v228 offset:512\n\t" \
    "v_mfma_f32_16x16x32_bf16 v[28:31], v[220:223], v[164:167], v[28:31]\n\t" \
    "ds_read_b64_tr_b16 v[204:205], v229 offset:0\n\t" \
    "v_mfma_f32_16x16x32_bf16 v[32:35], v[208:211], v[168:171], v[32:35]\n\t" \
    "ds_read_b64_tr_b16 v[206:207], v229 offset:512\n\t" \
    "v_mfma_f32_16x16x32_bf16 v[36:39], v[212:215], v[168:171], v[36:39]\n\t" \
    "ds_read_b128 v[132:135], v224 offset:2048\n\t" \
    "v_mfma_f32_16x16x32_bf16 v[40:43], v[216:219], v[168:171], v[40:43]\n\t" \
    "ds_read_b128 v[136:139], v224 offset:4096\n\t" \
    "v_mfma_f32_16x16x32_bf16 v[44:47], v[220:223], v[168:171], v[44:47]\n\t" \
    "ds_read_b128 v[140:143], v224 offset:6144\n\t" \
    "v_mfma_f32_16x16x32_bf16 v[48:51], v[208:211], v[172:175], v[48:51]\n\t" \
    "ds_read_b128 v[144:147], v224 offset:8192\n\t" \
    "v_mfma_f32_16x16x32_bf16 v[52:55], v[212:215], v[172:175], v[52:55]\n\t" \
    "ds_read_b128 v[148:151], v224 offset:10240\n\t" \
    "v_mfma_f32_16x16x32_bf16 v[56:59], v[216:219], v[172:175], v[56:59]\n\t" \
    "ds_read_b128 v[152:155], v224 offset:12288\n\t" \
    "v_mfma_f32_16x16x32_bf16 v[60:63], v[220:223], v[172:175], v[60:63]\n\t" \
    "ds_read_b128 v[156:159], v224 offset:14336\n\t" \
    "v_mfma_f32_16x16x32_bf16 v[64:67], v[208:211], v[176:179], v[64:67]\n\t" \
    "v_mfma_f32_16x16x32_bf16 v[68:71], v[212:215], v[176:179], v[68:71]\n\t" \
    "v_mfma_f32_16x16x32_bf16 v[72:75], v[216:219], v[176:179], v[72:75]\n\t" \
    "v_mfma_f32_16x16x32_bf16 v[76:79], v[220:223], v[176:179], v[76:79]\n\t" \
    "v_mfma_f32_16x16x32_bf16 v[80:83], v[208:211], v[180:183], v[80:83]\n\t" \
    "v_mfma_f32_16x16x32_bf16 v[84:87], v[212:215], v[180:183], v[84:87]\n\t" \
    "v_mfma_f32_16x16x32_bf16 v[88:91], v[216:219], v[180:183], v[88:91]\n\t" \
    "v_mfma_f32_16x16x32_bf16 v[92:95], v[220:223], v[180:183], v[92:95]\n\t" \
    "v_mfma_f32_16x16x32_bf16 v[96:99], v[208:211], v[184:187], v[96:99]\n\t" \
    "v_mfma_f32_16x16x32_bf16 v[100:103], v[212:215], v[184:187], v[100:103]\n\t" \
    "v_mfma_f32_16x16x32_bf16 v[104:107], v[216:219], v[184:187], v[104:107]\n\t" \
    "v_mfma_f32_16x16x32_bf16 v[108:111], v[220:223], v[184:187], v[108:111]\n\t" \
    "v_mfma_f32_16x16x32_bf16 v[112:115], v[208:211], v[188:191], v[112:115]\n\t" \
    "v_mfma_f32_16x16x32_bf16 v[116:119], v[212:215], v[188:191], v[116:119]\n\t" \
    "v_mfma_f32_16x16x32_bf16 v[120:123], v[216:219], v[188:191], v[120:123]\n\t" \
    "v_mfma_f32_16x16x32_bf16 v[124:127], v[220:223], v[188:191], v[124:127]\n\t" \
    "s_waitcnt lgkmcnt(13)\n\t" \
    "v_mfma_f32_16x16x32_bf16 v[0:3], v[192:195], v[128:131], v[0:3]\n\t" \
    "ds_read_b64_tr_b16 v[208:209], v226 offset:4096\n\t" \
    "s_waitcnt lgkmcnt(12)\n\t" \
    "v_mfma_f32_16x16x32_bf16 v[4:7], v[196:199], v[128:131], v[4:7]\n\t" \
    "ds_read_b64_tr_b16 v[210:211], v226 offset:4608\n\t" \
    "s_waitcnt lgkmcnt(11)\n\t" \
    "v_mfma_f32_16x16x32_bf16 v[8:11], v[200:203], v[128:131], v[8:11]\n\t" \
    "ds_read_b128 v[160:163], v225 offset:0\n\t" \
    "s_waitcnt lgkmcnt(10)\n\t" \
    "v_mfma_f32_16x16x32_bf16 v[12:15], v[204:207], v[128:131], v[12:15]\n\t" \
    "ds_read_b64_tr_b16 v[212:213], v227 offset:4096\n\t" \
    "s_waitcnt lgkmcnt(10)\n\t" \
    "v_mfma_f32_16x16x32_bf16 v[16:19], v[192:195], v[132:135], v[16:19]\n\t" \
    "ds_read_b64_tr_b16 v[214:215], v227 offset:4608\n\t" \
    "v_mfma_f32_16x16x32_bf16 v[20:23], v[196:199], v[132:135], v[20:23]\n\t" \
    "ds_read_b64_tr_b16 v[216:217], v228 offset:4096\n\t" \
    "v_mfma_f32_16x16x32_bf16 v[24:27], v[200:203], v[132:135], v[24:27]\n\t" \
    "ds_read_b64_tr_b16 v[218:219], v228 offset:4608\n\t" \
    "v_mfma_f32_16x16x32_bf16 v[28:31], v[204:207], v[132:135], v[28:31]\n\t" \
    "ds_read_b64_tr_b16 v[220:221], v229 offset:4096\n\t" \
    "s_waitcnt lgkmcnt(13)\n\t" \
    "v_mfma_f32_16x16x32_bf16 v[32:35], v[192:195], v[136:139], v[32:35]\n\t" \
    "ds_read_b64_tr_b16 v[222:223], v229 offset:4608\n\t" \
    "v_mfma_f32_16x16x32_bf16 v[36:39], v[196:199], v[136:139], v[36:39]\n\t" \
    "ds_read_b128 v[164:167], v225 offset:2048\n\t" \
    "v_mfma_f32_16x16x32_bf16 v[40:43], v[200:203], v[136:139], v[40:43]\n\t" \
    "ds_read_b128 v[168:171], v225 offset:4096\n\t" \
    "v_mfma_f32_16x16x32_bf16 v[44:47], v[204:207], v[136:139], v[44:47]\n\t" \
    "ds_read_b128 v[172:175], v225 offset:6144\n\t" \
    "s_waitcnt lgkmcnt(15)\n\t" \
    "v_mfma_f32_16x16x32_bf16 v[48:51], v[192:195], v[140:143], v[48:51]\n\t" \
    "ds_read_b128 v[176:179], v225 offset:8192\n\t" \
    "v_mfma_f32_16x16x32_bf16 v[52:55], v[196:199], v[140:143], v[52:55]\n\t" \
    "ds_read_b128 v[180:183], v225 offset:10240\n\t" \
    "v_mfma_f32_16x16x32_bf16 v[56:59], v[200:203], v[140:143], v[56:59]\n\t" \
    "ds_read_b128 v[184:187], v225 offset:12288\n\t" \
    "v_mfma_f32_16x16x32_bf16 v[60:63], v[204:207], v[140:143], v[60:63]\n\t" \
    "ds_read_b128 v[188:191], v225 offset:14336\n\t" \
    "v_mfma_f32_16x16x32_bf16 v[64:67], v[192:195], v[144:147], v[64:67]\n\t" \
    "s_add_u32 s51, s51, 49152\n\t" \
    "v_mfma_f32_16x16x32_bf16 v[68:71], v[196:199], v[144:147], v[68:71]\n\t" \
    "s_cmp_eq_u32 s51, 147456\n\t" \
    "v_mfma_f32_16x16x32_bf16 v[72:75], v[200:203], v[144:147], v[72:75]\n\t" \
    "s_cselect_b32 s50, s53, s54\n\t" \
    "v_mfma_f32_16x16x32_bf16 v[76:79], v[204:207], v[144:147], v[76:79]\n\t" \
    "s_cselect_b32 s51, 0, s51\n\t" \
    "s_waitcnt lgkmcnt(15)\n\t" \
    "v_mfma_f32_16x16x32_bf16 v[80:83], v[192:195], v[148:151], v[80:83]\n\t" \
    "v_add_u32 v224, s50, v224\n\t" \
    "v_mfma_f32_16x16x32_bf16 v[84:87], v[196:199], v[148:151], v[84:87]\n\t" \
    "v_add_u32 v225, s50, v225\n\t" \
    "v_mfma_f32_16x16x32_bf16 v[88:91], v[200:203], v[148:151], v[88:91]\n\t" \
    "v_add_u32 v226, s50, v226\n\t" \
    "v_mfma_f32_16x16x32_bf16 v[92:95], v[204:207], v[148:151], v[92:95]\n\t" \
    "v_add_u32 v227, s50, v227\n\t" \
    "v_mfma_f32_16x16x32_bf16 v[96:99], v[192:195], v[152:155], v[96:99]\n\t" \
    "v_add_u32 v228, s50, v228\n\t" \
    "v_mfma_f32_16x16x32_bf16 v[100:103], v[196:199], v[152:155], v[100:103]\n\t" \
    "v_add_u32 v229, s50, v229\n\t" \
    "v_mfma_f32_16x16x32_bf16 v[104:107], v[200:203], v[152:155], v[104:107]\n\t" \
    "v_mfma_f32_16x16x32_bf16 v[108:111], v[204:207], v[152:155], v[108:111]\n\t" \
    "v_mfma_f32_16x16x32_bf16 v[112:115], v[192:195], v[156:159], v[112:115]\n\t" \
    "v_mfma_f32_16x16x32_bf16 v[116:119], v[196:199], v[156:159], v[116:119]\n\t" \
    "v_mfma_f32_16x16x32_bf16 v[120:123], v[200:203], v[156:159], v[120:123]\n\t" \
    "v_mfma_f32_16x16x32_bf16 v[124:127], v[204:207], v[156:159], v[124:127]\n\t" \
    "s_waitcnt lgkmcnt(0)\n\t" \
    "s_barrier\n\t" \
    "v_mfma_f32_16x16x32_bf16 v[0:3], v[208:211], v[160:163], v[0:3]\n\t" \
    "ds_read_b64_tr_b16 v[192:193], v226 offset:0\n\t" \
    "v_mfma_f32_16x16x32_bf16 v[4:7], v[212:215], v[160:163], v[4:7]\n\t" \
    "ds_read_b64_tr_b16 v[194:195], v226 offset:512\n\t" \
    "v_mfma_f32_16x16x32_bf16 v[8:11], v[216:219], v[160:163], v[8:11]\n\t" \
    "ds_read_b128 v[128:131], v224 offset:0\n\t" \
    "v_mfma_f32_16x16x32_bf16 v[12:15], v[220:223], v[160:163], v[12:15]\n\t" \
    "ds_read_b64_tr_b16 v[196:197], v227 offset:0\n\t" \
    "v_mfma_f32_16x16x32_bf16 v[16:19], v[208:211], v[164:167], v[16:19]\n\t" \
    "ds_read_b64_tr_b16 v[198:199], v227 offset:512\n\t" \
    "v_mfma_f32_16x16x32_bf16 v[20:23], v[212:215], v[164:167], v[20:23]\n\t" \
    "ds_read_b64_tr_b16 v[200:201], v228 offset:0\n\t" \
    "v_mfma_f32_16x16x32_bf16 v[24:27], v[216:219], v[164:167], v[24:27]\n\t" \
    "ds_read_b64_tr_b16 v[202:203], v228 offset:512\n\t" \
    "v_mfma_f32_16x16x32_bf16 v[28:31], v[220:223], v[164:167], v[28:31]\n\t" \
    "ds_read_b64_tr_b16 v[204:205], v229 offset:0\n\t" \
    "v_mfma_f32_16x16x32_bf16 v[32:35], v[208:211], v[168:171], v[32:35]\n\t" \
    "ds_read_b64_tr_b16 v[206:207], v229 offset:512\n\t" \
    "v_mfma_f32_16x16x32_bf16 v[36:39], v[212:215], v[168:171], v[36:39]\n\t" \
    "ds_read_b128 v[132:135], v224 offset:2048\n\t" \
    "v_mfma_f32_16x16x32_bf16 v[40:43], v[216:219], v[168:171], v[40:43]\n\t" \
    "ds_read_b128 v[136:139], v224 offset:4096\n\t" \
    "v_mfma_f32_16x16x32_bf16 v[44:47], v[220:223], v[168:171], v[44:47]\n\t" \
    "ds_read_b128 v[140:143], v224 offset:6144\n\t" \
    "v_mfma_f32_16x16x32_bf16 v[48:51], v[208:211], v[172:175], v[48:51]\n\t" \
    "ds_read_b128 v[144:147], v224 offset:8192\n\t" \
    "v_mfma_f32_16x16x32_bf16 v[52:55], v[212:215], v[172:175], v[52:55]\n\t" \
    "ds_read_b128 v[148:151], v224 offset:10240\n\t" \
    "v_mfma_f32_16x16x32_bf16 v[56:59], v[216:219], v[172:175], v[56:59]\n\t" \
    "ds_read_b128 v[152:155], v224 offset:12288\n\t" \
    "v_mfma_f32_16x16x32_bf16 v[60:63], v[220:223], v[172:175], v[60:63]\n\t" \
    "ds_read_b128 v[156:159], v224 offset:14336\n\t" \
    "v_mfma_f32_16x16x32_bf16 v[64:67], v[208:211], v[176:179], v[64:67]\n\t" \
    "v_mfma_f32_16x16x32_bf16 v[68:71], v[212:215], v[176:179], v[68:71]\n\t" \
    "v_mfma_f32_16x16x32_bf16 v[72:75], v[216:219], v[176:179], v[72:75]\n\t" \
    "v_mfma_f32_16x16x32_bf16 v[76:79], v[220:223], v[176:179], v[76:79]\n\t" \
    "v_mfma_f32_16x16x32_bf16 v[80:83], v[208:211], v[180:183], v[80:83]\n\t" \
    "v_mfma_f32_16x16x32_bf16 v[84:87], v[212:215], v[180:183], v[84:87]\n\t" \
    "v_mfma_f32_16x16x32_bf16 v[88:91], v[216:219], v[180:183], v[88:91]\n\t" \
    "v_mfma_f32_16x16x32_bf16 v[92:95], v[220:223], v[180:183], v[92:95]\n\t" \
    "v_mfma_f32_16x16x32_bf16 v[96:99], v[208:211], v[184:187], v[96:99]\n\t" \
    "v_mfma_f32_16x16x32_bf16 v[100:103], v[212:215], v[184:187], v[100:103]\n\t" \
    "v_mfma_f32_16x16x32_bf16 v[104:107], v[216:219], v[184:187], v[104:107]\n\t" \
    "v_mfma_f32_16x16x32_bf16 v[108:111], v[220:223], v[184:187], v[108:111]\n\t" \
    "v_mfma_f32_16x16x32_bf16 v[112:115], v[208:211], v[188:191], v[112:115]\n\t" \
    "v_mfma_f32_16x16x32_bf16 v[116:119], v[212:215], v[188:191], v[116:119]\n\t" \
    "v_mfma_f32_16x16x32_bf16 v[120:123], v[216:219], v[188:191], v[120:123]\n\t" \
    "v_mfma_f32_16x16x32_bf16 v[124:127], v[220:223], v[188:191], v[124:127]\n\t" \
    "s_waitcnt lgkmcnt(13)\n\t" \
    "v_mfma_f32_16x16x32_bf16 v[0:3], v[192:195], v[128:131], v[0:3]\n\t" \
    "ds_read_b64_tr_b16 v[208:209], v226 offset:4096\n\t" \
    "s_waitcnt lgkmcnt(12)\n\t" \
    "v_mfma_f32_16x16x32_bf16 v[4:7], v[196:199], v[128:131], v[4:7]\n\t" \
    "ds_read_b64_tr_b16 v[210:211], v226 offset:4608\n\t" \
    "s_waitcnt lgkmcnt(11)\n\t" \
    "v_mfma_f32_16x16x32_bf16 v[8:11], v[200:203], v[128:131], v[8:11]\n\t" \
    "ds_read_b128 v[160:163], v225 offset:0\n\t" \
    "s_waitcnt lgkmcnt(10)\n\t" \
    "v_mfma_f32_16x16x32_bf16 v[12:15], v[204:207], v[128:131], v[12:15]\n\t" \
    "ds_read_b64_tr_b16 v[212:213], v227 offset:4096\n\t" \
    "s_waitcnt lgkmcnt(10)\n\t" \
    "v_mfma_f32_16x16x32_bf16 v[16:19], v[192:195], v[132:135], v[16:19]\n\t" \
    "ds_read_b64_tr_b16 v[214:215], v227 offset:4608\n\t" \
    "v_mfma_f32_16x16x32_bf16 v[20:23], v[196:199], v[132:135], v[20:23]\n\t" \
    "ds_read_b64_tr_b16 v[216:217], v228 offset:4096\n\t" \
    "v_mfma_f32_16x16x32_bf16 v[24:27], v[200:203], v[132:135], v[24:27]\n\t" \
    "ds_read_b64_tr_b16 v[218:219], v228 offset:4608\n\t" \
    "v_mfma_f32_16x16x32_bf16 v[28:31], v[204:207], v[132:135], v[28:31]\n\t" \
    "ds_read_b64_tr_b16 v[220:221], v229 offset:4096\n\t" \
    "s_waitcnt lgkmcnt(13)\n\t" \
    "v_mfma_f32_16x16x32_bf16 v[32:35], v[192:195], v[136:139], v[32:35]\n\t" \
    "ds_read_b64_tr_b16 v[222:223], v229 offset:4608\n\t" \
    "v_mfma_f32_16x16x32_bf16 v[36:39], v[196:199], v[136:139], v[36:39]\n\t" \
    "ds_read_b128 v[164:167], v225 offset:2048\n\t" \
    "v_mfma_f32_16x16x32_bf16 v[40:43], v[200:203], v[136:139], v[40:43]\n\t" \
    "ds_read_b128 v[168:171], v225 offset:4096\n\t" \
    "v_mfma_f32_16x16x32_bf16 v[44:47], v[204:207], v[136:139], v[44:47]\n\t" \
    "ds_read_b128 v[172:175], v225 offset:6144\n\t" \
    "s_waitcnt lgkmcnt(15)\n\t" \
    "v_mfma_f32_16x16x32_bf16 v[48:51], v[192:195], v[140:143], v[48:51]\n\t" \
    "ds_read_b128 v[176:179], v225 offset:8192\n\t" \
    "v_mfma_f32_16x16x32_bf16 v[52:55], v[196:199], v[140:143], v[52:55]\n\t" \
    "ds_read_b128 v[180:183], v225 offset:10240\n\t" \
    "v_mfma_f32_16x16x32_bf16 v[56:59], v[200:203], v[140:143], v[56:59]\n\t" \
    "ds_read_b128 v[184:187], v225 offset:12288\n\t" \
    "v_mfma_f32_16x16x32_bf16 v[60:63], v[204:207], v[140:143], v[60:63]\n\t" \
    "ds_read_b128 v[188:191], v225 offset:14336\n\t" \
    "v_mfma_f32_16x16x32_bf16 v[64:67], v[192:195], v[144:147], v[64:67]\n\t" \
    "s_add_u32 s51, s51, 49152\n\t" \
    "v_mfma_f32_16x16x32_bf16 v[68:71], v[196:199], v[144:147], v[68:71]\n\t" \
    "s_cmp_eq_u32 s51, 147456\n\t" \
    "v_mfma_f32_16x16x32_bf16 v[72:75], v[200:203], v[144:147], v[72:75]\n\t" \
    "s_cselect_b32 s50, s53, s54\n\t" \
    "v_mfma_f32_16x16x32_bf16 v[76:79], v[204:207], v[144:147], v[76:79]\n\t" \
    "s_cselect_b32 s51, 0, s51\n\t" \
    "s_waitcnt lgkmcnt(15)\n\t" \
    "v_mfma_f32_16x16x32_bf16 v[80:83], v[192:195], v[148:151], v[80:83]\n\t" \
    "v_add_u32 v224, s50, v224\n\t" \
    "v_mfma_f32_16x16x32_bf16 v[84:87], v[196:199], v[148:151], v[84:87]\n\t" \
    "v_add_u32 v225, s50, v225\n\t" \
    "v_mfma_f32_16x16x32_bf16 v[88:91], v[200:203], v[148:151], v[88:91]\n\t" \
    "v_add_u32 v226, s50, v226\n\t" \
    "v_mfma_f32_16x16x32_bf16 v[92:95], v[204:207], v[148:151], v[92:95]\n\t" \
    "v_add_u32 v227, s50, v227\n\t" \
    "v_mfma_f32_16x16x32_bf16 v[96:99], v[192:195], v[152:155], v[96:99]\n\t" \
    "v_add_u32 v228, s50, v228\n\t" \
    "v_mfma_f32_16x16x32_bf16 v[100:103], v[196:199], v[152:155], v[100:103]\n\t" \
    "v_add_u32 v229, s50, v229\n\t" \
    "v_mfma_f32_16x16x32_bf16 v[104:107], v[200:203], v[152:155], v[104:107]\n\t" \
    "v_mfma_f32_16x16x32_bf16 v[108:111], v[204:207], v[152:155], v[108:111]\n\t" \
    "v_mfma_f32_16x16x32_bf16 v[112:115], v[192:195], v[156:159], v[112:115]\n\t" \
    "v_mfma_f32_16x16x32_bf16 v[116:119], v[196:199], v[156:159], v[116:119]\n\t" \
    "v_mfma_f32_16x16x32_bf16 v[120:123], v[200:203], v[156:159], v[120:123]\n\t" \
    "v_mfma_f32_16x16x32_bf16 v[124:127], v[204:207], v[156:159], v[124:127]\n\t" \
    "s_waitcnt lgkmcnt(0)\n\t" \
    "s_barrier\n\t" \
    "v_mfma_f32_16x16x32_bf16 v[0:3], v[208:211], v[160:163], v[0:3]\n\t" \
    "ds_read_b64_tr_b16 v[192:193], v226 offset:0\n\t" \
    "v_mfma_f32_16x16x32_bf16 v[4:7], v[212:215], v[160:163], v[4:7]\n\t" \
    "ds_read_b64_tr_b16 v[194:195], v226 offset:512\n\t" \
    "v_mfma_f32_16x16x32_bf16 v[8:11], v[216:219], v[160:163], v[8:11]\n\t" \
    "ds_read_b128 v[128:131], v224 offset:0\n\t" \
    "v_mfma_f32_16x16x32_bf16 v[12:15], v[220:223], v[160:163], v[12:15]\n\t" \
    "ds_read_b64_tr_b16 v[196:197], v227 offset:0\n\t" \
    "v_mfma_f32_16x16x32_bf16 v[16:19], v[208:211], v[164:167], v[16:19]\n\t" \
    "ds_read_b64_tr_b16 v[198:199], v227 offset:512\n\t" \
    "v_mfma_f32_16x16x32_bf16 v[20:23], v[212:215], v[164:167], v[20:23]\n\t" \
    "ds_read_b64_tr_b16 v[200:201], v228 offset:0\n\t" \
    "v_mfma_f32_16x16x32_bf16 v[24:27], v[216:219], v[164:167], v[24:27]\n\t" \
    "ds_read_b64_tr_b16 v[202:203], v228 offset:512\n\t" \
    "v_mfma_f32_16x16x32_bf16 v[28:31], v[220:223], v[164:167], v[28:31]\n\t" \
    "ds_read_b64_tr_b16 v[204:205], v229 offset:0\n\t" \
    "v_mfma_f32_16x16x32_bf16 v[32:35], v[208:211], v[168:171], v[32:35]\n\t" \
    "ds_read_b64_tr_b16 v[206:207], v229 offset:512\n\t" \
    "v_mfma_f32_16x16x32_bf16 v[36:39], v[212:215], v[168:171], v[36:39]\n\t" \
    "ds_read_b128 v[132:135], v224 offset:2048\n\t" \
    "v_mfma_f32_16x16x32_bf16 v[40:43], v[216:219], v[168:171], v[40:43]\n\t" \
    "ds_read_b128 v[136:139], v224 offset:4096\n\t" \
    "v_mfma_f32_16x16x32_bf16 v[44:47], v[220:223], v[168:171], v[44:47]\n\t" \
    "ds_read_b128 v[140:143], v224 offset:6144\n\t" \
    "v_mfma_f32_16x16x32_bf16 v[48:51], v[208:211], v[172:175], v[48:51]\n\t" \
    "ds_read_b128 v[144:147], v224 offset:8192\n\t" \
    "v_mfma_f32_16x16x32_bf16 v[52:55], v[212:215], v[172:175], v[52:55]\n\t" \
    "ds_read_b128 v[148:151], v224 offset:10240\n\t" \
    "v_mfma_f32_16x16x32_bf16 v[56:59], v[216:219], v[172:175], v[56:59]\n\t" \
    "ds_read_b128 v[152:155], v224 offset:12288\n\t" \
    "v_mfma_f32_16x16x32_bf16 v[60:63], v[220:223], v[172:175], v[60:63]\n\t" \
    "ds_read_b128 v[156:159], v224 offset:14336\n\t" \
    "v_mfma_f32_16x16x32_bf16 v[64:67], v[208:211], v[176:179], v[64:67]\n\t" \
    "v_mfma_f32_16x16x32_bf16 v[68:71], v[212:215], v[176:179], v[68:71]\n\t" \
    "v_mfma_f32_16x16x32_bf16 v[72:75], v[216:219], v[176:179], v[72:75]\n\t" \
    "v_mfma_f32_16x16x32_bf16 v[76:79], v[220:223], v[176:179], v[76:79]\n\t" \
    "v_mfma_f32_16x16x32_bf16 v[80:83], v[208:211], v[180:183], v[80:83]\n\t" \
    "v_mfma_f32_16x16x32_bf16 v[84:87], v[212:215], v[180:183], v[84:87]\n\t" \
    "v_mfma_f32_16x16x32_bf16 v[88:91], v[216:219], v[180:183], v[88:91]\n\t" \
    "v_mfma_f32_16x16x32_bf16 v[92:95], v[220:223], v[180:183], v[92:95]\n\t" \
    "v_mfma_f32_16x16x32_bf16 v[96:99], v[208:211], v[184:187], v[96:99]\n\t" \
    "v_mfma_f32_16x16x32_bf16 v[100:103], v[212:215], v[184:187], v[100:103]\n\t" \
    "v_mfma_f32_16x16x32_bf16 v[104:107], v[216:219], v[184:187], v[104:107]\n\t" \
    "v_mfma_f32_16x16x32_bf16 v[108:111], v[220:223], v[184:187], v[108:111]\n\t" \
    "v_mfma_f32_16x16x32_bf16 v[112:115], v[208:211], v[188:191], v[112:115]\n\t" \
    "v_mfma_f32_16x16x32_bf16 v[116:119], v[212:215], v[188:191], v[116:119]\n\t" \
    "v_mfma_f32_16x16x32_bf16 v[120:123], v[216:219], v[188:191], v[120:123]\n\t" \
    "v_mfma_f32_16x16x32_bf16 v[124:127], v[220:223], v[188:191], v[124:127]\n\t" \
    "s_waitcnt lgkmcnt(13)\n\t" \
    "v_mfma_f32_16x16x32_bf16 v[0:3], v[192:195], v[128:131], v[0:3]\n\t" \
    "ds_read_b64_tr_b16 v[208:209], v226 offset:4096\n\t" \
    "s_waitcnt lgkmcnt(12)\n\t" \
    "v_mfma_f32_16x16x32_bf16 v[4:7], v[196:199], v[128:131], v[4:7]\n\t" \
    "ds_read_b64_tr_b16 v[210:211], v226 offset:4608\n\t" \
    "s_waitcnt lgkmcnt(11)\n\t" \
    "v_mfma_f32_16x16x32_bf16 v[8:11], v[200:203], v[128:131], v[8:11]\n\t" \
    "ds_read_b128 v[160:163], v225 offset:0\n\t" \
    "s_waitcnt lgkmcnt(10)\n\t" \
    "v_mfma_f32_16x16x32_bf16 v[12:15], v[204:207], v[128:131], v[12:15]\n\t" \
    "ds_read_b64_tr_b16 v[212:213], v227 offset:4096\n\t" \
    "s_waitcnt lgkmcnt(10)\n\t" \
    "v_mfma_f32_16x16x32_bf16 v[16:19], v[192:195], v[132:135], v[16:19]\n\t" \
    "ds_read_b64_tr_b16 v[214:215], v227 offset:4608\n\t" \
    "v_mfma_f32_16x16x32_bf16 v[20:23], v[196:199], v[132:135], v[20:23]\n\t" \
    "ds_read_b64_tr_b16 v[216:217], v228 offset:4096\n\t" \
    "v_mfma_f32_16x16x32_bf16 v[24:27], v[200:203], v[132:135], v[24:27]\n\t" \
    "ds_read_b64_tr_b16 v[218:219], v228 offset:4608\n\t" \
    "v_mfma_f32_16x16x32_bf16 v[28:31], v[204:207], v[132:135], v[28:31]\n\t" \
    "ds_read_b64_tr_b16 v[220:221], v229 offset:4096\n\t" \
    "s_waitcnt lgkmcnt(13)\n\t" \
    "v_mfma_f32_16x16x32_bf16 v[32:35], v[192:195], v[136:139], v[32:35]\n\t" \
    "ds_read_b64_tr_b16 v[222:223], v229 offset:4608\n\t" \
    "v_mfma_f32_16x16x32_bf16 v[36:39], v[196:199], v[136:139], v[36:39]\n\t" \
    "ds_read_b128 v[164:167], v225 offset:2048\n\t" \
    "v_mfma_f32_16x16x32_bf16 v[40:43], v[200:203], v[136:139], v[40:43]\n\t" \
    "ds_read_b128 v[168:171], v225 offset:4096\n\t" \
    "v_mfma_f32_16x16x32_bf16 v[44:47], v[204:207], v[136:139], v[44:47]\n\t" \
    "ds_read_b128 v[172:175], v225 offset:6144\n\t" \
    "s_waitcnt lgkmcnt(15)\n\t" \
    "v_mfma_f32_16x16x32_bf16 v[48:51], v[192:195], v[140:143], v[48:51]\n\t" \
    "ds_read_b128 v[176:179], v225 offset:8192\n\t" \
    "v_mfma_f32_16x16x32_bf16 v[52:55], v[196:199], v[140:143], v[52:55]\n\t" \
    "ds_read_b128 v[180:183], v225 offset:10240\n\t" \
    "v_mfma_f32_16x16x32_bf16 v[56:59], v[200:203], v[140:143], v[56:59]\n\t" \
    "ds_read_b128 v[184:187], v225 offset:12288\n\t" \
    "v_mfma_f32_16x16x32_bf16 v[60:63], v[204:207], v[140:143], v[60:63]\n\t" \
    "ds_read_b128 v[188:191], v225 offset:14336\n\t" \
    "v_mfma_f32_16x16x32_bf16 v[64:67], v[192:195], v[144:147], v[64:67]\n\t" \
    "s_add_u32 s51, s51, 49152\n\t" \
    "v_mfma_f32_16x16x32_bf16 v[68:71], v[196:199], v[144:147], v[68:71]\n\t" \
    "s_cmp_eq_u32 s51, 147456\n\t" \
    "v_mfma_f32_16x16x32_bf16 v[72:75], v[200:203], v[144:147], v[72:75]\n\t" \
    "s_cselect_b32 s50, s53, s54\n\t" \
    "v_mfma_f32_16x16x32_bf16 v[76:79], v[204:207], v[144:147], v[76:79]\n\t" \
    "s_cselect_b32 s51, 0, s51\n\t" \
    "s_waitcnt lgkmcnt(15)\n\t" \
    "v_mfma_f32_16x16x32_bf16 v[80:83], v[192:195], v[148:151], v[80:83]\n\t" \
    "v_add_u32 v224, s50, v224\n\t" \
    "v_mfma_f32_16x16x32_bf16 v[84:87], v[196:199], v[148:151], v[84:87]\n\t" \
    "v_add_u32 v225, s50, v225\n\t" \
    "v_mfma_f32_16x16x32_bf16 v[88:91], v[200:203], v[148:151], v[88:91]\n\t" \
    "v_add_u32 v226, s50, v226\n\t" \
    "v_mfma_f32_16x16x32_bf16 v[92:95], v[204:207], v[148:151], v[92:95]\n\t" \
    "v_add_u32 v227, s50, v227\n\t" \
    "v_mfma_f32_16x16x32_bf16 v[96:99], v[192:195], v[152:155], v[96:99]\n\t" \
    "v_add_u32 v228, s50, v228\n\t" \
    "v_mfma_f32_16x16x32_bf16 v[100:103], v[196:199], v[152:155], v[100:103]\n\t" \
    "v_add_u32 v229, s50, v229\n\t" \
    "v_mfma_f32_16x16x32_bf16 v[104:107], v[200:203], v[152:155], v[104:107]\n\t" \
    "v_mfma_f32_16x16x32_bf16 v[108:111], v[204:207], v[152:155], v[108:111]\n\t" \
    "v_mfma_f32_16x16x32_bf16 v[112:115], v[192:195], v[156:159], v[112:115]\n\t" \
    "v_mfma_f32_16x16x32_bf16 v[116:119], v[196:199], v[156:159], v[116:119]\n\t" \
    "v_mfma_f32_16x16x32_bf16 v[120:123], v[200:203], v[156:159], v[120:123]\n\t" \
    "v_mfma_f32_16x16x32_bf16 v[124:127], v[204:207], v[156:159], v[124:127]\n\t" \
    "s_waitcnt lgkmcnt(0)\n\t" \
    "s_barrier\n\t" \
    "v_mfma_f32_16x16x32_bf16 v[0:3], v[208:211], v[160:163], v[0:3]\n\t" \
    "ds_read_b64_tr_b16 v[192:193], v226 offset:0\n\t" \
    "v_mfma_f32_16x16x32_bf16 v[4:7], v[212:215], v[160:163], v[4:7]\n\t" \
    "ds_read_b64_tr_b16 v[194:195], v226 offset:512\n\t" \
    "v_mfma_f32_16x16x32_bf16 v[8:11], v[216:219], v[160:163], v[8:11]\n\t" \
    "ds_read_b128 v[128:131], v224 offset:0\n\t" \
    "v_mfma_f32_16x16x32_bf16 v[12:15], v[220:223], v[160:163], v[12:15]\n\t" \
    "ds_read_b64_tr_b16 v[196:197], v227 offset:0\n\t" \
    "v_mfma_f32_16x16x32_bf16 v[16:19], v[208:211], v[164:167], v[16:19]\n\t" \
    "ds_read_b64_tr_b16 v[198:199], v227 offset:512\n\t" \
    "v_mfma_f32_16x16x32_bf16 v[20:23], v[212:215], v[164:167], v[20:23]\n\t" \
    "ds_read_b64_tr_b16 v[200:201], v228 offset:0\n\t" \
    "v_mfma_f32_16x16x32_bf16 v[24:27], v[216:219], v[164:167], v[24:27]\n\t" \
    "ds_read_b64_tr_b16 v[202:203], v228 offset:512\n\t" \
    "v_mfma_f32_16x16x32_bf16 v[28:31], v[220:223], v[164:167], v[28:31]\n\t" \
    "ds_read_b64_tr_b16 v[204:205], v229 offset:0\n\t" \
    "v_mfma_f32_16x16x32_bf16 v[32:35], v[208:211], v[168:171], v[32:35]\n\t" \
    "ds_read_b64_tr_b16 v[206:207], v229 offset:512\n\t" \
    "v_mfma_f32_16x16x32_bf16 v[36:39], v[212:215], v[168:171], v[36:39]\n\t" \
    "ds_read_b128 v[132:135], v224 offset:2048\n\t" \
    "v_mfma_f32_16x16x32_bf16 v[40:43], v[216:219], v[168:171], v[40:43]\n\t" \
    "ds_read_b128 v[136:139], v224 offset:4096\n\t" \
    "v_mfma_f32_16x16x32_bf16 v[44:47], v[220:223], v[168:171], v[44:47]\n\t" \
    "ds_read_b128 v[140:143], v224 offset:6144\n\t" \
    "v_mfma_f32_16x16x32_bf16 v[48:51], v[208:211], v[172:175], v[48:51]\n\t" \
    "ds_read_b128 v[144:147], v224 offset:8192\n\t" \
    "v_mfma_f32_16x16x32_bf16 v[52:55], v[212:215], v[172:175], v[52:55]\n\t" \
    "ds_read_b128 v[148:151], v224 offset:10240\n\t" \
    "v_mfma_f32_16x16x32_bf16 v[56:59], v[216:219], v[172:175], v[56:59]\n\t" \
    "ds_read_b128 v[152:155], v224 offset:12288\n\t" \
    "v_mfma_f32_16x16x32_bf16 v[60:63], v[220:223], v[172:175], v[60:63]\n\t" \
    "ds_read_b128 v[156:159], v224 offset:14336\n\t" \
    "v_mfma_f32_16x16x32_bf16 v[64:67], v[208:211], v[176:179], v[64:67]\n\t" \
    "v_mfma_f32_16x16x32_bf16 v[68:71], v[212:215], v[176:179], v[68:71]\n\t" \
    "v_mfma_f32_16x16x32_bf16 v[72:75], v[216:219], v[176:179], v[72:75]\n\t" \
    "v_mfma_f32_16x16x32_bf16 v[76:79], v[220:223], v[176:179], v[76:79]\n\t" \
    "v_mfma_f32_16x16x32_bf16 v[80:83], v[208:211], v[180:183], v[80:83]\n\t" \
    "v_mfma_f32_16x16x32_bf16 v[84:87], v[212:215], v[180:183], v[84:87]\n\t" \
    "v_mfma_f32_16x16x32_bf16 v[88:91], v[216:219], v[180:183], v[88:91]\n\t" \
    "v_mfma_f32_16x16x32_bf16 v[92:95], v[220:223], v[180:183], v[92:95]\n\t" \
    "v_mfma_f32_16x16x32_bf16 v[96:99], v[208:211], v[184:187], v[96:99]\n\t" \
    "v_mfma_f32_16x16x32_bf16 v[100:103], v[212:215], v[184:187], v[100:103]\n\t" \
    "v_mfma_f32_16x16x32_bf16 v[104:107], v[216:219], v[184:187], v[104:107]\n\t" \
    "v_mfma_f32_16x16x32_bf16 v[108:111], v[220:223], v[184:187], v[108:111]\n\t" \
    "v_mfma_f32_16x16x32_bf16 v[112:115], v[208:211], v[188:191], v[112:115]\n\t" \
    "v_mfma_f32_16x16x32_bf16 v[116:119], v[212:215], v[188:191], v[116:119]\n\t" \
    "v_mfma_f32_16x16x32_bf16 v[120:123], v[216:219], v[188:191], v[120:123]\n\t" \
    "v_mfma_f32_16x16x32_bf16 v[124:127], v[220:223], v[188:191], v[124:127]\n\t" \
    "s_waitcnt lgkmcnt(13)\n\t" \
    "v_mfma_f32_16x16x32_bf16 v[0:3], v[192:195], v[128:131], v[0:3]\n\t" \
    "ds_read_b64_tr_b16 v[208:209], v226 offset:4096\n\t" \
    "s_waitcnt lgkmcnt(12)\n\t" \
    "v_mfma_f32_16x16x32_bf16 v[4:7], v[196:199], v[128:131], v[4:7]\n\t" \
    "ds_read_b64_tr_b16 v[210:211], v226 offset:4608\n\t" \
    "s_waitcnt lgkmcnt(11)\n\t" \
    "v_mfma_f32_16x16x32_bf16 v[8:11], v[200:203], v[128:131], v[8:11]\n\t" \
    "ds_read_b128 v[160:163], v225 offset:0\n\t" \
    "s_waitcnt lgkmcnt(10)\n\t" \
    "v_mfma_f32_16x16x32_bf16 v[12:15], v[204:207], v[128:131], v[12:15]\n\t" \
    "ds_read_b64_tr_b16 v[212:213], v227 offset:4096\n\t" \
    "s_waitcnt lgkmcnt(10)\n\t" \
    "v_mfma_f32_16x16x32_bf16 v[16:19], v[192:195], v[132:135], v[16:19]\n\t" \
    "ds_read_b64_tr_b16 v[214:215], v227 offset:4608\n\t" \
    "v_mfma_f32_16x16x32_bf16 v[20:23], v[196:199], v[132:135], v[20:23]\n\t" \
    "ds_read_b64_tr_b16 v[216:217], v228 offset:4096\n\t" \
    "v_mfma_f32_16x16x32_bf16 v[24:27], v[200:203], v[132:135], v[24:27]\n\t" \
    "ds_read_b64_tr_b16 v[218:219], v228 offset:4608\n\t" \
    "v_mfma_f32_16x16x32_bf16 v[28:31], v[204:207], v[132:135], v[28:31]\n\t" \
    "ds_read_b64_tr_b16 v[220:221], v229 offset:4096\n\t" \
    "s_waitcnt lgkmcnt(13)\n\t" \
    "v_mfma_f32_16x16x32_bf16 v[32:35], v[192:195], v[136:139], v[32:35]\n\t" \
    "ds_read_b64_tr_b16 v[222:223], v229 offset:4608\n\t" \
    "v_mfma_f32_16x16x32_bf16 v[36:39], v[196:199], v[136:139], v[36:39]\n\t" \
    "ds_read_b128 v[164:167], v225 offset:2048\n\t" \
    "v_mfma_f32_16x16x32_bf16 v[40:43], v[200:203], v[136:139], v[40:43]\n\t" \
    "ds_read_b128 v[168:171], v225 offset:4096\n\t" \
    "v_mfma_f32_16x16x32_bf16 v[44:47], v[204:207], v[136:139], v[44:47]\n\t" \
    "ds_read_b128 v[172:175], v225 offset:6144\n\t" \
    "s_waitcnt lgkmcnt(15)\n\t" \
    "v_mfma_f32_16x16x32_bf16 v[48:51], v[192:195], v[140:143], v[48:51]\n\t" \
    "ds_read_b128 v[176:179], v225 offset:8192\n\t" \
    "v_mfma_f32_16x16x32_bf16 v[52:55], v[196:199], v[140:143], v[52:55]\n\t" \
    "ds_read_b128 v[180:183], v225 offset:10240\n\t" \
    "v_mfma_f32_16x16x32_bf16 v[56:59], v[200:203], v[140:143], v[56:59]\n\t" \
    "ds_read_b128 v[184:187], v225 offset:12288\n\t" \
    "v_mfma_f32_16x16x32_bf16 v[60:63], v[204:207], v[140:143], v[60:63]\n\t" \
    "ds_read_b128 v[188:191], v225 offset:14336\n\t" \
    "v_mfma_f32_16x16x32_bf16 v[64:67], v[192:195], v[144:147], v[64:67]\n\t" \
    "s_add_u32 s51, s51, 49152\n\t" \
    "v_mfma_f32_16x16x32_bf16 v[68:71], v[196:199], v[144:147], v[68:71]\n\t" \
    "s_cmp_eq_u32 s51, 147456\n\t" \
    "v_mfma_f32_16x16x32_bf16 v[72:75], v[200:203], v[144:147], v[72:75]\n\t" \
    "s_cselect_b32 s50, s53, s54\n\t" \
    "v_mfma_f32_16x16x32_bf16 v[76:79], v[204:207], v[144:147], v[76:79]\n\t" \
    "s_cselect_b32 s51, 0, s51\n\t" \
    "s_waitcnt lgkmcnt(15)\n\t" \
    "v_mfma_f32_16x16x32_bf16 v[80:83], v[192:195], v[148:151], v[80:83]\n\t" \
    "v_add_u32 v224, s50, v224\n\t" \
    "v_mfma_f32_16x16x32_bf16 v[84:87], v[196:199], v[148:151], v[84:87]\n\t" \
    "v_add_u32 v225, s50, v225\n\t" \
    "v_mfma_f32_16x16x32_bf16 v[88:91], v[200:203], v[148:151], v[88:91]\n\t" \
    "v_add_u32 v226, s50, v226\n\t" \
    "v_mfma_f32_16x16x32_bf16 v[92:95], v[204:207], v[148:151], v[92:95]\n\t" \
    "v_add_u32 v227, s50, v227\n\t" \
    "v_mfma_f32_16x16x32_bf16 v[96:99], v[192:195], v[152:155], v[96:99]\n\t" \
    "v_add_u32 v228, s50, v228\n\t" \
    "v_mfma_f32_16x16x32_bf16 v[100:103], v[196:199], v[152:155], v[100:103]\n\t" \
    "v_add_u32 v229, s50, v229\n\t" \
    "v_mfma_f32_16x16x32_bf16 v[104:107], v[200:203], v[152:155], v[104:107]\n\t" \
    "v_mfma_f32_16x16x32_bf16 v[108:111], v[204:207], v[152:155], v[108:111]\n\t" \
    "v_mfma_f32_16x16x32_bf16 v[112:115], v[192:195], v[156:159], v[112:115]\n\t" \
    "v_mfma_f32_16x16x32_bf16 v[116:119], v[196:199], v[156:159], v[116:119]\n\t" \
    "v_mfma_f32_16x16x32_bf16 v[120:123], v[200:203], v[156:159], v[120:123]\n\t" \
    "v_mfma_f32_16x16x32_bf16 v[124:127], v[204:207], v[156:159], v[124:127]\n\t" \
    "s_waitcnt lgkmcnt(0)\n\t" \
    "s_barrier\n\t" \
    "v_mfma_f32_16x16x32_bf16 v[0:3], v[208:211], v[160:163], v[0:3]\n\t" \
    "ds_read_b64_tr_b16 v[192:193], v226 offset:0\n\t" \
    "v_mfma_f32_16x16x32_bf16 v[4:7], v[212:215], v[160:163], v[4:7]\n\t" \
    "ds_read_b64_tr_b16 v[194:195], v226 offset:512\n\t" \
    "v_mfma_f32_16x16x32_bf16 v[8:11], v[216:219], v[160:163], v[8:11]\n\t" \
    "ds_read_b128 v[128:131], v224 offset:0\n\t" \
    "v_mfma_f32_16x16x32_bf16 v[12:15], v[220:223], v[160:163], v[12:15]\n\t" \
    "ds_read_b64_tr_b16 v[196:197], v227 offset:0\n\t" \
    "v_mfma_f32_16x16x32_bf16 v[16:19], v[208:211], v[164:167], v[16:19]\n\t" \
    "ds_read_b64_tr_b16 v[198:199], v227 offset:512\n\t" \
    "v_mfma_f32_16x16x32_bf16 v[20:23], v[212:215], v[164:167], v[20:23]\n\t" \
    "ds_read_b64_tr_b16 v[200:201], v228 offset:0\n\t" \
    "v_mfma_f32_16x16x32_bf16 v[24:27], v[216:219], v[164:167], v[24:27]\n\t" \
    "ds_read_b64_tr_b16 v[202:203], v228 offset:512\n\t" \
    "v_mfma_f32_16x16x32_bf16 v[28:31], v[220:223], v[164:167], v[28:31]\n\t" \
    "ds_read_b64_tr_b16 v[204:205], v229 offset:0\n\t" \
    "v_mfma_f32_16x16x32_bf16 v[32:35], v[208:211], v[168:171], v[32:35]\n\t" \
    "ds_read_b64_tr_b16 v[206:207], v229 offset:512\n\t" \
    "v_mfma_f32_16x16x32_bf16 v[36:39], v[212:215], v[168:171], v[36:39]\n\t" \
    "ds_read_b128 v[132:135], v224 offset:2048\n\t" \
    "v_mfma_f32_16x16x32_bf16 v[40:43], v[216:219], v[168:171], v[40:43]\n\t" \
    "ds_read_b128 v[136:139], v224 offset:4096\n\t" \
    "v_mfma_f32_16x16x32_bf16 v[44:47], v[220:223], v[168:171], v[44:47]\n\t" \
    "ds_read_b128 v[140:143], v224 offset:6144\n\t" \
    "v_mfma_f32_16x16x32_bf16 v[48:51], v[208:211], v[172:175], v[48:51]\n\t" \
    "ds_read_b128 v[144:147], v224 offset:8192\n\t" \
    "v_mfma_f32_16x16x32_bf16 v[52:55], v[212:215], v[172:175], v[52:55]\n\t" \
    "ds_read_b128 v[148:151], v224 offset:10240\n\t" \
    "v_mfma_f32_16x16x32_bf16 v[56:59], v[216:219], v[172:175], v[56:59]\n\t" \
    "ds_read_b128 v[152:155], v224 offset:12288\n\t" \
    "v_mfma_f32_16x16x32_bf16 v[60:63], v[220:223], v[172:175], v[60:63]\n\t" \
    "ds_read_b128 v[156:159], v224 offset:14336\n\t" \
    "v_mfma_f32_16x16x32_bf16 v[64:67], v[208:211], v[176:179], v[64:67]\n\t" \
    "v_mfma_f32_16x16x32_bf16 v[68:71], v[212:215], v[176:179], v[68:71]\n\t" \
    "v_mfma_f32_16x16x32_bf16 v[72:75], v[216:219], v[176:179], v[72:75]\n\t" \
    "v_mfma_f32_16x16x32_bf16 v[76:79], v[220:223], v[176:179], v[76:79]\n\t" \
    "v_mfma_f32_16x16x32_bf16 v[80:83], v[208:211], v[180:183], v[80:83]\n\t" \
    "v_mfma_f32_16x16x32_bf16 v[84:87], v[212:215], v[180:183], v[84:87]\n\t" \
    "v_mfma_f32_16x16x32_bf16 v[88:91], v[216:219], v[180:183], v[88:91]\n\t" \
    "v_mfma_f32_16x16x32_bf16 v[92:95], v[220:223], v[180:183], v[92:95]\n\t" \
    "v_mfma_f32_16x16x32_bf16 v[96:99], v[208:211], v[184:187], v[96:99]\n\t" \
    "v_mfma_f32_16x16x32_bf16 v[100:103], v[212:215], v[184:187], v[100:103]\n\t" \
    "v_mfma_f32_16x16x32_bf16 v[104:107], v[216:219], v[184:187], v[104:107]\n\t" \
    "v_mfma_f32_16x16x32_bf16 v[108:111], v[220:223], v[184:187], v[108:111]\n\t" \
    "v_mfma_f32_16x16x32_bf16 v[112:115], v[208:211], v[188:191], v[112:115]\n\t" \
    "v_mfma_f32_16x16x32_bf16 v[116:119], v[212:215], v[188:191], v[116:119]\n\t" \
    "v_mfma_f32_16x16x32_bf16 v[120:123], v[216:219], v[188:191], v[120:123]\n\t" \
    "v_mfma_f32_16x16x32_bf16 v[124:127], v[220:223], v[188:191], v[124:127]\n\t" \
    "s_waitcnt lgkmcnt(13)\n\t" \
    "v_mfma_f32_16x16x32_bf16 v[0:3], v[192:195], v[128:131], v[0:3]\n\t" \
    "ds_read_b64_tr_b16 v[208:209], v226 offset:4096\n\t" \
    "s_waitcnt lgkmcnt(12)\n\t" \
    "v_mfma_f32_16x16x32_bf16 v[4:7], v[196:199], v[128:131], v[4:7]\n\t" \
    "ds_read_b64_tr_b16 v[210:211], v226 offset:4608\n\t" \
    "s_waitcnt lgkmcnt(11)\n\t" \
    "v_mfma_f32_16x16x32_bf16 v[8:11], v[200:203], v[128:131], v[8:11]\n\t" \
    "ds_read_b128 v[160:163], v225 offset:0\n\t" \
    "s_waitcnt lgkmcnt(10)\n\t" \
    "v_mfma_f32_16x16x32_bf16 v[12:15], v[204:207], v[128:131], v[12:15]\n\t" \
    "ds_read_b64_tr_b16 v[212:213], v227 offset:4096\n\t" \
    "s_waitcnt lgkmcnt(10)\n\t" \
    "v_mfma_f32_16x16x32_bf16 v[16:19], v[192:195], v[132:135], v[16:19]\n\t" \
    "ds_read_b64_tr_b16 v[214:215], v227 offset:4608\n\t" \
    "v_mfma_f32_16x16x32_bf16 v[20:23], v[196:199], v[132:135], v[20:23]\n\t" \
    "ds_read_b64_tr_b16 v[216:217], v228 offset:4096\n\t" \
    "v_mfma_f32_16x16x32_bf16 v[24:27], v[200:203], v[132:135], v[24:27]\n\t" \
    "ds_read_b64_tr_b16 v[218:219], v228 offset:4608\n\t" \
    "v_mfma_f32_16x16x32_bf16 v[28:31], v[204:207], v[132:135], v[28:31]\n\t" \
    "ds_read_b64_tr_b16 v[220:221], v229 offset:4096\n\t" \
    "s_waitcnt lgkmcnt(13)\n\t" \
    "v_mfma_f32_16x16x32_bf16 v[32:35], v[192:195], v[136:139], v[32:35]\n\t" \
    "ds_read_b64_tr_b16 v[222:223], v229 offset:4608\n\t" \
    "v_mfma_f32_16x16x32_bf16 v[36:39], v[196:199], v[136:139], v[36:39]\n\t" \
    "ds_read_b128 v[164:167], v225 offset:2048\n\t" \
    "v_mfma_f32_16x16x32_bf16 v[40:43], v[200:203], v[136:139], v[40:43]\n\t" \
    "ds_read_b128 v[168:171], v225 offset:4096\n\t" \
    "v_mfma_f32_16x16x32_bf16 v[44:47], v[204:207], v[136:139], v[44:47]\n\t" \
    "ds_read_b128 v[172:175], v225 offset:6144\n\t" \
    "s_waitcnt lgkmcnt(15)\n\t" \
    "v_mfma_f32_16x16x32_bf16 v[48:51], v[192:195], v[140:143], v[48:51]\n\t" \
    "ds_read_b128 v[176:179], v225 offset:8192\n\t" \
    "v_mfma_f32_16x16x32_bf16 v[52:55], v[196:199], v[140:143], v[52:55]\n\t" \
    "ds_read_b128 v[180:183], v225 offset:10240\n\t" \
    "v_mfma_f32_16x16x32_bf16 v[56:59], v[200:203], v[140:143], v[56:59]\n\t" \
    "ds_read_b128 v[184:187], v225 offset:12288\n\t" \
    "v_mfma_f32_16x16x32_bf16 v[60:63], v[204:207], v[140:143], v[60:63]\n\t" \
    "ds_read_b128 v[188:191], v225 offset:14336\n\t" \
    "v_mfma_f32_16x16x32_bf16 v[64:67], v[192:195], v[144:147], v[64:67]\n\t" \
    "s_add_u32 s51, s51, 49152\n\t" \
    "v_mfma_f32_16x16x32_bf16 v[68:71], v[196:199], v[144:147], v[68:71]\n\t" \
    "s_cmp_eq_u32 s51, 147456\n\t" \
    "v_mfma_f32_16x16x32_bf16 v[72:75], v[200:203], v[144:147], v[72:75]\n\t" \
    "s_cselect_b32 s50, s53, s54\n\t" \
    "v_mfma_f32_16x16x32_bf16 v[76:79], v[204:207], v[144:147], v[76:79]\n\t" \
    "s_cselect_b32 s51, 0, s51\n\t" \
    "s_waitcnt lgkmcnt(15)\n\t" \
    "v_mfma_f32_16x16x32_bf16 v[80:83], v[192:195], v[148:151], v[80:83]\n\t" \
    "v_add_u32 v224, s50, v224\n\t" \
    "v_mfma_f32_16x16x32_bf16 v[84:87], v[196:199], v[148:151], v[84:87]\n\t" \
    "v_add_u32 v225, s50, v225\n\t" \
    "v_mfma_f32_16x16x32_bf16 v[88:91], v[200:203], v[148:151], v[88:91]\n\t" \
    "v_add_u32 v226, s50, v226\n\t" \
    "v_mfma_f32_16x16x32_bf16 v[92:95], v[204:207], v[148:151], v[92:95]\n\t" \
    "v_add_u32 v227, s50, v227\n\t" \
    "v_mfma_f32_16x16x32_bf16 v[96:99], v[192:195], v[152:155], v[96:99]\n\t" \
    "v_add_u32 v228, s50, v228\n\t" \
    "v_mfma_f32_16x16x32_bf16 v[100:103], v[196:199], v[152:155], v[100:103]\n\t" \
    "v_add_u32 v229, s50, v229\n\t" \
    "v_mfma_f32_16x16x32_bf16 v[104:107], v[200:203], v[152:155], v[104:107]\n\t" \
    "v_mfma_f32_16x16x32_bf16 v[108:111], v[204:207], v[152:155], v[108:111]\n\t" \
    "v_mfma_f32_16x16x32_bf16 v[112:115], v[192:195], v[156:159], v[112:115]\n\t" \
    "v_mfma_f32_16x16x32_bf16 v[116:119], v[196:199], v[156:159], v[116:119]\n\t" \
    "v_mfma_f32_16x16x32_bf16 v[120:123], v[200:203], v[156:159], v[120:123]\n\t" \
    "v_mfma_f32_16x16x32_bf16 v[124:127], v[204:207], v[156:159], v[124:127]\n\t" \
    "s_waitcnt lgkmcnt(0)\n\t" \
    "s_barrier\n\t" \
    "v_mfma_f32_16x16x32_bf16 v[0:3], v[208:211], v[160:163], v[0:3]\n\t" \
    "ds_read_b64_tr_b16 v[192:193], v226 offset:0\n\t" \
    "v_mfma_f32_16x16x32_bf16 v[4:7], v[212:215], v[160:163], v[4:7]\n\t" \
    "ds_read_b64_tr_b16 v[194:195], v226 offset:512\n\t" \
    "v_mfma_f32_16x16x32_bf16 v[8:11], v[216:219], v[160:163], v[8:11]\n\t" \
    "ds_read_b128 v[128:131], v224 offset:0\n\t" \
    "v_mfma_f32_16x16x32_bf16 v[12:15], v[220:223], v[160:163], v[12:15]\n\t" \
    "ds_read_b64_tr_b16 v[196:197], v227 offset:0\n\t" \
    "v_mfma_f32_16x16x32_bf16 v[16:19], v[208:211], v[164:167], v[16:19]\n\t" \
    "ds_read_b64_tr_b16 v[198:199], v227 offset:512\n\t" \
    "v_mfma_f32_16x16x32_bf16 v[20:23], v[212:215], v[164:167], v[20:23]\n\t" \
    "ds_read_b64_tr_b16 v[200:201], v228 offset:0\n\t" \
    "v_mfma_f32_16x16x32_bf16 v[24:27], v[216:219], v[164:167], v[24:27]\n\t" \
    "ds_read_b64_tr_b16 v[202:203], v228 offset:512\n\t" \
    "v_mfma_f32_16x16x32_bf16 v[28:31], v[220:223], v[164:167], v[28:31]\n\t" \
    "ds_read_b64_tr_b16 v[204:205], v229 offset:0\n\t" \
    "v_mfma_f32_16x16x32_bf16 v[32:35], v[208:211], v[168:171], v[32:35]\n\t" \
    "ds_read_b64_tr_b16 v[206:207], v229 offset:512\n\t" \
    "v_mfma_f32_16x16x32_bf16 v[36:39], v[212:215], v[168:171], v[36:39]\n\t" \
    "ds_read_b128 v[132:135], v224 offset:2048\n\t" \
    "v_mfma_f32_16x16x32_bf16 v[40:43], v[216:219], v[168:171], v[40:43]\n\t" \
    "ds_read_b128 v[136:139], v224 offset:4096\n\t" \
    "v_mfma_f32_16x16x32_bf16 v[44:47], v[220:223], v[168:171], v[44:47]\n\t" \
    "ds_read_b128 v[140:143], v224 offset:6144\n\t" \
    "v_mfma_f32_16x16x32_bf16 v[48:51], v[208:211], v[172:175], v[48:51]\n\t" \
    "ds_read_b128 v[144:147], v224 offset:8192\n\t" \
    "v_mfma_f32_16x16x32_bf16 v[52:55], v[212:215], v[172:175], v[52:55]\n\t" \
    "ds_read_b128 v[148:151], v224 offset:10240\n\t" \
    "v_mfma_f32_16x16x32_bf16 v[56:59], v[216:219], v[172:175], v[56:59]\n\t" \
    "ds_read_b128 v[152:155], v224 offset:12288\n\t" \
    "v_mfma_f32_16x16x32_bf16 v[60:63], v[220:223], v[172:175], v[60:63]\n\t" \
    "ds_read_b128 v[156:159], v224 offset:14336\n\t" \
    "v_mfma_f32_16x16x32_bf16 v[64:67], v[208:211], v[176:179], v[64:67]\n\t" \
    "v_mfma_f32_16x16x32_bf16 v[68:71], v[212:215], v[176:179], v[68:71]\n\t" \
    "v_mfma_f32_16x16x32_bf16 v[72:75], v[216:219], v[176:179], v[72:75]\n\t" \
    "v_mfma_f32_16x16x32_bf16 v[76:79], v[220:223], v[176:179], v[76:79]\n\t" \
    "v_mfma_f32_16x16x32_bf16 v[80:83], v[208:211], v[180:183], v[80:83]\n\t" \
    "v_mfma_f32_16x16x32_bf16 v[84:87], v[212:215], v[180:183], v[84:87]\n\t" \
    "v_mfma_f32_16x16x32_bf16 v[88:91], v[216:219], v[180:183], v[88:91]\n\t" \
    "v_mfma_f32_16x16x32_bf16 v[92:95], v[220:223], v[180:183], v[92:95]\n\t" \
    "v_mfma_f32_16x16x32_bf16 v[96:99], v[208:211], v[184:187], v[96:99]\n\t" \
    "v_mfma_f32_16x16x32_bf16 v[100:103], v[212:215], v[184:187], v[100:103]\n\t" \
    "v_mfma_f32_16x16x32_bf16 v[104:107], v[216:219], v[184:187], v[104:107]\n\t" \
    "v_mfma_f32_16x16x32_bf16 v[108:111], v[220:223], v[184:187], v[108:111]\n\t" \
    "v_mfma_f32_16x16x32_bf16 v[112:115], v[208:211], v[188:191], v[112:115]\n\t" \
    "v_mfma_f32_16x16x32_bf16 v[116:119], v[212:215], v[188:191], v[116:119]\n\t" \
    "v_mfma_f32_16x16x32_bf16 v[120:123], v[216:219], v[188:191], v[120:123]\n\t" \
    "v_mfma_f32_16x16x32_bf16 v[124:127], v[220:223], v[188:191], v[124:127]\n\t" \
    "s_waitcnt lgkmcnt(13)\n\t" \
    "v_mfma_f32_16x16x32_bf16 v[0:3], v[192:195], v[128:131], v[0:3]\n\t" \
    "ds_read_b64_tr_b16 v[208:209], v226 offset:4096\n\t" \
    "s_waitcnt lgkmcnt(12)\n\t" \
    "v_mfma_f32_16x16x32_bf16 v[4:7], v[196:199], v[128:131], v[4:7]\n\t" \
    "ds_read_b64_tr_b16 v[210:211], v226 offset:4608\n\t" \
    "s_waitcnt lgkmcnt(11)\n\t" \
    "v_mfma_f32_16x16x32_bf16 v[8:11], v[200:203], v[128:131], v[8:11]\n\t" \
    "ds_read_b128 v[160:163], v225 offset:0\n\t" \
    "s_waitcnt lgkmcnt(10)\n\t" \
    "v_mfma_f32_16x16x32_bf16 v[12:15], v[204:207], v[128:131], v[12:15]\n\t" \
    "ds_read_b64_tr_b16 v[212:213], v227 offset:4096\n\t" \
    "s_waitcnt lgkmcnt(10)\n\t" \
    "v_mfma_f32_16x16x32_bf16 v[16:19], v[192:195], v[132:135], v[16:19]\n\t" \
    "ds_read_b64_tr_b16 v[214:215], v227 offset:4608\n\t" \
    "v_mfma_f32_16x16x32_bf16 v[20:23], v[196:199], v[132:135], v[20:23]\n\t" \
    "ds_read_b64_tr_b16 v[216:217], v228 offset:4096\n\t" \
    "v_mfma_f32_16x16x32_bf16 v[24:27], v[200:203], v[132:135], v[24:27]\n\t" \
    "ds_read_b64_tr_b16 v[218:219], v228 offset:4608\n\t" \
    "v_mfma_f32_16x16x32_bf16 v[28:31], v[204:207], v[132:135], v[28:31]\n\t" \
    "ds_read_b64_tr_b16 v[220:221], v229 offset:4096\n\t" \
    "s_waitcnt lgkmcnt(13)\n\t" \
    "v_mfma_f32_16x16x32_bf16 v[32:35], v[192:195], v[136:139], v[32:35]\n\t" \
    "ds_read_b64_tr_b16 v[222:223], v229 offset:4608\n\t" \
    "v_mfma_f32_16x16x32_bf16 v[36:39], v[196:199], v[136:139], v[36:39]\n\t" \
    "ds_read_b128 v[164:167], v225 offset:2048\n\t" \
    "v_mfma_f32_16x16x32_bf16 v[40:43], v[200:203], v[136:139], v[40:43]\n\t" \
    "ds_read_b128 v[168:171], v225 offset:4096\n\t" \
    "v_mfma_f32_16x16x32_bf16 v[44:47], v[204:207], v[136:139], v[44:47]\n\t" \
    "ds_read_b128 v[172:175], v225 offset:6144\n\t" \
    "s_waitcnt lgkmcnt(15)\n\t" \
    "v_mfma_f32_16x16x32_bf16 v[48:51], v[192:195], v[140:143], v[48:51]\n\t" \
    "ds_read_b128 v[176:179], v225 offset:8192\n\t" \
    "v_mfma_f32_16x16x32_bf16 v[52:55], v[196:199], v[140:143], v[52:55]\n\t" \
    "ds_read_b128 v[180:183], v225 offset:10240\n\t" \
    "v_mfma_f32_16x16x32_bf16 v[56:59], v[200:203], v[140:143], v[56:59]\n\t" \
    "ds_read_b128 v[184:187], v225 offset:12288\n\t" \
    "v_mfma_f32_16x16x32_bf16 v[60:63], v[204:207], v[140:143], v[60:63]\n\t" \
    "ds_read_b128 v[188:191], v225 offset:14336\n\t" \
    "v_mfma_f32_16x16x32_bf16 v[64:67], v[192:195], v[144:147], v[64:67]\n\t" \
    "s_add_u32 s51, s51, 49152\n\t" \
    "v_mfma_f32_16x16x32_bf16 v[68:71], v[196:199], v[144:147], v[68:71]\n\t" \
    "s_cmp_eq_u32 s51, 147456\n\t" \
    "v_mfma_f32_16x16x32_bf16 v[72:75], v[200:203], v[144:147], v[72:75]\n\t" \
    "s_cselect_b32 s50, s53, s54\n\t" \
    "v_mfma_f32_16x16x32_bf16 v[76:79], v[204:207], v[144:147], v[76:79]\n\t" \
    "s_cselect_b32 s51, 0, s51\n\t" \
    "s_waitcnt lgkmcnt(15)\n\t" \
    "v_mfma_f32_16x16x32_bf16 v[80:83], v[192:195], v[148:151], v[80:83]\n\t" \
    "v_add_u32 v224, s50, v224\n\t" \
    "v_mfma_f32_16x16x32_bf16 v[84:87], v[196:199], v[148:151], v[84:87]\n\t" \
    "v_add_u32 v225, s50, v225\n\t" \
    "v_mfma_f32_16x16x32_bf16 v[88:91], v[200:203], v[148:151], v[88:91]\n\t" \
    "v_add_u32 v226, s50, v226\n\t" \
    "v_mfma_f32_16x16x32_bf16 v[92:95], v[204:207], v[148:151], v[92:95]\n\t" \
    "v_add_u32 v227, s50, v227\n\t" \
    "v_mfma_f32_16x16x32_bf16 v[96:99], v[192:195], v[152:155], v[96:99]\n\t" \
    "v_add_u32 v228, s50, v228\n\t" \
    "v_mfma_f32_16x16x32_bf16 v[100:103], v[196:199], v[152:155], v[100:103]\n\t" \
    "v_add_u32 v229, s50, v229\n\t" \
    "v_mfma_f32_16x16x32_bf16 v[104:107], v[200:203], v[152:155], v[104:107]\n\t" \
    "v_mfma_f32_16x16x32_bf16 v[108:111], v[204:207], v[152:155], v[108:111]\n\t" \
    "v_mfma_f32_16x16x32_bf16 v[112:115], v[192:195], v[156:159], v[112:115]\n\t" \
    "v_mfma_f32_16x16x32_bf16 v[116:119], v[196:199], v[156:159], v[116:119]\n\t" \
    "v_mfma_f32_16x16x32_bf16 v[120:123], v[200:203], v[156:159], v[120:123]\n\t" \
    "v_mfma_f32_16x16x32_bf16 v[124:127], v[204:207], v[156:159], v[124:127]\n\t" \
    "s_waitcnt lgkmcnt(0)\n\t" \
    "s_barrier\n\t" \
    "v_mfma_f32_16x16x32_bf16 v[0:3], v[208:211], v[160:163], v[0:3]\n\t" \
    "ds_read_b64_tr_b16 v[192:193], v226 offset:0\n\t" \
    "v_mfma_f32_16x16x32_bf16 v[4:7], v[212:215], v[160:163], v[4:7]\n\t" \
    "ds_read_b64_tr_b16 v[194:195], v226 offset:512\n\t" \
    "v_mfma_f32_16x16x32_bf16 v[8:11], v[216:219], v[160:163], v[8:11]\n\t" \
    "ds_read_b128 v[128:131], v224 offset:0\n\t" \
    "v_mfma_f32_16x16x32_bf16 v[12:15], v[220:223], v[160:163], v[12:15]\n\t" \
    "ds_read_b64_tr_b16 v[196:197], v227 offset:0\n\t" \
    "v_mfma_f32_16x16x32_bf16 v[16:19], v[208:211], v[164:167], v[16:19]\n\t" \
    "ds_read_b64_tr_b16 v[198:199], v227 offset:512\n\t" \
    "v_mfma_f32_16x16x32_bf16 v[20:23], v[212:215], v[164:167], v[20:23]\n\t" \
    "ds_read_b64_tr_b16 v[200:201], v228 offset:0\n\t" \
    "v_mfma_f32_16x16x32_bf16 v[24:27], v[216:219], v[164:167], v[24:27]\n\t" \
    "ds_read_b64_tr_b16 v[202:203], v228 offset:512\n\t" \
    "v_mfma_f32_16x16x32_bf16 v[28:31], v[220:223], v[164:167], v[28:31]\n\t" \
    "ds_read_b64_tr_b16 v[204:205], v229 offset:0\n\t" \
    "v_mfma_f32_16x16x32_bf16 v[32:35], v[208:211], v[168:171], v[32:35]\n\t" \
    "ds_read_b64_tr_b16 v[206:207], v229 offset:512\n\t" \
    "v_mfma_f32_16x16x32_bf16 v[36:39], v[212:215], v[168:171], v[36:39]\n\t" \
    "ds_read_b128 v[132:135], v224 offset:2048\n\t" \
    "v_mfma_f32_16x16x32_bf16 v[40:43], v[216:219], v[168:171], v[40:43]\n\t" \
    "ds_read_b128 v[136:139], v224 offset:4096\n\t" \
    "v_mfma_f32_16x16x32_bf16 v[44:47], v[220:223], v[168:171], v[44:47]\n\t" \
    "ds_read_b128 v[140:143], v224 offset:6144\n\t" \
    "v_mfma_f32_16x16x32_bf16 v[48:51], v[208:211], v[172:175], v[48:51]\n\t" \
    "ds_read_b128 v[144:147], v224 offset:8192\n\t" \
    "v_mfma_f32_16x16x32_bf16 v[52:55], v[212:215], v[172:175], v[52:55]\n\t" \
    "ds_read_b128 v[148:151], v224 offset:10240\n\t" \
    "v_mfma_f32_16x16x32_bf16 v[56:59], v[216:219], v[172:175], v[56:59]\n\t" \
    "ds_read_b128 v[152:155], v224 offset:12288\n\t" \
    "v_mfma_f32_16x16x32_bf16 v[60:63], v[220:223], v[172:175], v[60:63]\n\t" \
    "ds_read_b128 v[156:159], v224 offset:14336\n\t" \
    "v_mfma_f32_16x16x32_bf16 v[64:67], v[208:211], v[176:179], v[64:67]\n\t" \
    "v_mfma_f32_16x16x32_bf16 v[68:71], v[212:215], v[176:179], v[68:71]\n\t" \
    "v_mfma_f32_16x16x32_bf16 v[72:75], v[216:219], v[176:179], v[72:75]\n\t" \
    "v_mfma_f32_16x16x32_bf16 v[76:79], v[220:223], v[176:179], v[76:79]\n\t" \
    "v_mfma_f32_16x16x32_bf16 v[80:83], v[208:211], v[180:183], v[80:83]\n\t" \
    "v_mfma_f32_16x16x32_bf16 v[84:87], v[212:215], v[180:183], v[84:87]\n\t" \
    "v_mfma_f32_16x16x32_bf16 v[88:91], v[216:219], v[180:183], v[88:91]\n\t" \
    "v_mfma_f32_16x16x32_bf16 v[92:95], v[220:223], v[180:183], v[92:95]\n\t" \
    "v_mfma_f32_16x16x32_bf16 v[96:99], v[208:211], v[184:187], v[96:99]\n\t" \
    "v_mfma_f32_16x16x32_bf16 v[100:103], v[212:215], v[184:187], v[100:103]\n\t" \
    "v_mfma_f32_16x16x32_bf16 v[104:107], v[216:219], v[184:187], v[104:107]\n\t" \
    "v_mfma_f32_16x16x32_bf16 v[108:111], v[220:223], v[184:187], v[108:111]\n\t" \
    "v_mfma_f32_16x16x32_bf16 v[112:115], v[208:211], v[188:191], v[112:115]\n\t" \
    "v_mfma_f32_16x16x32_bf16 v[116:119], v[212:215], v[188:191], v[116:119]\n\t" \
    "v_mfma_f32_16x16x32_bf16 v[120:123], v[216:219], v[188:191], v[120:123]\n\t" \
    "v_mfma_f32_16x16x32_bf16 v[124:127], v[220:223], v[188:191], v[124:127]\n\t" \
    "s_waitcnt lgkmcnt(13)\n\t" \
    "v_mfma_f32_16x16x32_bf16 v[0:3], v[192:195], v[128:131], v[0:3]\n\t" \
    "ds_read_b64_tr_b16 v[208:209], v226 offset:4096\n\t" \
    "s_waitcnt lgkmcnt(12)\n\t" \
    "v_mfma_f32_16x16x32_bf16 v[4:7], v[196:199], v[128:131], v[4:7]\n\t" \
    "ds_read_b64_tr_b16 v[210:211], v226 offset:4608\n\t" \
    "s_waitcnt lgkmcnt(11)\n\t" \
    "v_mfma_f32_16x16x32_bf16 v[8:11], v[200:203], v[128:131], v[8:11]\n\t" \
    "ds_read_b128 v[160:163], v225 offset:0\n\t" \
    "s_waitcnt lgkmcnt(10)\n\t" \
    "v_mfma_f32_16x16x32_bf16 v[12:15], v[204:207], v[128:131], v[12:15]\n\t" \
    "ds_read_b64_tr_b16 v[212:213], v227 offset:4096\n\t" \
    "s_waitcnt lgkmcnt(10)\n\t" \
    "v_mfma_f32_16x16x32_bf16 v[16:19], v[192:195], v[132:135], v[16:19]\n\t" \
    "ds_read_b64_tr_b16 v[214:215], v227 offset:4608\n\t" \
    "v_mfma_f32_16x16x32_bf16 v[20:23], v[196:199], v[132:135], v[20:23]\n\t" \
    "ds_read_b64_tr_b16 v[216:217], v228 offset:4096\n\t" \
    "v_mfma_f32_16x16x32_bf16 v[24:27], v[200:203], v[132:135], v[24:27]\n\t" \
    "ds_read_b64_tr_b16 v[218:219], v228 offset:4608\n\t" \
    "v_mfma_f32_16x16x32_bf16 v[28:31], v[204:207], v[132:135], v[28:31]\n\t" \
    "ds_read_b64_tr_b16 v[220:221], v229 offset:4096\n\t" \
    "s_waitcnt lgkmcnt(13)\n\t" \
    "v_mfma_f32_16x16x32_bf16 v[32:35], v[192:195], v[136:139], v[32:35]\n\t" \
    "ds_read_b64_tr_b16 v[222:223], v229 offset:4608\n\t" \
    "v_mfma_f32_16x16x32_bf16 v[36:39], v[196:199], v[136:139], v[36:39]\n\t" \
    "ds_read_b128 v[164:167], v225 offset:2048\n\t" \
    "v_mfma_f32_16x16x32_bf16 v[40:43], v[200:203], v[136:139], v[40:43]\n\t" \
    "ds_read_b128 v[168:171], v225 offset:4096\n\t" \
    "v_mfma_f32_16x16x32_bf16 v[44:47], v[204:207], v[136:139], v[44:47]\n\t" \
    "ds_read_b128 v[172:175], v225 offset:6144\n\t" \
    "s_waitcnt lgkmcnt(15)\n\t" \
    "v_mfma_f32_16x16x32_bf16 v[48:51], v[192:195], v[140:143], v[48:51]\n\t" \
    "ds_read_b128 v[176:179], v225 offset:8192\n\t" \
    "v_mfma_f32_16x16x32_bf16 v[52:55], v[196:199], v[140:143], v[52:55]\n\t" \
    "ds_read_b128 v[180:183], v225 offset:10240\n\t" \
    "v_mfma_f32_16x16x32_bf16 v[56:59], v[200:203], v[140:143], v[56:59]\n\t" \
    "ds_read_b128 v[184:187], v225 offset:12288\n\t" \
    "v_mfma_f32_16x16x32_bf16 v[60:63], v[204:207], v[140:143], v[60:63]\n\t" \
    "ds_read_b128 v[188:191], v225 offset:14336\n\t" \
    "v_mfma_f32_16x16x32_bf16 v[64:67], v[192:195], v[144:147], v[64:67]\n\t" \
    "s_add_u32 s51, s51, 49152\n\t" \
    "v_mfma_f32_16x16x32_bf16 v[68:71], v[196:199], v[144:147], v[68:71]\n\t" \
    "s_cmp_eq_u32 s51, 147456\n\t" \
    "v_mfma_f32_16x16x32_bf16 v[72:75], v[200:203], v[144:147], v[72:75]\n\t" \
    "s_cselect_b32 s50, s53, s54\n\t" \
    "v_mfma_f32_16x16x32_bf16 v[76:79], v[204:207], v[144:147], v[76:79]\n\t" \
    "s_cselect_b32 s51, 0, s51\n\t" \
    "s_waitcnt lgkmcnt(15)\n\t" \
    "v_mfma_f32_16x16x32_bf16 v[80:83], v[192:195], v[148:151], v[80:83]\n\t" \
    "v_add_u32 v224, s50, v224\n\t" \
    "v_mfma_f32_16x16x32_bf16 v[84:87], v[196:199], v[148:151], v[84:87]\n\t" \
    "v_add_u32 v225, s50, v225\n\t" \
    "v_mfma_f32_16x16x32_bf16 v[88:91], v[200:203], v[148:151], v[88:91]\n\t" \
    "v_add_u32 v226, s50, v226\n\t" \
    "v_mfma_f32_16x16x32_bf16 v[92:95], v[204:207], v[148:151], v[92:95]\n\t" \
    "v_add_u32 v227, s50, v227\n\t" \
    "v_mfma_f32_16x16x32_bf16 v[96:99], v[192:195], v[152:155], v[96:99]\n\t" \
    "v_add_u32 v228, s50, v228\n\t" \
    "v_mfma_f32_16x16x32_bf16 v[100:103], v[196:199], v[152:155], v[100:103]\n\t" \
    "v_add_u32 v229, s50, v229\n\t" \
    "v_mfma_f32_16x16x32_bf16 v[104:107], v[200:203], v[152:155], v[104:107]\n\t" \
    "v_mfma_f32_16x16x32_bf16 v[108:111], v[204:207], v[152:155], v[108:111]\n\t" \
    "v_mfma_f32_16x16x32_bf16 v[112:115], v[192:195], v[156:159], v[112:115]\n\t" \
    "v_mfma_f32_16x16x32_bf16 v[116:119], v[196:199], v[156:159], v[116:119]\n\t" \
    "v_mfma_f32_16x16x32_bf16 v[120:123], v[200:203], v[156:159], v[120:123]\n\t" \
    "v_mfma_f32_16x16x32_bf16 v[124:127], v[204:207], v[156:159], v[124:127]\n\t" \
    "s_waitcnt lgkmcnt(0)\n\t" \
    "s_barrier\n\t" \
    "v_mfma_f32_16x16x32_bf16 v[0:3], v[208:211], v[160:163], v[0:3]\n\t" \
    "ds_read_b64_tr_b16 v[192:193], v226 offset:0\n\t" \
    "v_mfma_f32_16x16x32_bf16 v[4:7], v[212:215], v[160:163], v[4:7]\n\t" \
    "ds_read_b64_tr_b16 v[194:195], v226 offset:512\n\t" \
    "v_mfma_f32_16x16x32_bf16 v[8:11], v[216:219], v[160:163], v[8:11]\n\t" \
    "ds_read_b128 v[128:131], v224 offset:0\n\t" \
    "v_mfma_f32_16x16x32_bf16 v[12:15], v[220:223], v[160:163], v[12:15]\n\t" \
    "ds_read_b64_tr_b16 v[196:197], v227 offset:0\n\t" \
    "v_mfma_f32_16x16x32_bf16 v[16:19], v[208:211], v[164:167], v[16:19]\n\t" \
    "ds_read_b64_tr_b16 v[198:199], v227 offset:512\n\t" \
    "v_mfma_f32_16x16x32_bf16 v[20:23], v[212:215], v[164:167], v[20:23]\n\t" \
    "ds_read_b64_tr_b16 v[200:201], v228 offset:0\n\t" \
    "v_mfma_f32_16x16x32_bf16 v[24:27], v[216:219], v[164:167], v[24:27]\n\t" \
    "ds_read_b64_tr_b16 v[202:203], v228 offset:512\n\t" \
    "v_mfma_f32_16x16x32_bf16 v[28:31], v[220:223], v[164:167], v[28:31]\n\t" \
    "ds_read_b64_tr_b16 v[204:205], v229 offset:0\n\t" \
    "v_mfma_f32_16x16x32_bf16 v[32:35], v[208:211], v[168:171], v[32:35]\n\t" \
    "ds_read_b64_tr_b16 v[206:207], v229 offset:512\n\t" \
    "v_mfma_f32_16x16x32_bf16 v[36:39], v[212:215], v[168:171], v[36:39]\n\t" \
    "ds_read_b128 v[132:135], v224 offset:2048\n\t" \
    "v_mfma_f32_16x16x32_bf16 v[40:43], v[216:219], v[168:171], v[40:43]\n\t" \
    "ds_read_b128 v[136:139], v224 offset:4096\n\t" \
    "v_mfma_f32_16x16x32_bf16 v[44:47], v[220:223], v[168:171], v[44:47]\n\t" \
    "ds_read_b128 v[140:143], v224 offset:6144\n\t" \
    "v_mfma_f32_16x16x32_bf16 v[48:51], v[208:211], v[172:175], v[48:51]\n\t" \
    "ds_read_b128 v[144:147], v224 offset:8192\n\t" \
    "v_mfma_f32_16x16x32_bf16 v[52:55], v[212:215], v[172:175], v[52:55]\n\t" \
    "ds_read_b128 v[148:151], v224 offset:10240\n\t" \
    "v_mfma_f32_16x16x32_bf16 v[56:59], v[216:219], v[172:175], v[56:59]\n\t" \
    "ds_read_b128 v[152:155], v224 offset:12288\n\t" \
    "v_mfma_f32_16x16x32_bf16 v[60:63], v[220:223], v[172:175], v[60:63]\n\t" \
    "ds_read_b128 v[156:159], v224 offset:14336\n\t" \
    "v_mfma_f32_16x16x32_bf16 v[64:67], v[208:211], v[176:179], v[64:67]\n\t" \
    "v_mfma_f32_16x16x32_bf16 v[68:71], v[212:215], v[176:179], v[68:71]\n\t" \
    "v_mfma_f32_16x16x32_bf16 v[72:75], v[216:219], v[176:179], v[72:75]\n\t" \
    "v_mfma_f32_16x16x32_bf16 v[76:79], v[220:223], v[176:179], v[76:79]\n\t" \
    "v_mfma_f32_16x16x32_bf16 v[80:83], v[208:211], v[180:183], v[80:83]\n\t" \
    "v_mfma_f32_16x16x32_bf16 v[84:87], v[212:215], v[180:183], v[84:87]\n\t" \
    "v_mfma_f32_16x16x32_bf16 v[88:91], v[216:219], v[180:183], v[88:91]\n\t" \
    "v_mfma_f32_16x16x32_bf16 v[92:95], v[220:223], v[180:183], v[92:95]\n\t" \
    "v_mfma_f32_16x16x32_bf16 v[96:99], v[208:211], v[184:187], v[96:99]\n\t" \
    "v_mfma_f32_16x16x32_bf16 v[100:103], v[212:215], v[184:187], v[100:103]\n\t" \
    "v_mfma_f32_16x16x32_bf16 v[104:107], v[216:219], v[184:187], v[104:107]\n\t" \
    "v_mfma_f32_16x16x32_bf16 v[108:111], v[220:223], v[184:187], v[108:111]\n\t" \
    "v_mfma_f32_16x16x32_bf16 v[112:115], v[208:211], v[188:191], v[112:115]\n\t" \
    "v_mfma_f32_16x16x32_bf16 v[116:119], v[212:215], v[188:191], v[116:119]\n\t" \
    "v_mfma_f32_16x16x32_bf16 v[120:123], v[216:219], v[188:191], v[120:123]\n\t" \
    "v_mfma_f32_16x16x32_bf16 v[124:127], v[220:223], v[188:191], v[124:127]\n\t" \
    "s_waitcnt lgkmcnt(13)\n\t" \
    "v_mfma_f32_16x16x32_bf16 v[0:3], v[192:195], v[128:131], v[0:3]\n\t" \
    "ds_read_b64_tr_b16 v[208:209], v226 offset:4096\n\t" \
    "s_waitcnt lgkmcnt(12)\n\t" \
    "v_mfma_f32_16x16x32_bf16 v[4:7], v[196:199], v[128:131], v[4:7]\n\t" \
    "ds_read_b64_tr_b16 v[210:211], v226 offset:4608\n\t" \
    "s_waitcnt lgkmcnt(11)\n\t" \
    "v_mfma_f32_16x16x32_bf16 v[8:11], v[200:203], v[128:131], v[8:11]\n\t" \
    "ds_read_b128 v[160:163], v225 offset:0\n\t" \
    "s_waitcnt lgkmcnt(10)\n\t" \
    "v_mfma_f32_16x16x32_bf16 v[12:15], v[204:207], v[128:131], v[12:15]\n\t" \
    "ds_read_b64_tr_b16 v[212:213], v227 offset:4096\n\t" \
    "s_waitcnt lgkmcnt(10)\n\t" \
    "v_mfma_f32_16x16x32_bf16 v[16:19], v[192:195], v[132:135], v[16:19]\n\t" \
    "ds_read_b64_tr_b16 v[214:215], v227 offset:4608\n\t" \
    "v_mfma_f32_16x16x32_bf16 v[20:23], v[196:199], v[132:135], v[20:23]\n\t" \
    "ds_read_b64_tr_b16 v[216:217], v228 offset:4096\n\t" \
    "v_mfma_f32_16x16x32_bf16 v[24:27], v[200:203], v[132:135], v[24:27]\n\t" \
    "ds_read_b64_tr_b16 v[218:219], v228 offset:4608\n\t" \
    "v_mfma_f32_16x16x32_bf16 v[28:31], v[204:207], v[132:135], v[28:31]\n\t" \
    "ds_read_b64_tr_b16 v[220:221], v229 offset:4096\n\t" \
    "s_waitcnt lgkmcnt(13)\n\t" \
    "v_mfma_f32_16x16x32_bf16 v[32:35], v[192:195], v[136:139], v[32:35]\n\t" \
    "ds_read_b64_tr_b16 v[222:223], v229 offset:4608\n\t" \
    "v_mfma_f32_16x16x32_bf16 v[36:39], v[196:199], v[136:139], v[36:39]\n\t" \
    "ds_read_b128 v[164:167], v225 offset:2048\n\t" \
    "v_mfma_f32_16x16x32_bf16 v[40:43], v[200:203], v[136:139], v[40:43]\n\t" \
    "ds_read_b128 v[168:171], v225 offset:4096\n\t" \
    "v_mfma_f32_16x16x32_bf16 v[44:47], v[204:207], v[136:139], v[44:47]\n\t" \
    "ds_read_b128 v[172:175], v225 offset:6144\n\t" \
    "s_waitcnt lgkmcnt(15)\n\t" \
    "v_mfma_f32_16x16x32_bf16 v[48:51], v[192:195], v[140:143], v[48:51]\n\t" \
    "ds_read_b128 v[176:179], v225 offset:8192\n\t" \
    "v_mfma_f32_16x16x32_bf16 v[52:55], v[196:199], v[140:143], v[52:55]\n\t" \
    "ds_read_b128 v[180:183], v225 offset:10240\n\t" \
    "v_mfma_f32_16x16x32_bf16 v[56:59], v[200:203], v[140:143], v[56:59]\n\t" \
    "ds_read_b128 v[184:187], v225 offset:12288\n\t" \
    "v_mfma_f32_16x16x32_bf16 v[60:63], v[204:207], v[140:143], v[60:63]\n\t" \
    "ds_read_b128 v[188:191], v225 offset:14336\n\t" \
    "v_mfma_f32_16x16x32_bf16 v[64:67], v[192:195], v[144:147], v[64:67]\n\t" \
    "s_add_u32 s51, s51, 49152\n\t" \
    "v_mfma_f32_16x16x32_bf16 v[68:71], v[196:199], v[144:147], v[68:71]\n\t" \
    "s_cmp_eq_u32 s51, 147456\n\t" \
    "v_mfma_f32_16x16x32_bf16 v[72:75], v[200:203], v[144:147], v[72:75]\n\t" \
    "s_cselect_b32 s50, s53, s54\n\t" \
    "v_mfma_f32_16x16x32_bf16 v[76:79], v[204:207], v[144:147], v[76:79]\n\t" \
    "s_cselect_b32 s51, 0, s51\n\t" \
    "s_waitcnt lgkmcnt(15)\n\t" \
    "v_mfma_f32_16x16x32_bf16 v[80:83], v[192:195], v[148:151], v[80:83]\n\t" \
    "v_add_u32 v224, s50, v224\n\t" \
    "v_mfma_f32_16x16x32_bf16 v[84:87], v[196:199], v[148:151], v[84:87]\n\t" \
    "v_add_u32 v225, s50, v225\n\t" \
    "v_mfma_f32_16x16x32_bf16 v[88:91], v[200:203], v[148:151], v[88:91]\n\t" \
    "v_add_u32 v226, s50, v226\n\t" \
    "v_mfma_f32_16x16x32_bf16 v[92:95], v[204:207], v[148:151], v[92:95]\n\t" \
    "v_add_u32 v227, s50, v227\n\t" \
    "v_mfma_f32_16x16x32_bf16 v[96:99], v[192:195], v[152:155], v[96:99]\n\t" \
    "v_add_u32 v228, s50, v228\n\t" \
    "v_mfma_f32_16x16x32_bf16 v[100:103], v[196:199], v[152:155], v[100:103]\n\t" \
    "v_add_u32 v229, s50, v229\n\t" \
    "v_mfma_f32_16x16x32_bf16 v[104:107], v[200:203], v[152:155], v[104:107]\n\t" \
    "v_mfma_f32_16x16x32_bf16 v[108:111], v[204:207], v[152:155], v[108:111]\n\t" \
    "v_mfma_f32_16x16x32_bf16 v[112:115], v[192:195], v[156:159], v[112:115]\n\t" \
    "v_mfma_f32_16x16x32_bf16 v[116:119], v[196:199], v[156:159], v[116:119]\n\t" \
    "v_mfma_f32_16x16x32_bf16 v[120:123], v[200:203], v[156:159], v[120:123]\n\t" \
    "v_mfma_f32_16x16x32_bf16 v[124:127], v[204:207], v[156:159], v[124:127]\n\t" \
    "s_waitcnt lgkmcnt(0)\n\t" \
    "s_barrier\n\t" \
    "v_mfma_f32_16x16x32_bf16 v[0:3], v[208:211], v[160:163], v[0:3]\n\t" \
    "ds_read_b64_tr_b16 v[192:193], v226 offset:0\n\t" \
    "v_mfma_f32_16x16x32_bf16 v[4:7], v[212:215], v[160:163], v[4:7]\n\t" \
    "ds_read_b64_tr_b16 v[194:195], v226 offset:512\n\t" \
    "v_mfma_f32_16x16x32_bf16 v[8:11], v[216:219], v[160:163], v[8:11]\n\t" \
    "ds_read_b128 v[128:131], v224 offset:0\n\t" \
    "v_mfma_f32_16x16x32_bf16 v[12:15], v[220:223], v[160:163], v[12:15]\n\t" \
    "ds_read_b64_tr_b16 v[196:197], v227 offset:0\n\t" \
    "v_mfma_f32_16x16x32_bf16 v[16:19], v[208:211], v[164:167], v[16:19]\n\t" \
    "ds_read_b64_tr_b16 v[198:199], v227 offset:512\n\t" \
    "v_mfma_f32_16x16x32_bf16 v[20:23], v[212:215], v[164:167], v[20:23]\n\t" \
    "ds_read_b64_tr_b16 v[200:201], v228 offset:0\n\t" \
    "v_mfma_f32_16x16x32_bf16 v[24:27], v[216:219], v[164:167], v[24:27]\n\t" \
    "ds_read_b64_tr_b16 v[202:203], v228 offset:512\n\t" \
    "v_mfma_f32_16x16x32_bf16 v[28:31], v[220:223], v[164:167], v[28:31]\n\t" \
    "ds_read_b64_tr_b16 v[204:205], v229 offset:0\n\t" \
    "v_mfma_f32_16x16x32_bf16 v[32:35], v[208:211], v[168:171], v[32:35]\n\t" \
    "ds_read_b64_tr_b16 v[206:207], v229 offset:512\n\t" \
    "v_mfma_f32_16x16x32_bf16 v[36:39], v[212:215], v[168:171], v[36:39]\n\t" \
    "ds_read_b128 v[132:135], v224 offset:2048\n\t" \
    "v_mfma_f32_16x16x32_bf16 v[40:43], v[216:219], v[168:171], v[40:43]\n\t" \
    "ds_read_b128 v[136:139], v224 offset:4096\n\t" \
    "v_mfma_f32_16x16x32_bf16 v[44:47], v[220:223], v[168:171], v[44:47]\n\t" \
    "ds_read_b128 v[140:143], v224 offset:6144\n\t" \
    "v_mfma_f32_16x16x32_bf16 v[48:51], v[208:211], v[172:175], v[48:51]\n\t" \
    "ds_read_b128 v[144:147], v224 offset:8192\n\t" \
    "v_mfma_f32_16x16x32_bf16 v[52:55], v[212:215], v[172:175], v[52:55]\n\t" \
    "ds_read_b128 v[148:151], v224 offset:10240\n\t" \
    "v_mfma_f32_16x16x32_bf16 v[56:59], v[216:219], v[172:175], v[56:59]\n\t" \
    "ds_read_b128 v[152:155], v224 offset:12288\n\t" \
    "v_mfma_f32_16x16x32_bf16 v[60:63], v[220:223], v[172:175], v[60:63]\n\t" \
    "ds_read_b128 v[156:159], v224 offset:14336\n\t" \
    "v_mfma_f32_16x16x32_bf16 v[64:67], v[208:211], v[176:179], v[64:67]\n\t" \
    "v_mfma_f32_16x16x32_bf16 v[68:71], v[212:215], v[176:179], v[68:71]\n\t" \
    "v_mfma_f32_16x16x32_bf16 v[72:75], v[216:219], v[176:179], v[72:75]\n\t" \
    "v_mfma_f32_16x16x32_bf16 v[76:79], v[220:223], v[176:179], v[76:79]\n\t" \
    "v_mfma_f32_16x16x32_bf16 v[80:83], v[208:211], v[180:183], v[80:83]\n\t" \
    "v_mfma_f32_16x16x32_bf16 v[84:87], v[212:215], v[180:183], v[84:87]\n\t" \
    "v_mfma_f32_16x16x32_bf16 v[88:91], v[216:219], v[180:183], v[88:91]\n\t" \
    "v_mfma_f32_16x16x32_bf16 v[92:95], v[220:223], v[180:183], v[92:95]\n\t" \
    "v_mfma_f32_16x16x32_bf16 v[96:99], v[208:211], v[184:187], v[96:99]\n\t" \
    "v_mfma_f32_16x16x32_bf16 v[100:103], v[212:215], v[184:187], v[100:103]\n\t" \
    "v_mfma_f32_16x16x32_bf16 v[104:107], v[216:219], v[184:187], v[104:107]\n\t" \
    "v_mfma_f32_16x16x32_bf16 v[108:111], v[220:223], v[184:187], v[108:111]\n\t" \
    "v_mfma_f32_16x16x32_bf16 v[112:115], v[208:211], v[188:191], v[112:115]\n\t" \
    "v_mfma_f32_16x16x32_bf16 v[116:119], v[212:215], v[188:191], v[116:119]\n\t" \
    "v_mfma_f32_16x16x32_bf16 v[120:123], v[216:219], v[188:191], v[120:123]\n\t" \
    "v_mfma_f32_16x16x32_bf16 v[124:127], v[220:223], v[188:191], v[124:127]\n\t" \
    "s_waitcnt lgkmcnt(13)\n\t" \
    "v_mfma_f32_16x16x32_bf16 v[0:3], v[192:195], v[128:131], v[0:3]\n\t" \
    "ds_read_b64_tr_b16 v[208:209], v226 offset:4096\n\t" \
    "s_waitcnt lgkmcnt(12)\n\t" \
    "v_mfma_f32_16x16x32_bf16 v[4:7], v[196:199], v[128:131], v[4:7]\n\t" \
    "ds_read_b64_tr_b16 v[210:211], v226 offset:4608\n\t" \
    "s_waitcnt lgkmcnt(11)\n\t" \
    "v_mfma_f32_16x16x32_bf16 v[8:11], v[200:203], v[128:131], v[8:11]\n\t" \
    "ds_read_b128 v[160:163], v225 offset:0\n\t" \
    "s_waitcnt lgkmcnt(10)\n\t" \
    "v_mfma_f32_16x16x32_bf16 v[12:15], v[204:207], v[128:131], v[12:15]\n\t" \
    "ds_read_b64_tr_b16 v[212:213], v227 offset:4096\n\t" \
    "s_waitcnt lgkmcnt(10)\n\t" \
    "v_mfma_f32_16x16x32_bf16 v[16:19], v[192:195], v[132:135], v[16:19]\n\t" \
    "ds_read_b64_tr_b16 v[214:215], v227 offset:4608\n\t" \
    "v_mfma_f32_16x16x32_bf16 v[20:23], v[196:199], v[132:135], v[20:23]\n\t" \
    "ds_read_b64_tr_b16 v[216:217], v228 offset:4096\n\t" \
    "v_mfma_f32_16x16x32_bf16 v[24:27], v[200:203], v[132:135], v[24:27]\n\t" \
    "ds_read_b64_tr_b16 v[218:219], v228 offset:4608\n\t" \
    "v_mfma_f32_16x16x32_bf16 v[28:31], v[204:207], v[132:135], v[28:31]\n\t" \
    "ds_read_b64_tr_b16 v[220:221], v229 offset:4096\n\t" \
    "s_waitcnt lgkmcnt(13)\n\t" \
    "v_mfma_f32_16x16x32_bf16 v[32:35], v[192:195], v[136:139], v[32:35]\n\t" \
    "ds_read_b64_tr_b16 v[222:223], v229 offset:4608\n\t" \
    "v_mfma_f32_16x16x32_bf16 v[36:39], v[196:199], v[136:139], v[36:39]\n\t" \
    "ds_read_b128 v[164:167], v225 offset:2048\n\t" \
    "v_mfma_f32_16x16x32_bf16 v[40:43], v[200:203], v[136:139], v[40:43]\n\t" \
    "ds_read_b128 v[168:171], v225 offset:4096\n\t" \
    "v_mfma_f32_16x16x32_bf16 v[44:47], v[204:207], v[136:139], v[44:47]\n\t" \
    "ds_read_b128 v[172:175], v225 offset:6144\n\t" \
    "s_waitcnt lgkmcnt(15)\n\t" \
    "v_mfma_f32_16x16x32_bf16 v[48:51], v[192:195], v[140:143], v[48:51]\n\t" \
    "ds_read_b128 v[176:179], v225 offset:8192\n\t" \
    "v_mfma_f32_16x16x32_bf16 v[52:55], v[196:199], v[140:143], v[52:55]\n\t" \
    "ds_read_b128 v[180:183], v225 offset:10240\n\t" \
    "v_mfma_f32_16x16x32_bf16 v[56:59], v[200:203], v[140:143], v[56:59]\n\t" \
    "ds_read_b128 v[184:187], v225 offset:12288\n\t" \
    "v_mfma_f32_16x16x32_bf16 v[60:63], v[204:207], v[140:143], v[60:63]\n\t" \
    "ds_read_b128 v[188:191], v225 offset:14336\n\t" \
    "v_mfma_f32_16x16x32_bf16 v[64:67], v[192:195], v[144:147], v[64:67]\n\t" \
    "s_add_u32 s51, s51, 49152\n\t" \
    "v_mfma_f32_16x16x32_bf16 v[68:71], v[196:199], v[144:147], v[68:71]\n\t" \
    "s_cmp_eq_u32 s51, 147456\n\t" \
    "v_mfma_f32_16x16x32_bf16 v[72:75], v[200:203], v[144:147], v[72:75]\n\t" \
    "s_cselect_b32 s50, s53, s54\n\t" \
    "v_mfma_f32_16x16x32_bf16 v[76:79], v[204:207], v[144:147], v[76:79]\n\t" \
    "s_cselect_b32 s51, 0, s51\n\t" \
    "s_waitcnt lgkmcnt(15)\n\t" \
    "v_mfma_f32_16x16x32_bf16 v[80:83], v[192:195], v[148:151], v[80:83]\n\t" \
    "v_add_u32 v224, s50, v224\n\t" \
    "v_mfma_f32_16x16x32_bf16 v[84:87], v[196:199], v[148:151], v[84:87]\n\t" \
    "v_add_u32 v225, s50, v225\n\t" \
    "v_mfma_f32_16x16x32_bf16 v[88:91], v[200:203], v[148:151], v[88:91]\n\t" \
    "v_add_u32 v226, s50, v226\n\t" \
    "v_mfma_f32_16x16x32_bf16 v[92:95], v[204:207], v[148:151], v[92:95]\n\t" \
    "v_add_u32 v227, s50, v227\n\t" \
    "v_mfma_f32_16x16x32_bf16 v[96:99], v[192:195], v[152:155], v[96:99]\n\t" \
    "v_add_u32 v228, s50, v228\n\t" \
    "v_mfma_f32_16x16x32_bf16 v[100:103], v[196:199], v[152:155], v[100:103]\n\t" \
    "v_add_u32 v229, s50, v229\n\t" \
    "v_mfma_f32_16x16x32_bf16 v[104:107], v[200:203], v[152:155], v[104:107]\n\t" \
    "v_mfma_f32_16x16x32_bf16 v[108:111], v[204:207], v[152:155], v[108:111]\n\t" \
    "v_mfma_f32_16x16x32_bf16 v[112:115], v[192:195], v[156:159], v[112:115]\n\t" \
    "v_mfma_f32_16x16x32_bf16 v[116:119], v[196:199], v[156:159], v[116:119]\n\t" \
    "v_mfma_f32_16x16x32_bf16 v[120:123], v[200:203], v[156:159], v[120:123]\n\t" \
    "v_mfma_f32_16x16x32_bf16 v[124:127], v[204:207], v[156:159], v[124:127]\n\t" \
    "s_waitcnt lgkmcnt(0)\n\t" \
    "s_barrier\n\t" \
    "v_mfma_f32_16x16x32_bf16 v[0:3], v[208:211], v[160:163], v[0:3]\n\t" \
    "ds_read_b64_tr_b16 v[192:193], v226 offset:0\n\t" \
    "v_mfma_f32_16x16x32_bf16 v[4:7], v[212:215], v[160:163], v[4:7]\n\t" \
    "ds_read_b64_tr_b16 v[194:195], v226 offset:512\n\t" \
    "v_mfma_f32_16x16x32_bf16 v[8:11], v[216:219], v[160:163], v[8:11]\n\t" \
    "ds_read_b128 v[128:131], v224 offset:0\n\t" \
    "v_mfma_f32_16x16x32_bf16 v[12:15], v[220:223], v[160:163], v[12:15]\n\t" \
    "ds_read_b64_tr_b16 v[196:197], v227 offset:0\n\t" \
    "v_mfma_f32_16x16x32_bf16 v[16:19], v[208:211], v[164:167], v[16:19]\n\t" \
    "ds_read_b64_tr_b16 v[198:199], v227 offset:512\n\t" \
    "v_mfma_f32_16x16x32_bf16 v[20:23], v[212:215], v[164:167], v[20:23]\n\t" \
    "ds_read_b64_tr_b16 v[200:201], v228 offset:0\n\t" \
    "v_mfma_f32_16x16x32_bf16 v[24:27], v[216:219], v[164:167], v[24:27]\n\t" \
    "ds_read_b64_tr_b16 v[202:203], v228 offset:512\n\t" \
    "v_mfma_f32_16x16x32_bf16 v[28:31], v[220:223], v[164:167], v[28:31]\n\t" \
    "ds_read_b64_tr_b16 v[204:205], v229 offset:0\n\t" \
    "v_mfma_f32_16x16x32_bf16 v[32:35], v[208:211], v[168:171], v[32:35]\n\t" \
    "ds_read_b64_tr_b16 v[206:207], v229 offset:512\n\t" \
    "v_mfma_f32_16x16x32_bf16 v[36:39], v[212:215], v[168:171], v[36:39]\n\t" \
    "ds_read_b128 v[132:135], v224 offset:2048\n\t" \
    "v_mfma_f32_16x16x32_bf16 v[40:43], v[216:219], v[168:171], v[40:43]\n\t" \
    "ds_read_b128 v[136:139], v224 offset:4096\n\t" \
    "v_mfma_f32_16x16x32_bf16 v[44:47], v[220:223], v[168:171], v[44:47]\n\t" \
    "ds_read_b128 v[140:143], v224 offset:6144\n\t" \
    "v_mfma_f32_16x16x32_bf16 v[48:51], v[208:211], v[172:175], v[48:51]\n\t" \
    "ds_read_b128 v[144:147], v224 offset:8192\n\t" \
    "v_mfma_f32_16x16x32_bf16 v[52:55], v[212:215], v[172:175], v[52:55]\n\t" \
    "ds_read_b128 v[148:151], v224 offset:10240\n\t" \
    "v_mfma_f32_16x16x32_bf16 v[56:59], v[216:219], v[172:175], v[56:59]\n\t" \
    "ds_read_b128 v[152:155], v224 offset:12288\n\t" \
    "v_mfma_f32_16x16x32_bf16 v[60:63], v[220:223], v[172:175], v[60:63]\n\t" \
    "ds_read_b128 v[156:159], v224 offset:14336\n\t" \
    "v_mfma_f32_16x16x32_bf16 v[64:67], v[208:211], v[176:179], v[64:67]\n\t" \
    "v_mfma_f32_16x16x32_bf16 v[68:71], v[212:215], v[176:179], v[68:71]\n\t" \
    "v_mfma_f32_16x16x32_bf16 v[72:75], v[216:219], v[176:179], v[72:75]\n\t" \
    "v_mfma_f32_16x16x32_bf16 v[76:79], v[220:223], v[176:179], v[76:79]\n\t" \
    "v_mfma_f32_16x16x32_bf16 v[80:83], v[208:211], v[180:183], v[80:83]\n\t" \
    "v_mfma_f32_16x16x32_bf16 v[84:87], v[212:215], v[180:183], v[84:87]\n\t" \
    "v_mfma_f32_16x16x32_bf16 v[88:91], v[216:219], v[180:183], v[88:91]\n\t" \
    "v_mfma_f32_16x16x32_bf16 v[92:95], v[220:223], v[180:183], v[92:95]\n\t" \
    "v_mfma_f32_16x16x32_bf16 v[96:99], v[208:211], v[184:187], v[96:99]\n\t" \
    "v_mfma_f32_16x16x32_bf16 v[100:103], v[212:215], v[184:187], v[100:103]\n\t" \
    "v_mfma_f32_16x16x32_bf16 v[104:107], v[216:219], v[184:187], v[104:107]\n\t" \
    "v_mfma_f32_16x16x32_bf16 v[108:111], v[220:223], v[184:187], v[108:111]\n\t" \
    "v_mfma_f32_16x16x32_bf16 v[112:115], v[208:211], v[188:191], v[112:115]\n\t" \
    "v_mfma_f32_16x16x32_bf16 v[116:119], v[212:215], v[188:191], v[116:119]\n\t" \
    "v_mfma_f32_16x16x32_bf16 v[120:123], v[216:219], v[188:191], v[120:123]\n\t" \
    "v_mfma_f32_16x16x32_bf16 v[124:127], v[220:223], v[188:191], v[124:127]\n\t" \
    "s_waitcnt vmcnt(0) lgkmcnt(0)\n\t" \
    "s_nop 9\n\t" \
    "s_branch Lend_%=\n\t" \
    "Lld_%=:\n\t" \
    "s_mov_b64 s[40:41], %[pp]\n\t" \
    "s_and_b32 s41, s41, 0xffff\n\t" \
    "s_mov_b32 s42, %[bp]\n\t" \
    "s_mov_b32 s43, 0x20000\n\t" \
    "s_mov_b64 s[44:45], %[pq]\n\t" \
    "s_and_b32 s45, s45, 0xffff\n\t" \
    "s_mov_b32 s46, %[bq]\n\t" \
    "s_mov_b32 s47, 0x20000\n\t" \
    "s_mov_b32 s54, 131072\n\t" \
    "s_mov_b32 s55, 262144\n\t" \
    "s_mov_b32 s56, 393216\n\t" \
    "s_mov_b32 s57, 524288\n\t" \
    "s_mov_b32 s58, 655360\n\t" \
    "s_mov_b32 s59, 786432\n\t" \
    "s_mov_b32 s60, 917504\n\t" \
    "s_mov_b32 s61, 65536\n\t" \
    "s_mov_b32 s62, 128\n\t" \
    "s_mov_b32 s63, 65664\n\t" \
    "v_mov_b32 v0, %[vp]\n\t" \
    "v_mov_b32 v1, %[vq]\n\t" \
    "s_mov_b32 s48, %[m0b]\n\t" \
    "s_mov_b32 s51, 0\n\t" \
    "s_add_u32 m0, s48, 0\n\t" \
    "s_nop 0\n\t" \
    "buffer_load_dwordx4 v0, s[40:43], 0 offen lds\n\t" \
    "s_add_u32 m0, s48, 4096\n\t" \
    "s_nop 0\n\t" \
    "buffer_load_dwordx4 v0, s[40:43], s54 offen lds\n\t" \
    "s_add_u32 m0, s48, 8192\n\t" \
    "s_nop 0\n\t" \
    "buffer_load_dwordx4 v0, s[40:43], s55 offen lds\n\t" \
    "s_add_u32 m0, s48, 12288\n\t" \
    "s_nop 0\n\t" \
    "buffer_load_dwordx4 v0, s[40:43], s56 offen lds\n\t" \
    "s_add_u32 m0, s48, 16384\n\t" \
    "s_nop 0\n\t" \
    "buffer_load_dwordx4 v0, s[40:43], s57 offen lds\n\t" \
    "s_add_u32 m0, s48, 20480\n\t" \
    "s_nop 0\n\t" \
    "buffer_load_dwordx4 v0, s[40:43], s58 offen lds\n\t" \
    "s_add_u32 m0, s48, 24576\n\t" \
    "s_nop 0\n\t" \
    "buffer_load_dwordx4 v0, s[40:43], s59 offen lds\n\t" \
    "s_add_u32 m0, s48, 28672\n\t" \
    "s_nop 0\n\t" \
    "buffer_load_dwordx4 v0, s[40:43], s60 offen lds\n\t" \
    "s_add_u32 m0, s48, 32768\n\t" \
    "s_nop 0\n\t" \
    "buffer_load_dwordx4 v1, s[44:47], 0 offen lds\n\t" \
    "s_add_u32 m0, s48, 36864\n\t" \
    "s_nop 0\n\t" \
    "buffer_load_dwordx4 v1, s[44:47], s61 offen lds\n\t" \
    "s_add_u32 m0, s48, 40960\n\t" \
    "s_nop 0\n\t" \
    "buffer_load_dwordx4 v1, s[44:47], s62 offen lds\n\t" \
    "s_add_u32 m0, s48, 45056\n\t" \
    "s_nop 0\n\t" \
    "buffer_load_dwordx4 v1, s[44:47], s63 offen lds\n\t" \
    "s_add_u32 s40, s40, 128\n\t" \
    "s_addc_u32 s41, s41, 0\n\t" \
    "s_add_u32 s44, s44, 131072\n\t" \
    "s_addc_u32 s45, s45, 0\n\t" \
    "s_add_u32 s51, s51, 49152\n\t" \
    "s_cmp_eq_u32 s51, 147456\n\t" \
    "s_cselect_b32 s51, 0, s51\n\t" \
    "s_add_u32 s48, %[m0b], s51\n\t" \
    "s_add_u32 m0, s48, 0\n\t" \
    "s_nop 0\n\t" \
    "buffer_load_dwordx4 v0, s[40:43], 0 offen lds\n\t" \
    "s_add_u32 m0, s48, 4096\n\t" \
    "s_nop 0\n\t" \
    "buffer_load_dwordx4 v0, s[40:43], s54 offen lds\n\t" \
    "s_add_u32 m0, s48, 8192\n\t" \
    "s_nop 0\n\t" \
    "buffer_load_dwordx4 v0, s[40:43], s55 offen lds\n\t" \
    "s_add_u32 m0, s48, 12288\n\t" \
    "s_nop 0\n\t" \
    "buffer_load_dwordx4 v0, s[40:43], s56 offen lds\n\t" \
    "s_add_u32 m0, s48, 16384\n\t" \
    "s_nop 0\n\t" \
    "buffer_load_dwordx4 v0, s[40:43], s57 offen lds\n\t" \
    "s_add_u32 m0, s48, 20480\n\t" \
    "s_nop 0\n\t" \
    "buffer_load_dwordx4 v0, s[40:43], s58 offen lds\n\t" \
    "s_add_u32 m0, s48, 24576\n\t" \
    "s_nop 0\n\t" \
    "buffer_load_dwordx4 v0, s[40:43], s59 offen lds\n\t" \
    "s_add_u32 m0, s48, 28672\n\t" \
    "s_nop 0\n\t" \
    "buffer_load_dwordx4 v0, s[40:43], s60 offen lds\n\t" \
    "s_add_u32 m0, s48, 32768\n\t" \
    "s_nop 0\n\t" \
    "buffer_load_dwordx4 v1, s[44:47], 0 offen lds\n\t" \
    "s_add_u32 m0, s48, 36864\n\t" \
    "s_nop 0\n\t" \
    "buffer_load_dwordx4 v1, s[44:47], s61 offen lds\n\t" \
    "s_add_u32 m0, s48, 40960\n\t" \
    "s_nop 0\n\t" \
    "buffer_load_dwordx4 v1, s[44:47], s62 offen lds\n\t" \
    "s_add_u32 m0, s48, 45056\n\t" \
    "s_nop 0\n\t" \
    "buffer_load_dwordx4 v1, s[44:47], s63 offen lds\n\t" \
    "s_add_u32 s40, s40, 128\n\t" \
    "s_addc_u32 s41, s41, 0\n\t" \
    "s_add_u32 s44, s44, 131072\n\t" \
    "s_addc_u32 s45, s45, 0\n\t" \
    "s_add_u32 s51, s51, 49152\n\t" \
    "s_cmp_eq_u32 s51, 147456\n\t" \
    "s_cselect_b32 s51, 0, s51\n\t" \
    "s_add_u32 s48, %[m0b], s51\n\t" \
    "s_waitcnt vmcnt(12)\n\t" \
    "s_barrier\n\t" \
    "s_add_u32 m0, s48, 0\n\t" \
    "s_nop 0\n\t" \
    "buffer_load_dwordx4 v0, s[40:43], 0 offen lds\n\t" \
    "s_add_u32 m0, s48, 4096\n\t" \
    "s_nop 0\n\t" \
    "buffer_load_dwordx4 v0, s[40:43], s54 offen lds\n\t" \
    "s_add_u32 m0, s48, 8192\n\t" \
    "s_nop 0\n\t" \
    "buffer_load_dwordx4 v0, s[40:43], s55 offen lds\n\t" \
    "s_add_u32 m0, s48, 12288\n\t" \
    "s_nop 0\n\t" \
    "buffer_load_dwordx4 v0, s[40:43], s56 offen lds\n\t" \
    "s_add_u32 m0, s48, 16384\n\t" \
    "s_nop 0\n\t" \
    "buffer_load_dwordx4 v0, s[40:43], s57 offen lds\n\t" \
    "s_add_u32 m0, s48, 20480\n\t" \
    "s_nop 0\n\t" \
    "buffer_load_dwordx4 v0, s[40:43], s58 offen lds\n\t" \
    "s_add_u32 m0, s48, 24576\n\t" \
    "s_nop 0\n\t" \
    "buffer_load_dwordx4 v0, s[40:43], s59 offen lds\n\t" \
    "s_add_u32 m0, s48, 28672\n\t" \
    "s_nop 0\n\t" \
    "buffer_load_dwordx4 v0, s[40:43], s60 offen lds\n\t" \
    "s_add_u32 m0, s48, 32768\n\t" \
    "s_nop 0\n\t" \
    "buffer_load_dwordx4 v1, s[44:47], 0 offen lds\n\t" \
    "s_add_u32 m0, s48, 36864\n\t" \
    "s_nop 0\n\t" \
    "buffer_load_dwordx4 v1, s[44:47], s61 offen lds\n\t" \
    "s_add_u32 m0, s48, 40960\n\t" \
    "s_nop 0\n\t" \
    "buffer_load_dwordx4 v1, s[44:47], s62 offen lds\n\t" \
    "s_add_u32 m0, s48, 45056\n\t" \
    "s_nop 0\n\t" \
    "buffer_load_dwordx4 v1, s[44:47], s63 offen lds\n\t" \
    "s_add_u32 s40, s40, 128\n\t" \
    "s_addc_u32 s41, s41, 0\n\t" \
    "s_add_u32 s44, s44, 131072\n\t" \
    "s_addc_u32 s45, s45, 0\n\t" \
    "s_add_u32 s51, s51, 49152\n\t" \
    "s_cmp_eq_u32 s51, 147456\n\t" \
    "s_cselect_b32 s51, 0, s51\n\t" \
    "s_add_u32 s48, %[m0b], s51\n\t" \
    "s_mov_b32 s49, 3\n\t" \
    "Lldl_%=:\n\t" \
    "s_waitcnt vmcnt(12)\n\t" \
    "s_barrier\n\t" \
    "s_add_u32 m0, s48, 0\n\t" \
    "s_nop 0\n\t" \
    "buffer_load_dwordx4 v0, s[40:43], 0 offen lds\n\t" \
    "s_add_u32 m0, s48, 4096\n\t" \
    "s_nop 0\n\t" \
    "buffer_load_dwordx4 v0, s[40:43], s54 offen lds\n\t" \
    "s_add_u32 m0, s48, 8192\n\t" \
    "s_nop 0\n\t" \
    "buffer_load_dwordx4 v0, s[40:43], s55 offen lds\n\t" \
    "s_add_u32 m0, s48, 12288\n\t" \
    "s_nop 0\n\t" \
    "buffer_load_dwordx4 v0, s[40:43], s56 offen lds\n\t" \
    "s_add_u32 m0, s48, 16384\n\t" \
    "s_nop 0\n\t" \
    "buffer_load_dwordx4 v0, s[40:43], s57 offen lds\n\t" \
    "s_add_u32 m0, s48, 20480\n\t" \
    "s_nop 0\n\t" \
    "buffer_load_dwordx4 v0, s[40:43], s58 offen lds\n\t" \
    "s_add_u32 m0, s48, 24576\n\t" \
    "s_nop 0\n\t" \
    "buffer_load_dwordx4 v0, s[40:43], s59 offen lds\n\t" \
    "s_add_u32 m0, s48, 28672\n\t" \
    "s_nop 0\n\t" \
    "buffer_load_dwordx4 v0, s[40:43], s60 offen lds\n\t" \
    "s_add_u32 m0, s48, 32768\n\t" \
    "s_nop 0\n\t" \
    "buffer_load_dwordx4 v1, s[44:47], 0 offen lds\n\t" \
    "s_add_u32 m0, s48, 36864\n\t" \
    "s_nop 0\n\t" \
    "buffer_load_dwordx4 v1, s[44:47], s61 offen lds\n\t" \
    "s_add_u32 m0, s48, 40960\n\t" \
    "s_nop 0\n\t" \
    "buffer_load_dwordx4 v1, s[44:47], s62 offen lds\n\t" \
    "s_add_u32 m0, s48, 45056\n\t" \
    "s_nop 0\n\t" \
    "buffer_load_dwordx4 v1, s[44:47], s63 offen lds\n\t" \
    "s_add_u32 s40, s40, 128\n\t" \
    "s_addc_u32 s41, s41, 0\n\t" \
    "s_add_u32 s44, s44, 131072\n\t" \
    "s_addc_u32 s45, s45, 0\n\t" \
    "s_add_u32 s51, s51, 49152\n\t" \
    "s_cmp_eq_u32 s51, 147456\n\t" \
    "s_cselect_b32 s51, 0, s51\n\t" \
    "s_add_u32 s48, %[m0b], s51\n\t" \
    "s_add_u32 s49, s49, 1\n\t" \
    "s_cmp_lt_u32 s49, 32\n\t" \
    "s_cbranch_scc1 Lldl_%=\n\t" \
    "s_waitcnt vmcnt(12)\n\t" \
    "s_barrier\n\t" \
    "s_waitcnt vmcnt(0)\n\t" \
    "s_barrier\n\t" \
    "s_waitcnt vmcnt(0)\n\t" \
    "s_barrier\n\t" \
    "s_branch Lend_%=\n\t" \
    "Lend_%=:\n\t" \
    "s_waitcnt vmcnt(0)\n\t" \
    "v_readfirstlane_b32 s82, v250\n\t" \
    "v_readfirstlane_b32 s83, v251\n\t" \
    "s_nop 3\n\t"
#define UU_CLOB "v128", "v129", "v130", "v131", "v132", "v133", "v134", "v135", "v136", "v137", "v138", "v139", "v140", "v141", "v142", "v143", "v144", "v145", "v146", "v147", "v148", "v149", "v150", "v151", "v152", "v153", "v154", "v155", "v156", "v157", "v158", "v159", "v160", "v161", "v162", "v163", "v164", "v165", "v166", "v167", "v168", "v169", "v170", "v171", "v172", "v173", "v174", "v175", "v176", "v177", "v178", "v179", "v180", "v181", "v182", "v183", "v184", "v185", "v186", "v187", "v188", "v189", "v190", "v191", "v192", "v193", "v194", "v195", "v196", "v197", "v198", "v199", "v200", "v201", "v202", "v203", "v204", "v205", "v206", "v207", "v208", "v209", "v210", "v211", "v212", "v213", "v214", "v215", "v216", "v217", "v218", "v219", "v220", "v221", "v222", "v223", "v224", "v225", "v226", "v227", "v228", "v229", "v250", "v251", "v252", "v253", "v254", "v255", "s40", "s41", "s42", "s43", "s44", "s45", "s46", "s47", "s48", "s49", "s50", "s51", "s52", "s53", "s54", "s55", "s56", "s57", "s58", "s59", "s60", "s61", "s62", "s63", "s70", "s71", "s72", "s73", "s74", "s75", "s76", "s77", "s78", "s79", "s80", "s81", "vcc", "scc", "memory"
#define Y_ASM \
    "s_nop 4\n\t" \
    "s_cmp_lg_u32 %[isld], 0\n\t" \
    "s_cbranch_scc1 Lld_%=\n\t" \
    "v_mov_b32 v128, %[ap]\n\t" \
    "v_mov_b32 v130, %[aq]\n\t" \
    "v_xor_b32 v131, 64, v130\n\t" \
    "v_mov_b32 v0, 0\n\t" \
    "v_mov_b32 v1, 0\n\t" \
    "v_mov_b32 v2, 0\n\t" \
    "v_mov_b32 v3, 0\n\t" \
    "v_mov_b32 v4, 0\n\t" \
    "v_mov_b32 v5, 0\n\t" \
    "v_mov_b32 v6, 0\n\t" \
    "v_mov_b32 v7, 0\n\t" \
    "v_mov_b32 v8, 0\n\t" \
    "v_mov_b32 v9, 0\n\t" \
    "v_mov_b32 v10, 0\n\t" \
    "v_mov_b32 v11, 0\n\t" \
    "v_mov_b32 v12, 0\n\t" \
    "v_mov_b32 v13, 0\n\t" \
    "v_mov_b32 v14, 0\n\t" \
    "v_mov_b32 v15, 0\n\t" \
    "v_mov_b32 v16, 0\n\t" \
    "v_mov_b32 v17, 0\n\t" \
    "v_mov_b32 v18, 0\n\t" \
    "v_mov_b32 v19, 0\n\t" \
    "v_mov_b32 v20, 0\n\t" \
    "v_mov_b32 v21, 0\n\t" \
    "v_mov_b32 v22, 0\n\t" \
    "v_mov_b32 v23, 0\n\t" \
    "v_mov_b32 v24, 0\n\t" \
    "v_mov_b32 v25, 0\n\t" \
    "v_mov_b32 v26, 0\n\t" \
    "v_mov_b32 v27, 0\n\t" \
    "v_mov_b32 v28, 0\n\t" \
    "v_mov_b32 v29, 0\n\t" \
    "v_mov_b32 v30, 0\n\t" \
    "v_mov_b32 v31, 0\n\t" \
    "v_mov_b32 v32, 0\n\t" \
    "v_mov_b32 v33, 0\n\t" \
    "v_mov_b32 v34, 0\n\t" \
    "v_mov_b32 v35, 0\n\t" \
    "v_mov_b32 v36, 0\n\t" \
    "v_mov_b32 v37, 0\n\t" \
    "v_mov_b32 v38, 0\n\t" \
    "v_mov_b32 v39, 0\n\t" \
    "v_mov_b32 v40, 0\n\t" \
    "v_mov_b32 v41, 0\n\t" \
    "v_mov_b32 v42, 0\n\t" \
    "v_mov_b32 v43, 0\n\t" \
    "v_mov_b32 v44, 0\n\t" \
    "v_mov_b32 v45, 0\n\t" \
    "v_mov_b32 v46, 0\n\t" \
    "v_mov_b32 v47, 0\n\t" \
    "v_mov_b32 v48, 0\n\t" \
    "v_mov_b32 v49, 0\n\t" \
    "v_mov_b32 v50, 0\n\t" \
    "v_mov_b32 v51, 0\n\t" \
    "v_mov_b32 v52, 0\n\t" \
    "v_mov_b32 v53, 0\n\t" \
    "v_mov_b32 v54, 0\n\t" \
    "v_mov_b32 v55, 0\n\t" \
    "v_mov_b32 v56, 0\n\t" \
    "v_mov_b32 v57, 0\n\t" \
    "v_mov_b32 v58, 0\n\t" \
    "v_mov_b32 v59, 0\n\t" \
    "v_mov_b32 v60, 0\n\t" \
    "v_mov_b32 v61, 0\n\t" \
    "v_mov_b32 v62, 0\n\t" \
    "v_mov_b32 v63, 0\n\t" \
    "s_mov_b32 s51, 0\n\t" \
    "s_mov_b32 s49, 32\n\t" \
    "s_mov_b32 s53, 4294836224\n\t" \
    "s_mov_b32 s54, 32768\n\t" \
    "s_barrier\n\t" \
    "ds_read_b128 v[96:99], v130 offset:0\n\t" \
    "ds_read_b128 v[64:67], v128 offset:0\n\t" \
    "ds_read_b128 v[100:103], v130 offset:2048\n\t" \
    "ds_read_b128 v[104:107], v130 offset:4096\n\t" \
    "ds_read_b128 v[108:111], v130 offset:6144\n\t" \
    "ds_read_b128 v[68:71], v128 offset:2048\n\t" \
    "ds_read_b128 v[72:75], v128 offset:4096\n\t" \
    "ds_read_b128 v[76:79], v128 offset:6144\n\t" \
    "Lc_%=:\n\t" \
    "s_waitcnt lgkmcnt(6)\n\t" \
    "v_mfma_f32_16x16x32_bf16 v[0:3], v[96:99], v[64:67], v[0:3]\n\t" \
    "ds_read_b128 v[112:115], v131 offset:0\n\t" \
    "s_waitcnt lgkmcnt(6)\n\t" \
    "v_mfma_f32_16x16x32_bf16 v[4:7], v[100:103], v[64:67], v[4:7]\n\t" \
    "ds_read_b128 v[80:83], v128 offset:1024\n\t" \
    "s_waitcnt lgkmcnt(6)\n\t" \
    "v_mfma_f32_16x16x32_bf16 v[8:11], v[104:107], v[64:67], v[8:11]\n\t" \
    "ds_read_b128 v[116:119], v131 offset:2048\n\t" \
    "s_waitcnt lgkmcnt(6)\n\t" \
    "v_mfma_f32_16x16x32_bf16 v[12:15], v[108:111], v[64:67], v[12:15]\n\t" \
    "ds_read_b128 v[120:123], v131 offset:4096\n\t" \
    "s_waitcnt lgkmcnt(6)\n\t" \
    "v_mfma_f32_16x16x32_bf16 v[16:19], v[96:99], v[68:71], v[16:19]\n\t" \
    "ds_read_b128 v[124:127], v131 offset:6144\n\t" \
    "v_mfma_f32_16x16x32_bf16 v[20:23], v[100:103], v[68:71], v[20:23]\n\t" \
    "ds_read_b128 v[84:87], v128 offset:3072\n\t" \
    "v_mfma_f32_16x16x32_bf16 v[24:27], v[104:107], v[68:71], v[24:27]\n\t" \
    "ds_read_b128 v[88:91], v128 offset:5120\n\t" \
    "v_mfma_f32_16x16x32_bf16 v[28:31], v[108:111], v[68:71], v[28:31]\n\t" \
    "ds_read_b128 v[92:95], v128 offset:7168\n\t" \
    "s_waitcnt lgkmcnt(9)\n\t" \
    "v_mfma_f32_16x16x32_bf16 v[32:35], v[96:99], v[72:75], v[32:35]\n\t" \
    "s_add_u32 s51, s51, 32768\n\t" \
    "v_mfma_f32_16x16x32_bf16 v[36:39], v[100:103], v[72:75], v[36:39]\n\t" \
    "s_cmp_eq_u32 s51, 163840\n\t" \
    "v_mfma_f32_16x16x32_bf16 v[40:43], v[104:107], v[72:75], v[40:43]\n\t" \
    "s_cselect_b32 s50, s53, s54\n\t" \
    "v_mfma_f32_16x16x32_bf16 v[44:47], v[108:111], v[72:75], v[44:47]\n\t" \
    "s_cselect_b32 s51, 0, s51\n\t" \
    "s_waitcnt lgkmcnt(8)\n\t" \
    "v_mfma_f32_16x16x32_bf16 v[48:51], v[96:99], v[76:79], v[48:51]\n\t" \
    "v_add_u32 v128, s50, v128\n\t" \
    "v_mfma_f32_16x16x32_bf16 v[52:55], v[100:103], v[76:79], v[52:55]\n\t" \
    "v_add_u32 v130, s50, v130\n\t" \
    "v_mfma_f32_16x16x32_bf16 v[56:59], v[104:107], v[76:79], v[56:59]\n\t" \
    "v_add_u32 v131, s50, v131\n\t" \
    "v_mfma_f32_16x16x32_bf16 v[60:63], v[108:111], v[76:79], v[60:63]\n\t" \
    "s_waitcnt lgkmcnt(0)\n\t" \
    "s_barrier\n\t" \
    "v_mfma_f32_16x16x32_bf16 v[0:3], v[112:115], v[80:83], v[0:3]\n\t" \
    "ds_read_b128 v[96:99], v130 offset:0\n\t" \
    "v_mfma_f32_16x16x32_bf16 v[4:7], v[116:119], v[80:83], v[4:7]\n\t" \
    "ds_read_b128 v[64:67], v128 offset:0\n\t" \
    "v_mfma_f32_16x16x32_bf16 v[8:11], v[120:123], v[80:83], v[8:11]\n\t" \
    "ds_read_b128 v[100:103], v130 offset:2048\n\t" \
    "v_mfma_f32_16x16x32_bf16 v[12:15], v[124:127], v[80:83], v[12:15]\n\t" \
    "ds_read_b128 v[104:107], v130 offset:4096\n\t" \
    "v_mfma_f32_16x16x32_bf16 v[16:19], v[112:115], v[84:87], v[16:19]\n\t" \
    "ds_read_b128 v[108:111], v130 offset:6144\n\t" \
    "v_mfma_f32_16x16x32_bf16 v[20:23], v[116:119], v[84:87], v[20:23]\n\t" \
    "ds_read_b128 v[68:71], v128 offset:2048\n\t" \
    "v_mfma_f32_16x16x32_bf16 v[24:27], v[120:123], v[84:87], v[24:27]\n\t" \
    "ds_read_b128 v[72:75], v128 offset:4096\n\t" \
    "v_mfma_f32_16x16x32_bf16 v[28:31], v[124:127], v[84:87], v[28:31]\n\t" \
    "ds_read_b128 v[76:79], v128 offset:6144\n\t" \
    "v_mfma_f32_16x16x32_bf16 v[32:35], v[112:115], v[88:91], v[32:35]\n\t" \
    "s_sub_u32 s49, s49, 1\n\t" \
    "v_mfma_f32_16x16x32_bf16 v[36:39], v[116:119], v[88:91], v[36:39]\n\t" \
    "s_cmp_lg_u32 s49, 0\n\t" \
    "v_mfma_f32_16x16x32_bf16 v[40:43], v[120:123], v[88:91], v[40:43]\n\t" \
    "v_mfma_f32_16x16x32_bf16 v[44:47], v[124:127], v[88:91], v[44:47]\n\t" \
    "v_mfma_f32_16x16x32_bf16 v[48:51], v[112:115], v[92:95], v[48:51]\n\t" \
    "v_mfma_f32_16x16x32_bf16 v[52:55], v[116:119], v[92:95], v[52:55]\n\t" \
    "v_mfma_f32_16x16x32_bf16 v[56:59], v[120:123], v[92:95], v[56:59]\n\t" \
    "v_mfma_f32_16x16x32_bf16 v[60:63], v[124:127], v[92:95], v[60:63]\n\t" \
    "s_cbranch_scc1 Lc_%=\n\t" \
    "s_waitcnt lgkmcnt(0)\n\t" \
    "s_nop 9\n\t" \
    "s_branch Lend_%=\n\t" \
    "Lld_%=:\n\t" \
    "s_mov_b64 s[40:41], %[pp]\n\t" \
    "s_and_b32 s41, s41, 0xffff\n\t" \
    "s_mov_b32 s42, %[bp]\n\t" \
    "s_mov_b32 s43, 0x20000\n\t" \
    "s_mov_b64 s[44:45], %[pq]\n\t" \
    "s_and_b32 s45, s45, 0xffff\n\t" \
    "s_mov_b32 s46, %[bq]\n\t" \
    "s_mov_b32 s47, 0x20000\n\t" \
    "s_mov_b64 s[52:53], %[pq2]\n\t" \
    "s_and_b32 s53, s53, 0xffff\n\t" \
    "s_mov_b32 s54, 131072\n\t" \
    "s_mov_b32 s55, 262144\n\t" \
    "s_mov_b32 s56, 393216\n\t" \
    "s_mov_b32 s61, 65536\n\t" \
    "s_mov_b32 s62, 131072\n\t" \
    "s_mov_b32 s63, 196608\n\t" \
    "v_mov_b32 v0, %[vp]\n\t" \
    "v_mov_b32 v1, %[vq]\n\t" \
    "s_mov_b32 s48, %[m0b]\n\t" \
    "s_mov_b32 s51, 0\n\t" \
    "s_add_u32 m0, s48, 0\n\t" \
    "s_nop 0\n\t" \
    "buffer_load_dwordx4 v0, s[40:43], 0 offen sc1 lds\n\t" \
    "s_add_u32 m0, s48, 4096\n\t" \
    "s_nop 0\n\t" \
    "buffer_load_dwordx4 v0, s[40:43], s54 offen sc1 lds\n\t" \
    "s_add_u32 m0, s48, 8192\n\t" \
    "s_nop 0\n\t" \
    "buffer_load_dwordx4 v0, s[40:43], s55 offen sc1 lds\n\t" \
    "s_add_u32 m0, s48, 12288\n\t" \
    "s_nop 0\n\t" \
    "buffer_load_dwordx4 v0, s[40:43], s56 offen sc1 lds\n\t" \
    "s_add_u32 m0, s48, 16384\n\t" \
    "s_nop 0\n\t" \
    "buffer_load_dwordx4 v1, s[44:47], 0 offen lds\n\t" \
    "s_add_u32 m0, s48, 20480\n\t" \
    "s_nop 0\n\t" \
    "buffer_load_dwordx4 v1, s[44:47], s61 offen lds\n\t" \
    "s_add_u32 m0, s48, 24576\n\t" \
    "s_nop 0\n\t" \
    "buffer_load_dwordx4 v1, s[44:47], s62 offen lds\n\t" \
    "s_add_u32 m0, s48, 28672\n\t" \
    "s_nop 0\n\t" \
    "buffer_load_dwordx4 v1, s[44:47], s63 offen lds\n\t" \
    "s_add_u32 s40, s40, 2048\n\t" \
    "s_addc_u32 s41, s41, 0\n\t" \
    "s_add_u32 s44, s44, 128\n\t" \
    "s_addc_u32 s45, s45, 0\n\t" \
    "s_add_u32 s51, s51, 32768\n\t" \
    "s_cmp_eq_u32 s51, 163840\n\t" \
    "s_cselect_b32 s51, 0, s51\n\t" \
    "s_add_u32 s48, %[m0b], s51\n\t" \
    "s_add_u32 m0, s48, 0\n\t" \
    "s_nop 0\n\t" \
    "buffer_load_dwordx4 v0, s[40:43], 0 offen sc1 lds\n\t" \
    "s_add_u32 m0, s48, 4096\n\t" \
    "s_nop 0\n\t" \
    "buffer_load_dwordx4 v0, s[40:43], s54 offen sc1 lds\n\t" \
    "s_add_u32 m0, s48, 8192\n\t" \
    "s_nop 0\n\t" \
    "buffer_load_dwordx4 v0, s[40:43], s55 offen sc1 lds\n\t" \
    "s_add_u32 m0, s48, 12288\n\t" \
    "s_nop 0\n\t" \
    "buffer_load_dwordx4 v0, s[40:43], s56 offen sc1 lds\n\t" \
    "s_add_u32 m0, s48, 16384\n\t" \
    "s_nop 0\n\t" \
    "buffer_load_dwordx4 v1, s[44:47], 0 offen lds\n\t" \
    "s_add_u32 m0, s48, 20480\n\t" \
    "s_nop 0\n\t" \
    "buffer_load_dwordx4 v1, s[44:47], s61 offen lds\n\t" \
    "s_add_u32 m0, s48, 24576\n\t" \
    "s_nop 0\n\t" \
    "buffer_load_dwordx4 v1, s[44:47], s62 offen lds\n\t" \
    "s_add_u32 m0, s48, 28672\n\t" \
    "s_nop 0\n\t" \
    "buffer_load_dwordx4 v1, s[44:47], s63 offen lds\n\t" \
    "s_add_u32 s40, s40, 2048\n\t" \
    "s_addc_u32 s41, s41, 0\n\t" \
    "s_add_u32 s44, s44, 128\n\t" \
    "s_addc_u32 s45, s45, 0\n\t" \
    "s_add_u32 s51, s51, 32768\n\t" \
    "s_cmp_eq_u32 s51, 163840\n\t" \
    "s_cselect_b32 s51, 0, s51\n\t" \
    "s_add_u32 s48, %[m0b], s51\n\t" \
    "s_waitcnt vmcnt(8)\n\t" \
    "s_barrier\n\t" \
    "s_add_u32 m0, s48, 0\n\t" \
    "s_nop 0\n\t" \
    "buffer_load_dwordx4 v0, s[40:43], 0 offen sc1 lds\n\t" \
    "s_add_u32 m0, s48, 4096\n\t" \
    "s_nop 0\n\t" \
    "buffer_load_dwordx4 v0, s[40:43], s54 offen sc1 lds\n\t" \
    "s_add_u32 m0, s48, 8192\n\t" \
    "s_nop 0\n\t" \
    "buffer_load_dwordx4 v0, s[40:43], s55 offen sc1 lds\n\t" \
    "s_add_u32 m0, s48, 12288\n\t" \
    "s_nop 0\n\t" \
    "buffer_load_dwordx4 v0, s[40:43], s56 offen sc1 lds\n\t" \
    "s_add_u32 m0, s48, 16384\n\t" \
    "s_nop 0\n\t" \
    "buffer_load_dwordx4 v1, s[44:47], 0 offen lds\n\t" \
    "s_add_u32 m0, s48, 20480\n\t" \
    "s_nop 0\n\t" \
    "buffer_load_dwordx4 v1, s[44:47], s61 offen lds\n\t" \
    "s_add_u32 m0, s48, 24576\n\t" \
    "s_nop 0\n\t" \
    "buffer_load_dwordx4 v1, s[44:47], s62 offen lds\n\t" \
    "s_add_u32 m0, s48, 28672\n\t" \
    "s_nop 0\n\t" \
    "buffer_load_dwordx4 v1, s[44:47], s63 offen lds\n\t" \
    "s_add_u32 s40, s40, 2048\n\t" \
    "s_addc_u32 s41, s41, 0\n\t" \
    "s_add_u32 s44, s44, 128\n\t" \
    "s_addc_u32 s45, s45, 0\n\t" \
    "s_add_u32 s51, s51, 32768\n\t" \
    "s_cmp_eq_u32 s51, 163840\n\t" \
    "s_cselect_b32 s51, 0, s51\n\t" \
    "s_add_u32 s48, %[m0b], s51\n\t" \
    "s_add_u32 m0, s48, 0\n\t" \
    "s_nop 0\n\t" \
    "buffer_load_dwordx4 v0, s[40:43], 0 offen sc1 lds\n\t" \
    "s_add_u32 m0, s48, 4096\n\t" \
    "s_nop 0\n\t" \
    "buffer_load_dwordx4 v0, s[40:43], s54 offen sc1 lds\n\t" \
    "s_add_u32 m0, s48, 8192\n\t" \
    "s_nop 0\n\t" \
    "buffer_load_dwordx4 v0, s[40:43], s55 offen sc1 lds\n\t" \
    "s_add_u32 m0, s48, 12288\n\t" \
    "s_nop 0\n\t" \
    "buffer_load_dwordx4 v0, s[40:43], s56 offen sc1 lds\n\t" \
    "s_add_u32 m0, s48, 16384\n\t" \
    "s_nop 0\n\t" \
    "buffer_load_dwordx4 v1, s[44:47], 0 offen lds\n\t" \
    "s_add_u32 m0, s48, 20480\n\t" \
    "s_nop 0\n\t" \
    "buffer_load_dwordx4 v1, s[44:47], s61 offen lds\n\t" \
    "s_add_u32 m0, s48, 24576\n\t" \
    "s_nop 0\n\t" \
    "buffer_load_dwordx4 v1, s[44:47], s62 offen lds\n\t" \
    "s_add_u32 m0, s48, 28672\n\t" \
    "s_nop 0\n\t" \
    "buffer_load_dwordx4 v1, s[44:47], s63 offen lds\n\t" \
    "s_add_u32 s40, s40, 2048\n\t" \
    "s_addc_u32 s41, s41, 0\n\t" \
    "s_add_u32 s44, s44, 128\n\t" \
    "s_addc_u32 s45, s45, 0\n\t" \
    "s_add_u32 s51, s51, 32768\n\t" \
    "s_cmp_eq_u32 s51, 163840\n\t" \
    "s_cselect_b32 s51, 0, s51\n\t" \
    "s_add_u32 s48, %[m0b], s51\n\t" \
    "s_add_u32 m0, s48, 0\n\t" \
    "s_nop 0\n\t" \
    "buffer_load_dwordx4 v0, s[40:43], 0 offen sc1 lds\n\t" \
    "s_add_u32 m0, s48, 4096\n\t" \
    "s_nop 0\n\t" \
    "buffer_load_dwordx4 v0, s[40:43], s54 offen sc1 lds\n\t" \
    "s_add_u32 m0, s48, 8192\n\t" \
    "s_nop 0\n\t" \
    "buffer_load_dwordx4 v0, s[40:43], s55 offen sc1 lds\n\t" \
    "s_add_u32 m0, s48, 12288\n\t" \
    "s_nop 0\n\t" \
    "buffer_load_dwordx4 v0, s[40:43], s56 offen sc1 lds\n\t" \
    "s_add_u32 m0, s48, 16384\n\t" \
    "s_nop 0\n\t" \
    "buffer_load_dwordx4 v1, s[44:47], 0 offen lds\n\t" \
    "s_add_u32 m0, s48, 20480\n\t" \
    "s_nop 0\n\t" \
    "buffer_load_dwordx4 v1, s[44:47], s61 offen lds\n\t" \
    "s_add_u32 m0, s48, 24576\n\t" \
    "s_nop 0\n\t" \
    "buffer_load_dwordx4 v1, s[44:47], s62 offen lds\n\t" \
    "s_add_u32 m0, s48, 28672\n\t" \
    "s_nop 0\n\t" \
    "buffer_load_dwordx4 v1, s[44:47], s63 offen lds\n\t" \
    "s_add_u32 s40, s40, 2048\n\t" \
    "s_addc_u32 s41, s41, 0\n\t" \
    "s_add_u32 s44, s44, 128\n\t" \
    "s_addc_u32 s45, s45, 0\n\t" \
    "s_add_u32 s51, s51, 32768\n\t" \
    "s_cmp_eq_u32 s51, 163840\n\t" \
    "s_cselect_b32 s51, 0, s51\n\t" \
    "s_add_u32 s48, %[m0b], s51\n\t" \
    "s_mov_b32 s49, 5\n\t" \
    "Lldl_%=:\n\t" \
    "s_waitcnt vmcnt(24)\n\t" \
    "s_barrier\n\t" \
    "s_cmp_eq_u32 s49, 16\n\t" \
    "s_cselect_b64 s[44:45], s[52:53], s[44:45]\n\t" \
    "s_add_u32 m0, s48, 0\n\t" \
    "s_nop 0\n\t" \
    "buffer_load_dwordx4 v0, s[40:43], 0 offen sc1 lds\n\t" \
    "s_add_u32 m0, s48, 4096\n\t" \
    "s_nop 0\n\t" \
    "buffer_load_dwordx4 v0, s[40:43], s54 offen sc1 lds\n\t" \
    "s_add_u32 m0, s48, 8192\n\t" \
    "s_nop 0\n\t" \
    "buffer_load_dwordx4 v0, s[40:43], s55 offen sc1 lds\n\t" \
    "s_add_u32 m0, s48, 12288\n\t" \
    "s_nop 0\n\t" \
    "buffer_load_dwordx4 v0, s[40:43], s56 offen sc1 lds\n\t" \
    "s_add_u32 m0, s48, 16384\n\t" \
    "s_nop 0\n\t" \
    "buffer_load_dwordx4 v1, s[44:47], 0 offen lds\n\t" \
    "s_add_u32 m0, s48, 20480\n\t" \
    "s_nop 0\n\t" \
    "buffer_load_dwordx4 v1, s[44:47], s61 offen lds\n\t" \
    "s_add_u32 m0, s48, 24576\n\t" \
    "s_nop 0\n\t" \
    "buffer_load_dwordx4 v1, s[44:47], s62 offen lds\n\t" \
    "s_add_u32 m0, s48, 28672\n\t" \
    "s_nop 0\n\t" \
    "buffer_load_dwordx4 v1, s[44:47], s63 offen lds\n\t" \
    "s_add_u32 s40, s40, 2048\n\t" \
    "s_addc_u32 s41, s41, 0\n\t" \
    "s_add_u32 s44, s44, 128\n\t" \
    "s_addc_u32 s45, s45, 0\n\t" \
    "s_add_u32 s51, s51, 32768\n\t" \
    "s_cmp_eq_u32 s51, 163840\n\t" \
    "s_cselect_b32 s51, 0, s51\n\t" \
    "s_add_u32 s48, %[m0b], s51\n\t" \
    "s_add_u32 s49, s49, 1\n\t" \
    "s_cmp_lt_u32 s49, 32\n\t" \
    "s_cbranch_scc1 Lldl_%=\n\t" \
    "s_waitcnt vmcnt(24)\n\t" \
    "s_barrier\n\t" \
    "s_waitcnt vmcnt(16)\n\t" \
    "s_barrier\n\t" \
    "s_waitcnt vmcnt(8)\n\t" \
    "s_barrier\n\t" \
    "s_waitcnt vmcnt(0)\n\t" \
    "s_barrier\n\t" \
    "s_waitcnt vmcnt(0)\n\t" \
    "s_barrier\n\t" \
    "s_branch Lend_%=\n\t" \
    "Lend_%=:\n\t"
#define Y_CLOB "v64", "v65", "v66", "v67", "v68", "v69", "v70", "v71", "v72", "v73", "v74", "v75", "v76", "v77", "v78", "v79", "v80", "v81", "v82", "v83", "v84", "v85", "v86", "v87", "v88", "v89", "v90", "v91", "v92", "v93", "v94", "v95", "v96", "v97", "v98", "v99", "v100", "v101", "v102", "v103", "v104", "v105", "v106", "v107", "v108", "v109", "v110", "v111", "v112", "v113", "v114", "v115", "v116", "v117", "v118", "v119", "v120", "v121", "v122", "v123", "v124", "v125", "v126", "v127", "v128", "v129", "v130", "v131", "v132", "v133", "s40", "s41", "s42", "s43", "s44", "s45", "s46", "s47", "s48", "s49", "s50", "s51", "s52", "s53", "s54", "s55", "s56", "s57", "s58", "s59", "s60", "s61", "s62", "s63", "vcc", "scc", "memory"
#define OP_ASM \
    "s_nop 4\n\t" \
    "s_cmp_lg_u32 %[isld], 0\n\t" \
    "s_cbranch_scc1 Lld_%=\n\t" \
    "v_mov_b32 v128, %[ap]\n\t" \
    "v_mov_b32 v130, %[aq]\n\t" \
    "v_xor_b32 v131, 32, v130\n\t" \
    "v_xor_b32 v132, 64, v130\n\t" \
    "v_xor_b32 v133, 96, v130\n\t" \
    "v_mov_b32 v0, 0\n\t" \
    "v_mov_b32 v1, 0\n\t" \
    "v_mov_b32 v2, 0\n\t" \
    "v_mov_b32 v3, 0\n\t" \
    "v_mov_b32 v4, 0\n\t" \
    "v_mov_b32 v5, 0\n\t" \
    "v_mov_b32 v6, 0\n\t" \
    "v_mov_b32 v7, 0\n\t" \
    "v_mov_b32 v8, 0\n\t" \
    "v_mov_b32 v9, 0\n\t" \
    "v_mov_b32 v10, 0\n\t" \
    "v_mov_b32 v11, 0\n\t" \
    "v_mov_b32 v12, 0\n\t" \
    "v_mov_b32 v13, 0\n\t" \
    "v_mov_b32 v14, 0\n\t" \
    "v_mov_b32 v15, 0\n\t" \
    "v_mov_b32 v16, 0\n\t" \
    "v_mov_b32 v17, 0\n\t" \
    "v_mov_b32 v18, 0\n\t" \
    "v_mov_b32 v19, 0\n\t" \
    "v_mov_b32 v20, 0\n\t" \
    "v_mov_b32 v21, 0\n\t" \
    "v_mov_b32 v22, 0\n\t" \
    "v_mov_b32 v23, 0\n\t" \
    "v_mov_b32 v24, 0\n\t" \
    "v_mov_b32 v25, 0\n\t" \
    "v_mov_b32 v26, 0\n\t" \
    "v_mov_b32 v27, 0\n\t" \
    "v_mov_b32 v28, 0\n\t" \
    "v_mov_b32 v29, 0\n\t" \
    "v_mov_b32 v30, 0\n\t" \
    "v_mov_b32 v31, 0\n\t" \
    "v_mov_b32 v32, 0\n\t" \
    "v_mov_b32 v33, 0\n\t" \
    "v_mov_b32 v34, 0\n\t" \
    "v_mov_b32 v35, 0\n\t" \
    "v_mov_b32 v36, 0\n\t" \
    "v_mov_b32 v37, 0\n\t" \
    "v_mov_b32 v38, 0\n\t" \
    "v_mov_b32 v39, 0\n\t" \
    "v_mov_b32 v40, 0\n\t" \
    "v_mov_b32 v41, 0\n\t" \
    "v_mov_b32 v42, 0\n\t" \
    "v_mov_b32 v43, 0\n\t" \
    "v_mov_b32 v44, 0\n\t" \
    "v_mov_b32 v45, 0\n\t" \
    "v_mov_b32 v46, 0\n\t" \
    "v_mov_b32 v47, 0\n\t" \
    "v_mov_b32 v48, 0\n\t" \
    "v_mov_b32 v49, 0\n\t" \
    "v_mov_b32 v50, 0\n\t" \
    "v_mov_b32 v51, 0\n\t" \
    "v_mov_b32 v52, 0\n\t" \
    "v_mov_b32 v53, 0\n\t" \
    "v_mov_b32 v54, 0\n\t" \
    "v_mov_b32 v55, 0\n\t" \
    "v_mov_b32 v56, 0\n\t" \
    "v_mov_b32 v57, 0\n\t" \
    "v_mov_b32 v58, 0\n\t" \
    "v_mov_b32 v59, 0\n\t" \
    "v_mov_b32 v60, 0\n\t" \
    "v_mov_b32 v61, 0\n\t" \
    "v_mov_b32 v62, 0\n\t" \
    "v_mov_b32 v63, 0\n\t" \
    "s_mov_b32 s51, 0\n\t" \
    "s_mov_b32 s49, 16\n\t" \
    "s_mov_b32 s53, 4294836224\n\t" \
    "s_mov_b32 s54, 32768\n\t" \
    "s_barrier\n\t" \
    "ds_read_b64_tr_b16 v[96:97], v130 offset:0\n\t" \
    "ds_read_b64_tr_b16 v[98:99], v130 offset:1024\n\t" \
    "ds_read_b128 v[64:67], v128 offset:0\n\t" \
    "ds_read_b64_tr_b16 v[100:101], v131 offset:0\n\t" \
    "ds_read_b64_tr_b16 v[102:103], v131 offset:1024\n\t" \
    "ds_read_b64_tr_b16 v[104:105], v132 offset:0\n\t" \
    "ds_read_b64_tr_b16 v[106:107], v132 offset:1024\n\t" \
    "ds_read_b64_tr_b16 v[108:109], v133 offset:0\n\t" \
    "ds_read_b64_tr_b16 v[110:111], v133 offset:1024\n\t" \
    "ds_read_b128 v[68:71], v128 offset:2048\n\t" \
    "ds_read_b128 v[72:75], v128 offset:4096\n\t" \
    "ds_read_b128 v[76:79], v128 offset:6144\n\t" \
    "Lc_%=:\n\t" \
    "s_waitcnt lgkmcnt(9)\n\t" \
    "v_mfma_f32_16x16x32_bf16 v[0:3], v[96:99], v[64:67], v[0:3]\n\t" \
    "ds_read_b64_tr_b16 v[112:113], v130 offset:4096\n\t" \
    "s_waitcnt lgkmcnt(8)\n\t" \
    "v_mfma_f32_16x16x32_bf16 v[4:7], v[100:103], v[64:67], v[4:7]\n\t" \
    "ds_read_b64_tr_b16 v[114:115], v130 offset:5120\n\t" \
    "s_waitcnt lgkmcnt(7)\n\t" \
    "v_mfma_f32_16x16x32_bf16 v[8:11], v[104:107], v[64:67], v[8:11]\n\t" \
    "ds_read_b128 v[80:83], v128 offset:1024\n\t" \
    "s_waitcnt lgkmcnt(6)\n\t" \
    "v_mfma_f32_16x16x32_bf16 v[12:15], v[108:111], v[64:67], v[12:15]\n\t" \
    "ds_read_b64_tr_b16 v[116:117], v131 offset:4096\n\t" \
    "s_waitcnt lgkmcnt(6)\n\t" \
    "v_mfma_f32_16x16x32_bf16 v[16:19], v[96:99], v[68:71], v[16:19]\n\t" \
    "ds_read_b64_tr_b16 v[118:119], v131 offset:5120\n\t" \
    "v_mfma_f32_16x16x32_bf16 v[20:23], v[100:103], v[68:71], v[20:23]\n\t" \
    "ds_read_b64_tr_b16 v[120:121], v132 offset:4096\n\t" \
    "v_mfma_f32_16x16x32_bf16 v[24:27], v[104:107], v[68:71], v[24:27]\n\t" \
    "ds_read_b64_tr_b16 v[122:123], v132 offset:5120\n\t" \
    "v_mfma_f32_16x16x32_bf16 v[28:31], v[108:111], v[68:71], v[28:31]\n\t" \
    "ds_read_b64_tr_b16 v[124:125], v133 offset:4096\n\t" \
    "s_waitcnt lgkmcnt(9)\n\t" \
    "v_mfma_f32_16x16x32_bf16 v[32:35], v[96:99], v[72:75], v[32:35]\n\t" \
    "ds_read_b64_tr_b16 v[126:127], v133 offset:5120\n\t" \
    "v_mfma_f32_16x16x32_bf16 v[36:39], v[100:103], v[72:75], v[36:39]\n\t" \
    "ds_read_b128 v[84:87], v128 offset:3072\n\t" \
    "v_mfma_f32_16x16x32_bf16 v[40:43], v[104:107], v[72:75], v[40:43]\n\t" \
    "ds_read_b128 v[88:91], v128 offset:5120\n\t" \
    "v_mfma_f32_16x16x32_bf16 v[44:47], v[108:111], v[72:75], v[44:47]\n\t" \
    "ds_read_b128 v[92:95], v128 offset:7168\n\t" \
    "s_waitcnt lgkmcnt(12)\n\t" \
    "v_mfma_f32_16x16x32_bf16 v[48:51], v[96:99], v[76:79], v[48:51]\n\t" \
    "v_mfma_f32_16x16x32_bf16 v[52:55], v[100:103], v[76:79], v[52:55]\n\t" \
    "v_mfma_f32_16x16x32_bf16 v[56:59], v[104:107], v[76:79], v[56:59]\n\t" \
    "v_mfma_f32_16x16x32_bf16 v[60:63], v[108:111], v[76:79], v[60:63]\n\t" \
    "s_add_u32 s51, s51, 32768\n\t" \
    "s_cmp_eq_u32 s51, 163840\n\t" \
    "s_cselect_b32 s50, s53, s54\n\t" \
    "s_cselect_b32 s51, 0, s51\n\t" \
    "v_add_u32 v128, s50, v128\n\t" \
    "v_add_u32 v130, s50, v130\n\t" \
    "v_add_u32 v131, s50, v131\n\t" \
    "v_add_u32 v132, s50, v132\n\t" \
    "v_add_u32 v133, s50, v133\n\t" \
    "s_waitcnt lgkmcnt(0)\n\t" \
    "s_barrier\n\t" \
    "v_mfma_f32_16x16x32_bf16 v[0:3], v[112:115], v[80:83], v[0:3]\n\t" \
    "ds_read_b64_tr_b16 v[96:97], v130 offset:0\n\t" \
    "v_mfma_f32_16x16x32_bf16 v[4:7], v[116:119], v[80:83], v[4:7]\n\t" \
    "ds_read_b64_tr_b16 v[98:99], v130 offset:1024\n\t" \
    "v_mfma_f32_16x16x32_bf16 v[8:11], v[120:123], v[80:83], v[8:11]\n\t" \
    "ds_read_b128 v[64:67], v128 offset:0\n\t" \
    "v_mfma_f32_16x16x32_bf16 v[12:15], v[124:127], v[80:83], v[12:15]\n\t" \
    "ds_read_b64_tr_b16 v[100:101], v131 offset:0\n\t" \
    "v_mfma_f32_16x16x32_bf16 v[16:19], v[112:115], v[84:87], v[16:19]\n\t" \
    "ds_read_b64_tr_b16 v[102:103], v131 offset:1024\n\t" \
    "v_mfma_f32_16x16x32_bf16 v[20:23], v[116:119], v[84:87], v[20:23]\n\t" \
    "ds_read_b64_tr_b16 v[104:105], v132 offset:0\n\t" \
    "v_mfma_f32_16x16x32_bf16 v[24:27], v[120:123], v[84:87], v[24:27]\n\t" \
    "ds_read_b64_tr_b16 v[106:107], v132 offset:1024\n\t" \
    "v_mfma_f32_16x16x32_bf16 v[28:31], v[124:127], v[84:87], v[28:31]\n\t" \
    "ds_read_b64_tr_b16 v[108:109], v133 offset:0\n\t" \
    "v_mfma_f32_16x16x32_bf16 v[32:35], v[112:115], v[88:91], v[32:35]\n\t" \
    "ds_read_b64_tr_b16 v[110:111], v133 offset:1024\n\t" \
    "v_mfma_f32_16x16x32_bf16 v[36:39], v[116:119], v[88:91], v[36:39]\n\t" \
    "ds_read_b128 v[68:71], v128 offset:2048\n\t" \
    "v_mfma_f32_16x16x32_bf16 v[40:43], v[120:123], v[88:91], v[40:43]\n\t" \
    "ds_read_b128 v[72:75], v128 offset:4096\n\t" \
    "v_mfma_f32_16x16x32_bf16 v[44:47], v[124:127], v[88:91], v[44:47]\n\t" \
    "ds_read_b128 v[76:79], v128 offset:6144\n\t" \
    "v_mfma_f32_16x16x32_bf16 v[48:51], v[112:115], v[92:95], v[48:51]\n\t" \
    "s_sub_u32 s49, s49, 1\n\t" \
    "v_mfma_f32_16x16x32_bf16 v[52:55], v[116:119], v[92:95], v[52:55]\n\t" \
    "s_cmp_lg_u32 s49, 0\n\t" \
    "v_mfma_f32_16x16x32_bf16 v[56:59], v[120:123], v[92:95], v[56:59]\n\t" \
    "v_mfma_f32_16x16x32_bf16 v[60:63], v[124:127], v[92:95], v[60:63]\n\t" \
    "s_cbranch_scc1 Lc_%=\n\t" \
    "s_waitcnt lgkmcnt(0)\n\t" \
    "s_nop 9\n\t" \
    "s_branch Lend_%=\n\t" \
    "Lld_%=:\n\t" \
    "s_mov_b64 s[40:41], %[pp]\n\t" \
    "s_and_b32 s41, s41, 0xffff\n\t" \
    "s_mov_b32 s42, %[bp]\n\t" \
    "s_mov_b32 s43, 0x20000\n\t" \
    "s_mov_b64 s[44:45], %[pq]\n\t" \
    "s_and_b32 s45, s45, 0xffff\n\t" \
    "s_mov_b32 s46, %[bq]\n\t" \
    "s_mov_b32 s47, 0x20000\n\t" \
    "s_mov_b32 s54, 65536\n\t" \
    "s_mov_b32 s55, 131072\n\t" \
    "s_mov_b32 s56, 196608\n\t" \
    "s_mov_b32 s61, 65536\n\t" \
    "s_mov_b32 s62, 128\n\t" \
    "s_mov_b32 s63, 65664\n\t" \
    "v_mov_b32 v0, %[vp]\n\t" \
    "v_mov_b32 v1, %[vq]\n\t" \
    "s_mov_b32 s48, %[m0b]\n\t" \
    "s_mov_b32 s51, 0\n\t" \
    "s_add_u32 m0, s48, 0\n\t" \
    "s_nop 0\n\t" \
    "buffer_load_dwordx4 v0, s[40:43], 0 offen sc1 lds\n\t" \
    "s_add_u32 m0, s48, 4096\n\t" \
    "s_nop 0\n\t" \
    "buffer_load_dwordx4 v0, s[40:43], s54 offen sc1 lds\n\t" \
    "s_add_u32 m0, s48, 8192\n\t" \
    "s_nop 0\n\t" \
    "buffer_load_dwordx4 v0, s[40:43], s55 offen sc1 lds\n\t" \
    "s_add_u32 m0, s48, 12288\n\t" \
    "s_nop 0\n\t" \
    "buffer_load_dwordx4 v0, s[40:43], s56 offen sc1 lds\n\t" \
    "s_add_u32 m0, s48, 16384\n\t" \
    "s_nop 0\n\t" \
    "buffer_load_dwordx4 v1, s[44:47], 0 offen lds\n\t" \
    "s_add_u32 m0, s48, 20480\n\t" \
    "s_nop 0\n\t" \
    "buffer_load_dwordx4 v1, s[44:47], s61 offen lds\n\t" \
    "s_add_u32 m0, s48, 24576\n\t" \
    "s_nop 0\n\t" \
    "buffer_load_dwordx4 v1, s[44:47], s62 offen lds\n\t" \
    "s_add_u32 m0, s48, 28672\n\t" \
    "s_nop 0\n\t" \
    "buffer_load_dwordx4 v1, s[44:47], s63 offen lds\n\t" \
    "s_add_u32 s40, s40, 2048\n\t" \
    "s_addc_u32 s41, s41, 0\n\t" \
    "s_add_u32 s44, s44, 131072\n\t" \
    "s_addc_u32 s45, s45, 0\n\t" \
    "s_add_u32 s51, s51, 32768\n\t" \
    "s_cmp_eq_u32 s51, 163840\n\t" \
    "s_cselect_b32 s51, 0, s51\n\t" \
    "s_add_u32 s48, %[m0b], s51\n\t" \
    "s_add_u32 m0, s48, 0\n\t" \
    "s_nop 0\n\t" \
    "buffer_load_dwordx4 v0, s[40:43], 0 offen sc1 lds\n\t" \
    "s_add_u32 m0, s48, 4096\n\t" \
    "s_nop 0\n\t" \
    "buffer_load_dwordx4 v0, s[40:43], s54 offen sc1 lds\n\t" \
    "s_add_u32 m0, s48, 8192\n\t" \
    "s_nop 0\n\t" \
    "buffer_load_dwordx4 v0, s[40:43], s55 offen sc1 lds\n\t" \
    "s_add_u32 m0, s48, 12288\n\t" \
    "s_nop 0\n\t" \
    "buffer_load_dwordx4 v0, s[40:43], s56 offen sc1 lds\n\t" \
    "s_add_u32 m0, s48, 16384\n\t" \
    "s_nop 0\n\t" \
    "buffer_load_dwordx4 v1, s[44:47], 0 offen lds\n\t" \
    "s_add_u32 m0, s48, 20480\n\t" \
    "s_nop 0\n\t" \
    "buffer_load_dwordx4 v1, s[44:47], s61 offen lds\n\t" \
    "s_add_u32 m0, s48, 24576\n\t" \
    "s_nop 0\n\t" \
    "buffer_load_dwordx4 v1, s[44:47], s62 offen lds\n\t" \
    "s_add_u32 m0, s48, 28672\n\t" \
    "s_nop 0\n\t" \
    "buffer_load_dwordx4 v1, s[44:47], s63 offen lds\n\t" \
    "s_add_u32 s40, s40, 2048\n\t" \
    "s_addc_u32 s41, s41, 0\n\t" \
    "s_add_u32 s44, s44, 131072\n\t" \
    "s_addc_u32 s45, s45, 0\n\t" \
    "s_add_u32 s51, s51, 32768\n\t" \
    "s_cmp_eq_u32 s51, 163840\n\t" \
    "s_cselect_b32 s51, 0, s51\n\t" \
    "s_add_u32 s48, %[m0b], s51\n\t" \
    "s_waitcnt vmcnt(8)\n\t" \
    "s_barrier\n\t" \
    "s_add_u32 m0, s48, 0\n\t" \
    "s_nop 0\n\t" \
    "buffer_load_dwordx4 v0, s[40:43], 0 offen sc1 lds\n\t" \
    "s_add_u32 m0, s48, 4096\n\t" \
    "s_nop 0\n\t" \
    "buffer_load_dwordx4 v0, s[40:43], s54 offen sc1 lds\n\t" \
    "s_add_u32 m0, s48, 8192\n\t" \
    "s_nop 0\n\t" \
    "buffer_load_dwordx4 v0, s[40:43], s55 offen sc1 lds\n\t" \
    "s_add_u32 m0, s48, 12288\n\t" \
    "s_nop 0\n\t" \
    "buffer_load_dwordx4 v0, s[40:43], s56 offen sc1 lds\n\t" \
    "s_add_u32 m0, s48, 16384\n\t" \
    "s_nop 0\n\t" \
    "buffer_load_dwordx4 v1, s[44:47], 0 offen lds\n\t" \
    "s_add_u32 m0, s48, 20480\n\t" \
    "s_nop 0\n\t" \
    "buffer_load_dwordx4 v1, s[44:47], s61 offen lds\n\t" \
    "s_add_u32 m0, s48, 24576\n\t" \
    "s_nop 0\n\t" \
    "buffer_load_dwordx4 v1, s[44:47], s62 offen lds\n\t" \
    "s_add_u32 m0, s48, 28672\n\t" \
    "s_nop 0\n\t" \
    "buffer_load_dwordx4 v1, s[44:47], s63 offen lds\n\t" \
    "s_add_u32 s40, s40, 2048\n\t" \
    "s_addc_u32 s41, s41, 0\n\t" \
    "s_add_u32 s44, s44, 131072\n\t" \
    "s_addc_u32 s45, s45, 0\n\t" \
    "s_add_u32 s51, s51, 32768\n\t" \
    "s_cmp_eq_u32 s51, 163840\n\t" \
    "s_cselect_b32 s51, 0, s51\n\t" \
    "s_add_u32 s48, %[m0b], s51\n\t" \
    "s_add_u32 m0, s48, 0\n\t" \
    "s_nop 0\n\t" \
    "buffer_load_dwordx4 v0, s[40:43], 0 offen sc1 lds\n\t" \
    "s_add_u32 m0, s48, 4096\n\t" \
    "s_nop 0\n\t" \
    "buffer_load_dwordx4 v0, s[40:43], s54 offen sc1 lds\n\t" \
    "s_add_u32 m0, s48, 8192\n\t" \
    "s_nop 0\n\t" \
    "buffer_load_dwordx4 v0, s[40:43], s55 offen sc1 lds\n\t" \
    "s_add_u32 m0, s48, 12288\n\t" \
    "s_nop 0\n\t" \
    "buffer_load_dwordx4 v0, s[40:43], s56 offen sc1 lds\n\t" \
    "s_add_u32 m0, s48, 16384\n\t" \
    "s_nop 0\n\t" \
    "buffer_load_dwordx4 v1, s[44:47], 0 offen lds\n\t" \
    "s_add_u32 m0, s48, 20480\n\t" \
    "s_nop 0\n\t" \
    "buffer_load_dwordx4 v1, s[44:47], s61 offen lds\n\t" \
    "s_add_u32 m0, s48, 24576\n\t" \
    "s_nop 0\n\t" \
    "buffer_load_dwordx4 v1, s[44:47], s62 offen lds\n\t" \
    "s_add_u32 m0, s48, 28672\n\t" \
    "s_nop 0\n\t" \
    "buffer_load_dwordx4 v1, s[44:47], s63 offen lds\n\t" \
    "s_add_u32 s40, s40, 2048\n\t" \
    "s_addc_u32 s41, s41, 0\n\t" \
    "s_add_u32 s44, s44, 131072\n\t" \
    "s_addc_u32 s45, s45, 0\n\t" \
    "s_add_u32 s51, s51, 32768\n\t" \
    "s_cmp_eq_u32 s51, 163840\n\t" \
    "s_cselect_b32 s51, 0, s51\n\t" \
    "s_add_u32 s48, %[m0b], s51\n\t" \
    "s_add_u32 m0, s48, 0\n\t" \
    "s_nop 0\n\t" \
    "buffer_load_dwordx4 v0, s[40:43], 0 offen sc1 lds\n\t" \
    "s_add_u32 m0, s48, 4096\n\t" \
    "s_nop 0\n\t" \
    "buffer_load_dwordx4 v0, s[40:43], s54 offen sc1 lds\n\t" \
    "s_add_u32 m0, s48, 8192\n\t" \
    "s_nop 0\n\t" \
    "buffer_load_dwordx4 v0, s[40:43], s55 offen sc1 lds\n\t" \
    "s_add_u32 m0, s48, 12288\n\t" \
    "s_nop 0\n\t" \
    "buffer_load_dwordx4 v0, s[40:43], s56 offen sc1 lds\n\t" \
    "s_add_u32 m0, s48, 16384\n\t" \
    "s_nop 0\n\t" \
    "buffer_load_dwordx4 v1, s[44:47], 0 offen lds\n\t" \
    "s_add_u32 m0, s48, 20480\n\t" \
    "s_nop 0\n\t" \
    "buffer_load_dwordx4 v1, s[44:47], s61 offen lds\n\t" \
    "s_add_u32 m0, s48, 24576\n\t" \
    "s_nop 0\n\t" \
    "buffer_load_dwordx4 v1, s[44:47], s62 offen lds\n\t" \
    "s_add_u32 m0, s48, 28672\n\t" \
    "s_nop 0\n\t" \
    "buffer_load_dwordx4 v1, s[44:47], s63 offen lds\n\t" \
    "s_add_u32 s40, s40, 2048\n\t" \
    "s_addc_u32 s41, s41, 0\n\t" \
    "s_add_u32 s44, s44, 131072\n\t" \
    "s_addc_u32 s45, s45, 0\n\t" \
    "s_add_u32 s51, s51, 32768\n\t" \
    "s_cmp_eq_u32 s51, 163840\n\t" \
    "s_cselect_b32 s51, 0, s51\n\t" \
    "s_add_u32 s48, %[m0b], s51\n\t" \
    "s_mov_b32 s49, 5\n\t" \
    "Lldl_%=:\n\t" \
    "s_waitcnt vmcnt(24)\n\t" \
    "s_barrier\n\t" \
    "s_add_u32 m0, s48, 0\n\t" \
    "s_nop 0\n\t" \
    "buffer_load_dwordx4 v0, s[40:43], 0 offen sc1 lds\n\t" \
    "s_add_u32 m0, s48, 4096\n\t" \
    "s_nop 0\n\t" \
    "buffer_load_dwordx4 v0, s[40:43], s54 offen sc1 lds\n\t" \
    "s_add_u32 m0, s48, 8192\n\t" \
    "s_nop 0\n\t" \
    "buffer_load_dwordx4 v0, s[40:43], s55 offen sc1 lds\n\t" \
    "s_add_u32 m0, s48, 12288\n\t" \
    "s_nop 0\n\t" \
    "buffer_load_dwordx4 v0, s[40:43], s56 offen sc1 lds\n\t" \
    "s_add_u32 m0, s48, 16384\n\t" \
    "s_nop 0\n\t" \
    "buffer_load_dwordx4 v1, s[44:47], 0 offen lds\n\t" \
    "s_add_u32 m0, s48, 20480\n\t" \
    "s_nop 0\n\t" \
    "buffer_load_dwordx4 v1, s[44:47], s61 offen lds\n\t" \
    "s_add_u32 m0, s48, 24576\n\t" \
    "s_nop 0\n\t" \
    "buffer_load_dwordx4 v1, s[44:47], s62 offen lds\n\t" \
    "s_add_u32 m0, s48, 28672\n\t" \
    "s_nop 0\n\t" \
    "buffer_load_dwordx4 v1, s[44:47], s63 offen lds\n\t" \
    "s_add_u32 s40, s40, 2048\n\t" \
    "s_addc_u32 s41, s41, 0\n\t" \
    "s_add_u32 s44, s44, 131072\n\t" \
    "s_addc_u32 s45, s45, 0\n\t" \
    "s_add_u32 s51, s51, 32768\n\t" \
    "s_cmp_eq_u32 s51, 163840\n\t" \
    "s_cselect_b32 s51, 0, s51\n\t" \
    "s_add_u32 s48, %[m0b], s51\n\t" \
    "s_add_u32 s49, s49, 1\n\t" \
    "s_cmp_lt_u32 s49, 16\n\t" \
    "s_cbranch_scc1 Lldl_%=\n\t" \
    "s_waitcnt vmcnt(24)\n\t" \
    "s_barrier\n\t" \
    "s_waitcnt vmcnt(16)\n\t" \
    "s_barrier\n\t" \
    "s_waitcnt vmcnt(8)\n\t" \
    "s_barrier\n\t" \
    "s_waitcnt vmcnt(0)\n\t" \
    "s_barrier\n\t" \
    "s_waitcnt vmcnt(0)\n\t" \
    "s_barrier\n\t" \
    "s_branch Lend_%=\n\t" \
    "Lend_%=:\n\t"
#define OP_CLOB "v64", "v65", "v66", "v67", "v68", "v69", "v70", "v71", "v72", "v73", "v74", "v75", "v76", "v77", "v78", "v79", "v80", "v81", "v82", "v83", "v84", "v85", "v86", "v87", "v88", "v89", "v90", "v91", "v92", "v93", "v94", "v95", "v96", "v97", "v98", "v99", "v100", "v101", "v102", "v103", "v104", "v105", "v106", "v107", "v108", "v109", "v110", "v111", "v112", "v113", "v114", "v115", "v116", "v117", "v118", "v119", "v120", "v121", "v122", "v123", "v124", "v125", "v126", "v127", "v128", "v129", "v130", "v131", "v132", "v133", "s40", "s41", "s42", "s43", "s44", "s45", "s46", "s47", "s48", "s49", "s50", "s51", "s52", "s53", "s54", "s55", "s56", "s57", "s58", "s59", "s60", "s61", "s62", "s63", "vcc", "scc", "memory"
#define QKVS_ASM \
    "s_nop 4\n\t" \
    "v_mov_b32 v252, 0\n\t" \
    "s_mov_b64 s[82:83], %[pe]\n\t" \
    "s_nop 0\n\t" \
    "global_load_dwordx2 v[250:251], v252, s[82:83] sc1\n\t" \
    "s_mov_b64 s[40:41], %[pp]\n\t" \
    "s_and_b32 s41, s41, 0xffff\n\t" \
    "s_mov_b32 s42, %[bp]\n\t" \
    "s_mov_b32 s43, 0x20000\n\t" \
    "s_mov_b64 s[44:45], %[pq]\n\t" \
    "s_and_b32 s45, s45, 0xffff\n\t" \
    "s_mov_b32 s46, %[bq]\n\t" \
    "s_mov_b32 s47, 0x20000\n\t" \
    "s_mov_b32 s51, %[bp]\n\t" \
    "s_mov_b32 s52, %[bq]\n\t" \
    "s_mov_b32 s48, %[m0b]\n\t" \
    "s_mov_b32 s53, 131072\n\t" \
    "s_mov_b32 s54, 262144\n\t" \
    "s_mov_b32 s55, 393216\n\t" \
    "s_mov_b32 s56, 2048\n\t" \
    "s_mov_b32 s57, 4096\n\t" \
    "v_mov_b32 v180, %[vp]\n\t" \
    "v_mov_b32 v181, %[vq]\n\t" \
    "s_add_u32 m0, s48, 0\n\t" \
    "s_nop 0\n\t" \
    "buffer_load_dwordx4 v180, s[40:43], 0 offen lds\n\t" \
    "s_add_u32 m0, s48, 8192\n\t" \
    "s_nop 0\n\t" \
    "buffer_load_dwordx4 v180, s[40:43], s53 offen lds\n\t" \
    "s_add_u32 m0, s48, 16384\n\t" \
    "s_nop 0\n\t" \
    "buffer_load_dwordx4 v180, s[40:43], s54 offen lds\n\t" \
    "s_add_u32 m0, s48, 24576\n\t" \
    "s_nop 0\n\t" \
    "buffer_load_dwordx4 v180, s[40:43], s55 offen lds\n\t" \
    "s_add_u32 m0, s48, 32768\n\t" \
    "s_nop 0\n\t" \
    "buffer_load_dwordx4 v181, s[44:47], 0 offen lds\n\t" \
    "s_add_u32 m0, s48, 40960\n\t" \
    "s_nop 0\n\t" \
    "buffer_load_dwordx4 v181, s[44:47], s56 offen lds\n\t" \
    "s_add_u32 m0, s48, 49152\n\t" \
    "s_nop 0\n\t" \
    "buffer_load_dwordx4 v181, s[44:47], s57 offen lds\n\t" \
    "s_add_u32 s40, s40, 128\n\t" \
    "s_addc_u32 s41, s41, 0\n\t" \
    "s_add_u32 s44, s44, 393216\n\t" \
    "s_addc_u32 s45, s45, 0\n\t" \
    "s_add_u32 s48, s48, 57344\n\t" \
    "s_add_u32 m0, s48, 0\n\t" \
    "s_nop 0\n\t" \
    "buffer_load_dwordx4 v180, s[40:43], 0 offen lds\n\t" \
    "s_add_u32 m0, s48, 8192\n\t" \
    "s_nop 0\n\t" \
    "buffer_load_dwordx4 v180, s[40:43], s53 offen lds\n\t" \
    "s_add_u32 m0, s48, 16384\n\t" \
    "s_nop 0\n\t" \
    "buffer_load_dwordx4 v180, s[40:43], s54 offen lds\n\t" \
    "s_add_u32 m0, s48, 24576\n\t" \
    "s_nop 0\n\t" \
    "buffer_load_dwordx4 v180, s[40:43], s55 offen lds\n\t" \
    "s_add_u32 m0, s48, 32768\n\t" \
    "s_nop 0\n\t" \
    "buffer_load_dwordx4 v181, s[44:47], 0 offen lds\n\t" \
    "s_add_u32 m0, s48, 40960\n\t" \
    "s_nop 0\n\t" \
    "buffer_load_dwordx4 v181, s[44:47], s56 offen lds\n\t" \
    "s_add_u32 m0, s48, 49152\n\t" \
    "s_nop 0\n\t" \
    "buffer_load_dwordx4 v181, s[44:47], s57 offen lds\n\t" \
    "s_add_u32 s40, s40, 128\n\t" \
    "s_addc_u32 s41, s41, 0\n\t" \
    "s_add_u32 s44, s44, 393216\n\t" \
    "s_addc_u32 s45, s45, 0\n\t" \
    "s_sub_u32 s48, s48, 57344\n\t" \
    "v_mov_b32 v176, %[ap]\n\t" \
    "v_mov_b32 v178, %[aq]\n\t" \
    "v_xor_b32 v177, 64, v176\n\t" \
    "v_xor_b32 v179, 32, v178\n\t" \
    "v_mov_b32 v0, 0\n\t" \
    "v_mov_b32 v1, 0\n\t" \
    "v_mov_b32 v2, 0\n\t" \
    "v_mov_b32 v3, 0\n\t" \
    "v_mov_b32 v4, 0\n\t" \
    "v_mov_b32 v5, 0\n\t" \
    "v_mov_b32 v6, 0\n\t" \
    "v_mov_b32 v7, 0\n\t" \
    "v_mov_b32 v8, 0\n\t" \
    "v_mov_b32 v9, 0\n\t" \
    "v_mov_b32 v10, 0\n\t" \
    "v_mov_b32 v11, 0\n\t" \
    "v_mov_b32 v12, 0\n\t" \
    "v_mov_b32 v13, 0\n\t" \
    "v_mov_b32 v14, 0\n\t" \
    "v_mov_b32 v15, 0\n\t" \
    "v_mov_b32 v16, 0\n\t" \
    "v_mov_b32 v17, 0\n\t" \
    "v_mov_b32 v18, 0\n\t" \
    "v_mov_b32 v19, 0\n\t" \
    "v_mov_b32 v20, 0\n\t" \
    "v_mov_b32 v21, 0\n\t" \
    "v_mov_b32 v22, 0\n\t" \
    "v_mov_b32 v23, 0\n\t" \
    "v_mov_b32 v24, 0\n\t" \
    "v_mov_b32 v25, 0\n\t" \
    "v_mov_b32 v26, 0\n\t" \
    "v_mov_b32 v27, 0\n\t" \
    "v_mov_b32 v28, 0\n\t" \
    "v_mov_b32 v29, 0\n\t" \
    "v_mov_b32 v30, 0\n\t" \
    "v_mov_b32 v31, 0\n\t" \
    "v_mov_b32 v32, 0\n\t" \
    "v_mov_b32 v33, 0\n\t" \
    "v_mov_b32 v34, 0\n\t" \
    "v_mov_b32 v35, 0\n\t" \
    "v_mov_b32 v36, 0\n\t" \
    "v_mov_b32 v37, 0\n\t" \
    "v_mov_b32 v38, 0\n\t" \
    "v_mov_b32 v39, 0\n\t" \
    "v_mov_b32 v40, 0\n\t" \
    "v_mov_b32 v41, 0\n\t" \
    "v_mov_b32 v42, 0\n\t" \
    "v_mov_b32 v43, 0\n\t" \
    "v_mov_b32 v44, 0\n\t" \
    "v_mov_b32 v45, 0\n\t" \
    "v_mov_b32 v46, 0\n\t" \
    "v_mov_b32 v47, 0\n\t" \
    "v_mov_b32 v48, 0\n\t" \
    "v_mov_b32 v49, 0\n\t" \
    "v_mov_b32 v50, 0\n\t" \
    "v_mov_b32 v51, 0\n\t" \
    "v_mov_b32 v52, 0\n\t" \
    "v_mov_b32 v53, 0\n\t" \
    "v_mov_b32 v54, 0\n\t" \
    "v_mov_b32 v55, 0\n\t" \
    "v_mov_b32 v56, 0\n\t" \
    "v_mov_b32 v57, 0\n\t" \
    "v_mov_b32 v58, 0\n\t" \
    "v_mov_b32 v59, 0\n\t" \
    "v_mov_b32 v60, 0\n\t" \
    "v_mov_b32 v61, 0\n\t" \
    "v_mov_b32 v62, 0\n\t" \
    "v_mov_b32 v63, 0\n\t" \
    "v_mov_b32 v64, 0\n\t" \
    "v_mov_b32 v65, 0\n\t" \
    "v_mov_b32 v66, 0\n\t" \
    "v_mov_b32 v67, 0\n\t" \
    "v_mov_b32 v68, 0\n\t" \
    "v_mov_b32 v69, 0\n\t" \
    "v_mov_b32 v70, 0\n\t" \
    "v_mov_b32 v71, 0\n\t" \
    "v_mov_b32 v72, 0\n\t" \
    "v_mov_b32 v73, 0\n\t" \
    "v_mov_b32 v74, 0\n\t" \
    "v_mov_b32 v75, 0\n\t" \
    "v_mov_b32 v76, 0\n\t" \
    "v_mov_b32 v77, 0\n\t" \
    "v_mov_b32 v78, 0\n\t" \
    "v_mov_b32 v79, 0\n\t" \
    "v_mov_b32 v80, 0\n\t" \
    "v_mov_b32 v81, 0\n\t" \
    "v_mov_b32 v82, 0\n\t" \
    "v_mov_b32 v83, 0\n\t" \
    "v_mov_b32 v84, 0\n\t" \
    "v_mov_b32 v85, 0\n\t" \
    "v_mov_b32 v86, 0\n\t" \
    "v_mov_b32 v87, 0\n\t" \
    "v_mov_b32 v88, 0\n\t" \
    "v_mov_b32 v89, 0\n\t" \
    "v_mov_b32 v90, 0\n\t" \
    "v_mov_b32 v91, 0\n\t" \
    "v_mov_b32 v92, 0\n\t" \
    "v_mov_b32 v93, 0\n\t" \
    "v_mov_b32 v94, 0\n\t" \
    "v_mov_b32 v95, 0\n\t" \
    "s_mov_b32 s50, 57344\n\t" \
    "s_mov_b32 s49, 0\n\t" \
    "s_waitcnt vmcnt(7)\n\t" \
    "s_barrier\n\t" \
    "s_mov_b64 s[72:73], %[pf0]\n\t" \
    "s_mov_b64 s[74:75], %[pf1]\n\t" \
    "s_add_u32 s70, s72, 0\n\t" \
    "s_addc_u32 s71, s73, 0\n\t" \
    "global_load_dwordx4 v[252:255], %[vl16], s[70:71]\n\t" \
    "s_add_u32 s70, s72, 2097152\n\t" \
    "s_addc_u32 s71, s73, 0\n\t" \
    "global_load_dwordx4 v[252:255], %[vl16], s[70:71]\n\t" \
    "s_add_u32 s70, s74, 0\n\t" \
    "s_addc_u32 s71, s75, 0\n\t" \
    "global_load_dwordx4 v[252:255], %[vl16], s[70:71]\n\t" \
    "s_add_u32 s70, s74, 1048576\n\t" \
    "s_addc_u32 s71, s75, 0\n\t" \
    "global_load_dwordx4 v[252:255], %[vl16], s[70:71]\n\t" \
    "s_add_u32 s70, s74, 2097152\n\t" \
    "s_addc_u32 s71, s75, 0\n\t" \
    "global_load_dwordx4 v[252:255], %[vl16], s[70:71]\n\t" \
    "s_add_u32 s70, s72, 1024\n\t" \
    "s_addc_u32 s71, s73, 0\n\t" \
    "global_load_dwordx4 v[252:255], %[vl16], s[70:71]\n\t" \
    "s_add_u32 s70, s72, 2098176\n\t" \
    "s_addc_u32 s71, s73, 0\n\t" \
    "global_load_dwordx4 v[252:255], %[vl16], s[70:71]\n\t" \
    "s_add_u32 s70, s74, 3145728\n\t" \
    "s_addc_u32 s71, s75, 0\n\t" \
    "global_load_dwordx4 v[252:255], %[vl16], s[70:71]\n\t" \
    "s_add_u32 s70, s74, 4194304\n\t" \
    "s_addc_u32 s71, s75, 0\n\t" \
    "global_load_dwordx4 v[252:255], %[vl16], s[70:71]\n\t" \
    "s_add_u32 s70, s74, 5242880\n\t" \
    "s_addc_u32 s71, s75, 0\n\t" \
    "global_load_dwordx4 v[252:255], %[vl16], s[70:71]\n\t" \
    "ds_read_b64_tr_b16 v[128:129], v178 offset:0\n\t" \
    "ds_read_b64_tr_b16 v[130:131], v178 offset:512\n\t" \
    "ds_read_b128 v[96:99], v176 offset:0\n\t" \
    "ds_read_b64_tr_b16 v[132:133], v179 offset:0\n\t" \
    "ds_read_b64_tr_b16 v[134:135], v179 offset:512\n\t" \
    "ds_read_b64_tr_b16 v[136:137], v178 offset:8192\n\t" \
    "ds_read_b64_tr_b16 v[138:139], v178 offset:8704\n\t" \
    "ds_read_b64_tr_b16 v[140:141], v179 offset:8192\n\t" \
    "ds_read_b64_tr_b16 v[142:143], v179 offset:8704\n\t" \
    "ds_read_b64_tr_b16 v[144:145], v178 offset:16384\n\t" \
    "ds_read_b64_tr_b16 v[146:147], v178 offset:16896\n\t" \
    "ds_read_b64_tr_b16 v[148:149], v179 offset:16384\n\t" \
    "ds_read_b64_tr_b16 v[150:151], v179 offset:16896\n\t" \
    "ds_read_b128 v[100:103], v176 offset:2048\n\t" \
    "ds_read_b128 v[104:107], v176 offset:4096\n\t" \
    "ds_read_b128 v[108:111], v176 offset:6144\n\t" \
    "Lq_%=:\n\t" \
    "s_waitcnt lgkmcnt(13)\n\t" \
    "v_mfma_f32_16x16x32_bf16 v[0:3], v[128:131], v[96:99], v[0:3]\n\t" \
    "ds_read_b64_tr_b16 v[152:153], v178 offset:4096\n\t" \
    "s_waitcnt lgkmcnt(12)\n\t" \
    "v_mfma_f32_16x16x32_bf16 v[4:7], v[132:135], v[96:99], v[4:7]\n\t" \
    "ds_read_b64_tr_b16 v[154:155], v178 offset:4608\n\t" \
    "s_waitcnt lgkmcnt(11)\n\t" \
    "v_mfma_f32_16x16x32_bf16 v[8:11], v[136:139], v[96:99], v[8:11]\n\t" \
    "ds_read_b128 v[112:115], v177 offset:0\n\t" \
    "s_waitcnt lgkmcnt(10)\n\t" \
    "v_mfma_f32_16x16x32_bf16 v[12:15], v[140:143], v[96:99], v[12:15]\n\t" \
    "ds_read_b64_tr_b16 v[156:157], v179 offset:4096\n\t" \
    "s_waitcnt lgkmcnt(9)\n\t" \
    "v_mfma_f32_16x16x32_bf16 v[16:19], v[96:99], v[144:147], v[16:19]\n\t" \
    "ds_read_b64_tr_b16 v[158:159], v179 offset:4608\n\t" \
    "s_waitcnt lgkmcnt(8)\n\t" \
    "v_mfma_f32_16x16x32_bf16 v[20:23], v[96:99], v[148:151], v[20:23]\n\t" \
    "ds_read_b64_tr_b16 v[160:161], v178 offset:12288\n\t" \
    "s_waitcnt lgkmcnt(8)\n\t" \
    "v_mfma_f32_16x16x32_bf16 v[24:27], v[128:131], v[100:103], v[24:27]\n\t" \
    "ds_read_b64_tr_b16 v[162:163], v178 offset:12800\n\t" \
    "v_mfma_f32_16x16x32_bf16 v[28:31], v[132:135], v[100:103], v[28:31]\n\t" \
    "ds_read_b64_tr_b16 v[164:165], v179 offset:12288\n\t" \
    "v_mfma_f32_16x16x32_bf16 v[32:35], v[136:139], v[100:103], v[32:35]\n\t" \
    "ds_read_b64_tr_b16 v[166:167], v179 offset:12800\n\t" \
    "v_mfma_f32_16x16x32_bf16 v[36:39], v[140:143], v[100:103], v[36:39]\n\t" \
    "ds_read_b64_tr_b16 v[168:169], v178 offset:20480\n\t" \
    "v_mfma_f32_16x16x32_bf16 v[40:43], v[100:103], v[144:147], v[40:43]\n\t" \
    "ds_read_b64_tr_b16 v[170:171], v178 offset:20992\n\t" \
    "v_mfma_f32_16x16x32_bf16 v[44:47], v[100:103], v[148:151], v[44:47]\n\t" \
    "ds_read_b64_tr_b16 v[172:173], v179 offset:20480\n\t" \
    "s_waitcnt lgkmcnt(13)\n\t" \
    "v_mfma_f32_16x16x32_bf16 v[48:51], v[128:131], v[104:107], v[48:51]\n\t" \
    "ds_read_b64_tr_b16 v[174:175], v179 offset:20992\n\t" \
    "v_mfma_f32_16x16x32_bf16 v[52:55], v[132:135], v[104:107], v[52:55]\n\t" \
    "ds_read_b128 v[116:119], v177 offset:2048\n\t" \
    "v_mfma_f32_16x16x32_bf16 v[56:59], v[136:139], v[104:107], v[56:59]\n\t" \
    "ds_read_b128 v[120:123], v177 offset:4096\n\t" \
    "v_mfma_f32_16x16x32_bf16 v[60:63], v[140:143], v[104:107], v[60:63]\n\t" \
    "ds_read_b128 v[124:127], v177 offset:6144\n\t" \
    "v_mfma_f32_16x16x32_bf16 v[64:67], v[104:107], v[144:147], v[64:67]\n\t" \
    "v_add_u32 v176, s50, v176\n\t" \
    "v_mfma_f32_16x16x32_bf16 v[68:71], v[104:107], v[148:151], v[68:71]\n\t" \
    "v_add_u32 v177, s50, v177\n\t" \
    "s_waitcnt lgkmcnt(15)\n\t" \
    "v_mfma_f32_16x16x32_bf16 v[72:75], v[128:131], v[108:111], v[72:75]\n\t" \
    "v_add_u32 v178, s50, v178\n\t" \
    "v_mfma_f32_16x16x32_bf16 v[76:79], v[132:135], v[108:111], v[76:79]\n\t" \
    "v_add_u32 v179, s50, v179\n\t" \
    "v_mfma_f32_16x16x32_bf16 v[80:83], v[136:139], v[108:111], v[80:83]\n\t" \
    "v_mfma_f32_16x16x32_bf16 v[84:87], v[140:143], v[108:111], v[84:87]\n\t" \
    "v_mfma_f32_16x16x32_bf16 v[88:91], v[108:111], v[144:147], v[88:91]\n\t" \
    "v_mfma_f32_16x16x32_bf16 v[92:95], v[108:111], v[148:151], v[92:95]\n\t" \
    "s_waitcnt vmcnt(0) lgkmcnt(0)\n\t" \
    "s_barrier\n\t" \
    "s_cmp_lt_u32 s49, 14\n\t" \
    "s_cselect_b32 s42, s51, 0\n\t" \
    "s_cselect_b32 s46, s52, 0\n\t" \
    "v_mfma_f32_16x16x32_bf16 v[0:3], v[152:155], v[112:115], v[0:3]\n\t" \
    "ds_read_b64_tr_b16 v[128:129], v178 offset:0\n\t" \
    "v_mfma_f32_16x16x32_bf16 v[4:7], v[156:159], v[112:115], v[4:7]\n\t" \
    "ds_read_b64_tr_b16 v[130:131], v178 offset:512\n\t" \
    "s_add_u32 m0, s48, 0\n\t" \
    "s_nop 0\n\t" \
    "buffer_load_dwordx4 v180, s[40:43], 0 offen lds\n\t" \
    "v_mfma_f32_16x16x32_bf16 v[8:11], v[160:163], v[112:115], v[8:11]\n\t" \
    "ds_read_b128 v[96:99], v176 offset:0\n\t" \
    "v_mfma_f32_16x16x32_bf16 v[12:15], v[164:167], v[112:115], v[12:15]\n\t" \
    "ds_read_b64_tr_b16 v[132:133], v179 offset:0\n\t" \
    "v_mfma_f32_16x16x32_bf16 v[16:19], v[112:115], v[168:171], v[16:19]\n\t" \
    "ds_read_b64_tr_b16 v[134:135], v179 offset:512\n\t" \
    "s_add_u32 m0, s48, 8192\n\t" \
    "s_nop 0\n\t" \
    "buffer_load_dwordx4 v180, s[40:43], s53 offen lds\n\t" \
    "v_mfma_f32_16x16x32_bf16 v[20:23], v[112:115], v[172:175], v[20:23]\n\t" \
    "ds_read_b64_tr_b16 v[136:137], v178 offset:8192\n\t" \
    "v_mfma_f32_16x16x32_bf16 v[24:27], v[152:155], v[116:119], v[24:27]\n\t" \
    "ds_read_b64_tr_b16 v[138:139], v178 offset:8704\n\t" \
    "v_mfma_f32_16x16x32_bf16 v[28:31], v[156:159], v[116:119], v[28:31]\n\t" \
    "ds_read_b64_tr_b16 v[140:141], v179 offset:8192\n\t" \
    "s_add_u32 m0, s48, 16384\n\t" \
    "s_nop 0\n\t" \
    "buffer_load_dwordx4 v180, s[40:43], s54 offen lds\n\t" \
    "v_mfma_f32_16x16x32_bf16 v[32:35], v[160:163], v[116:119], v[32:35]\n\t" \
    "ds_read_b64_tr_b16 v[142:143], v179 offset:8704\n\t" \
    "v_mfma_f32_16x16x32_bf16 v[36:39], v[164:167], v[116:119], v[36:39]\n\t" \
    "ds_read_b64_tr_b16 v[144:145], v178 offset:16384\n\t" \
    "v_mfma_f32_16x16x32_bf16 v[40:43], v[116:119], v[168:171], v[40:43]\n\t" \
    "ds_read_b64_tr_b16 v[146:147], v178 offset:16896\n\t" \
    "s_add_u32 m0, s48, 24576\n\t" \
    "s_nop 0\n\t" \
    "buffer_load_dwordx4 v180, s[40:43], s55 offen lds\n\t" \
    "v_mfma_f32_16x16x32_bf16 v[44:47], v[116:119], v[172:175], v[44:47]\n\t" \
    "ds_read_b64_tr_b16 v[148:149], v179 offset:16384\n\t" \
    "v_mfma_f32_16x16x32_bf16 v[48:51], v[152:155], v[120:123], v[48:51]\n\t" \
    "ds_read_b64_tr_b16 v[150:151], v179 offset:16896\n\t" \
    "v_mfma_f32_16x16x32_bf16 v[52:55], v[156:159], v[120:123], v[52:55]\n\t" \
    "ds_read_b128 v[100:103], v176 offset:2048\n\t" \
    "s_add_u32 m0, s48, 32768\n\t" \
    "s_nop 0\n\t" \
    "buffer_load_dwordx4 v181, s[44:47], 0 offen lds\n\t" \
    "v_mfma_f32_16x16x32_bf16 v[56:59], v[160:163], v[120:123], v[56:59]\n\t" \
    "ds_read_b128 v[104:107], v176 offset:4096\n\t" \
    "v_mfma_f32_16x16x32_bf16 v[60:63], v[164:167], v[120:123], v[60:63]\n\t" \
    "ds_read_b128 v[108:111], v176 offset:6144\n\t" \
    "v_mfma_f32_16x16x32_bf16 v[64:67], v[120:123], v[168:171], v[64:67]\n\t" \
    "s_add_u32 m0, s48, 40960\n\t" \
    "s_nop 0\n\t" \
    "buffer_load_dwordx4 v181, s[44:47], s56 offen lds\n\t" \
    "v_mfma_f32_16x16x32_bf16 v[68:71], v[120:123], v[172:175], v[68:71]\n\t" \
    "v_mfma_f32_16x16x32_bf16 v[72:75], v[152:155], v[124:127], v[72:75]\n\t" \
    "v_mfma_f32_16x16x32_bf16 v[76:79], v[156:159], v[124:127], v[76:79]\n\t" \
    "s_add_u32 m0, s48, 49152\n\t" \
    "s_nop 0\n\t" \
    "buffer_load_dwordx4 v181, s[44:47], s57 offen lds\n\t" \
    "v_mfma_f32_16x16x32_bf16 v[80:83], v[160:163], v[124:127], v[80:83]\n\t" \
    "v_mfma_f32_16x16x32_bf16 v[84:87], v[164:167], v[124:127], v[84:87]\n\t" \
    "v_mfma_f32_16x16x32_bf16 v[88:91], v[124:127], v[168:171], v[88:91]\n\t" \
    "v_mfma_f32_16x16x32_bf16 v[92:95], v[124:127], v[172:175], v[92:95]\n\t" \
    "s_add_u32 s40, s40, 128\n\t" \
    "s_addc_u32 s41, s41, 0\n\t" \
    "s_add_u32 s44, s44, 393216\n\t" \
    "s_addc_u32 s45, s45, 0\n\t" \
    "s_add_u32 s48, s48, s50\n\t" \
    "s_sub_u32 s50, 0, s50\n\t" \
    "s_add_u32 s49, s49, 1\n\t" \
    "s_cmp_lt_u32 s49, 16\n\t" \
    "s_cbranch_scc1 Lq_%=\n\t" \
    "s_waitcnt vmcnt(0) lgkmcnt(0)\n\t" \
    "s_nop 7\n\t" \
    "v_readfirstlane_b32 s82, v250\n\t" \
    "v_readfirstlane_b32 s83, v251\n\t" \
    "s_nop 3\n\t"
#define QKVS_CLOB "v96", "v97", "v98", "v99", "v100", "v101", "v102", "v103", "v104", "v105", "v106", "v107", "v108", "v109", "v110", "v111", "v112", "v113", "v114", "v115", "v116", "v117", "v118", "v119", "v120", "v121", "v122", "v123", "v124", "v125", "v126", "v127", "v128", "v129", "v130", "v131", "v132", "v133", "v134", "v135", "v136", "v137", "v138", "v139", "v140", "v141", "v142", "v143", "v144", "v145", "v146", "v147", "v148", "v149", "v150", "v151", "v152", "v153", "v154", "v155", "v156", "v157", "v158", "v159", "v160", "v161", "v162", "v163", "v164", "v165", "v166", "v167", "v168", "v169", "v170", "v171", "v172", "v173", "v174", "v175", "v176", "v177", "v178", "v179", "v180", "v181", "v250", "v251", "v252", "v253", "v254", "v255", "s40", "s41", "s42", "s43", "s44", "s45", "s46", "s47", "s48", "s49", "s50", "s51", "s52", "s53", "s54", "s55", "s56", "s57", "s70", "s71", "s72", "s73", "s74", "s75", "s76", "s77", "s84", "s85", "s86", "s87", "vcc", "scc", "memory"
#define ATTN_ASM \
    "s_nop 4\n\t" \
    "s_mov_b64 s[40:41], %[pkv]\n\t" \
    "s_and_b32 s41, s41, 0xffff\n\t" \
    "s_mov_b32 s42, 131072\n\t" \
    "s_mov_b32 s43, 0x20000\n\t" \
    "s_mov_b64 s[44:45], %[pq]\n\t" \
    "s_mov_b64 s[46:47], %[po]\n\t" \
    "s_mov_b32 s48, %[m0b]\n\t" \
    "v_lshlrev_b32 v208, 4, %[lane]\n\t" \
    "v_add_u32 v204, %[lds0], v208\n\t" \
    "v_add_u32 v207, %[w1k], v208\n\t" \
    "s_mov_b32 s50, 0\n\t" \
    "s_mov_b32 s51, 0\n\t" \
    "s_mov_b32 s52, s48\n\t" \
    "s_add_u32 m0, s52, 0\n\t" \
    "s_add_u32 s53, s50, 0\n\t" \
    "buffer_load_dwordx4 v207, s[40:43], s53 offen sc1 lds\n\t" \
    "s_add_u32 m0, s52, 4096\n\t" \
    "s_add_u32 s53, s50, 4096\n\t" \
    "buffer_load_dwordx4 v207, s[40:43], s53 offen sc1 lds\n\t" \
    "s_add_u32 m0, s52, 8192\n\t" \
    "s_add_u32 s53, s50, 8192\n\t" \
    "buffer_load_dwordx4 v207, s[40:43], s53 offen sc1 lds\n\t" \
    "s_add_u32 m0, s52, 12288\n\t" \
    "s_add_u32 s53, s50, 12288\n\t" \
    "buffer_load_dwordx4 v207, s[40:43], s53 offen sc1 lds\n\t" \
    "global_load_dwordx4 v[128:131], v208, s[44:45] offset:0 sc1\n\t" \
    "global_load_dwordx4 v[132:135], v208, s[44:45] offset:1024 sc1\n\t" \
    "global_load_dwordx4 v[136:139], v208, s[44:45] offset:2048 sc1\n\t" \
    "global_load_dwordx4 v[140:143], v208, s[44:45] offset:3072 sc1\n\t" \
    "s_add_u32 s50, s50, 16384\n\t" \
    "s_add_u32 s51, s51, 32768\n\t" \
    "s_and_b32 s51, s51, 131071\n\t" \
    "s_cmp_lt_u32 s50, 131072\n\t" \
    "s_cselect_b32 s42, 131072, 0\n\t" \
    "s_add_u32 s52, s48, s51\n\t" \
    "s_add_u32 m0, s52, 0\n\t" \
    "s_add_u32 s53, s50, 0\n\t" \
    "buffer_load_dwordx4 v207, s[40:43], s53 offen sc1 lds\n\t" \
    "s_add_u32 m0, s52, 4096\n\t" \
    "s_add_u32 s53, s50, 4096\n\t" \
    "buffer_load_dwordx4 v207, s[40:43], s53 offen sc1 lds\n\t" \
    "s_add_u32 m0, s52, 8192\n\t" \
    "s_add_u32 s53, s50, 8192\n\t" \
    "buffer_load_dwordx4 v207, s[40:43], s53 offen sc1 lds\n\t" \
    "s_add_u32 m0, s52, 12288\n\t" \
    "s_add_u32 s53, s50, 12288\n\t" \
    "buffer_load_dwordx4 v207, s[40:43], s53 offen sc1 lds\n\t" \
    "s_add_u32 s50, s50, 16384\n\t" \
    "s_add_u32 s51, s51, 32768\n\t" \
    "s_and_b32 s51, s51, 131071\n\t" \
    "s_cmp_lt_u32 s50, 131072\n\t" \
    "s_cselect_b32 s42, 131072, 0\n\t" \
    "s_add_u32 s52, s48, s51\n\t" \
    "s_add_u32 m0, s52, 0\n\t" \
    "s_add_u32 s53, s50, 0\n\t" \
    "buffer_load_dwordx4 v207, s[40:43], s53 offen sc1 lds\n\t" \
    "s_mov_b64 s[60:61], %[pa]\n\t" \
    "s_and_b32 s61, s61, 0xffff\n\t" \
    "s_mov_b32 s62, %[na]\n\t" \
    "s_mov_b32 s63, 0x20000\n\t" \
    "s_mov_b64 s[72:73], %[pb]\n\t" \
    "s_and_b32 s73, s73, 0xffff\n\t" \
    "s_mov_b32 s74, %[nb]\n\t" \
    "s_mov_b32 s75, 0x20000\n\t" \
    "s_mov_b32 s58, %[ps]\n\t" \
    "s_mov_b32 s57, %[psb]\n\t" \
    "v_mov_b32 v211, %[vpf]\n\t" \
    "v_mov_b32 v216, %[vpb]\n\t" \
    "s_mov_b32 s54, 0x5d800000\n\t" \
    "s_mov_b32 s56, 0x42700000\n\t" \
    "s_mov_b32 s49, 8\n\t" \
    "s_mov_b32 s55, 32768\n\t" \
    "v_lshrrev_b32 v209, 2, %[lane]\n\t" \
    "v_lshrrev_b32 v210, 4, %[lane]\n\t" \
    "v_xor_b32 v209, v209, v210\n\t" \
    "v_and_b32 v209, 1, v209\n\t" \
    "v_and_b32 v210, 3, %[lane]\n\t" \
    "v_or_b32 v209, v209, v210\n\t" \
    "v_cmp_eq_u32 vcc, 0, v209\n\t" \
    "v_mov_b32 v210, 0x3f803f80\n\t" \
    "s_nop 0\n\t" \
    "v_cndmask_b32 v184, 0, v210, vcc\n\t" \
    "v_mov_b32 v185, v184\n\t" \
    "v_mov_b32 v186, v184\n\t" \
    "v_mov_b32 v187, v184\n\t" \
    "v_mov_b32 v48, 0\n\t" \
    "v_mov_b32 v64, 0\n\t" \
    "v_mov_b32 v49, 0\n\t" \
    "v_mov_b32 v65, 0\n\t" \
    "v_mov_b32 v50, 0\n\t" \
    "v_mov_b32 v66, 0\n\t" \
    "v_mov_b32 v51, 0\n\t" \
    "v_mov_b32 v67, 0\n\t" \
    "v_mov_b32 v52, 0\n\t" \
    "v_mov_b32 v68, 0\n\t" \
    "v_mov_b32 v53, 0\n\t" \
    "v_mov_b32 v69, 0\n\t" \
    "v_mov_b32 v54, 0\n\t" \
    "v_mov_b32 v70, 0\n\t" \
    "v_mov_b32 v55, 0\n\t" \
    "v_mov_b32 v71, 0\n\t" \
    "v_mov_b32 v56, 0\n\t" \
    "v_mov_b32 v72, 0\n\t" \
    "v_mov_b32 v57, 0\n\t" \
    "v_mov_b32 v73, 0\n\t" \
    "v_mov_b32 v58, 0\n\t" \
    "v_mov_b32 v74, 0\n\t" \
    "v_mov_b32 v59, 0\n\t" \
    "v_mov_b32 v75, 0\n\t" \
    "v_mov_b32 v60, 0\n\t" \
    "v_mov_b32 v76, 0\n\t" \
    "v_mov_b32 v61, 0\n\t" \
    "v_mov_b32 v77, 0\n\t" \
    "v_mov_b32 v62, 0\n\t" \
    "v_mov_b32 v78, 0\n\t" \
    "v_mov_b32 v63, 0\n\t" \
    "v_mov_b32 v79, 0\n\t" \
    "v_mov_b32 v168, 0\n\t" \
    "v_mov_b32 v172, 0\n\t" \
    "v_mov_b32 v176, 0\n\t" \
    "v_mov_b32 v180, 0\n\t" \
    "v_mov_b32 v169, 0\n\t" \
    "v_mov_b32 v173, 0\n\t" \
    "v_mov_b32 v177, 0\n\t" \
    "v_mov_b32 v181, 0\n\t" \
    "v_mov_b32 v170, 0\n\t" \
    "v_mov_b32 v174, 0\n\t" \
    "v_mov_b32 v178, 0\n\t" \
    "v_mov_b32 v182, 0\n\t" \
    "v_mov_b32 v171, 0\n\t" \
    "v_mov_b32 v175, 0\n\t" \
    "v_mov_b32 v179, 0\n\t" \
    "v_mov_b32 v183, 0\n\t" \
    "v_mov_b32 v120, 0\n\t" \
    "v_mov_b32 v160, 0\n\t" \
    "v_mov_b32 v152, 0\n\t" \
    "v_mov_b32 v121, 0\n\t" \
    "v_mov_b32 v161, 0\n\t" \
    "v_mov_b32 v153, 0\n\t" \
    "v_mov_b32 v122, 0\n\t" \
    "v_mov_b32 v162, 0\n\t" \
    "v_mov_b32 v154, 0\n\t" \
    "v_mov_b32 v123, 0\n\t" \
    "v_mov_b32 v163, 0\n\t" \
    "v_mov_b32 v155, 0\n\t" \
    "v_mov_b32 v124, 0\n\t" \
    "v_mov_b32 v164, 0\n\t" \
    "v_mov_b32 v156, 0\n\t" \
    "v_mov_b32 v125, 0\n\t" \
    "v_mov_b32 v165, 0\n\t" \
    "v_mov_b32 v157, 0\n\t" \
    "v_mov_b32 v126, 0\n\t" \
    "v_mov_b32 v166, 0\n\t" \
    "v_mov_b32 v158, 0\n\t" \
    "v_mov_b32 v127, 0\n\t" \
    "v_mov_b32 v167, 0\n\t" \
    "v_mov_b32 v159, 0\n\t" \
    "v_mov_b32 v205, v204\n\t" \
    "v_add_u32 v206, 32768, v204\n\t" \
    "s_waitcnt vmcnt(5)\n\t" \
    "s_barrier\n\t" \
    "ds_read_b128 v[144:147], v205 offset:0\n\t" \
    "ds_read_b128 v[148:151], v205 offset:1024\n\t" \
    "s_waitcnt lgkmcnt(1)\n\t" \
    "v_mfma_f32_32x32x16_bf16 v[188:203], v[144:147], v[136:139], 0\n\t" \
    "s_waitcnt lgkmcnt(0)\n\t" \
    "v_mfma_f32_32x32x16_bf16 v[188:203], v[148:151], v[140:143], v[188:203]\n\t" \
    "v_mfma_f32_32x32x16_bf16 v[16:31], v[144:147], v[128:131], 0\n\t" \
    "v_mfma_f32_32x32x16_bf16 v[16:31], v[148:151], v[132:135], v[16:31]\n\t" \
    "s_nop 9\n\t" \
    "v_max3_f32 v208, v188, v189, v190\n\t" \
    "v_max3_f32 v209, v191, v192, v193\n\t" \
    "v_max3_f32 v208, v208, v194, v195\n\t" \
    "v_max3_f32 v209, v209, v196, v197\n\t" \
    "v_max3_f32 v208, v208, v198, v199\n\t" \
    "v_max3_f32 v209, v209, v200, v201\n\t" \
    "v_max3_f32 v208, v208, v202, v203\n\t" \
    "v_max_f32 v208, v208, v209\n\t" \
    "v_mov_b32 v209, v208\n\t" \
    "s_nop 1\n\t" \
    "v_permlane32_swap_b32 v208, v209\n\t" \
    "v_max_f32 v208, v208, v209\n\t" \
    "v_add_f32 v208, s56, v208\n\t" \
    "v_sub_f32 v96, 0, v208\n\t" \
    "v_sub_f32 v97, 0, v208\n\t" \
    "v_sub_f32 v98, 0, v208\n\t" \
    "v_sub_f32 v99, 0, v208\n\t" \
    "v_sub_f32 v100, 0, v208\n\t" \
    "v_sub_f32 v101, 0, v208\n\t" \
    "v_sub_f32 v102, 0, v208\n\t" \
    "v_sub_f32 v103, 0, v208\n\t" \
    "v_sub_f32 v104, 0, v208\n\t" \
    "v_sub_f32 v105, 0, v208\n\t" \
    "v_sub_f32 v106, 0, v208\n\t" \
    "v_sub_f32 v107, 0, v208\n\t" \
    "v_sub_f32 v108, 0, v208\n\t" \
    "v_sub_f32 v109, 0, v208\n\t" \
    "v_sub_f32 v110, 0, v208\n\t" \
    "v_sub_f32 v111, 0, v208\n\t" \
    "v_max3_f32 v208, v16, v17, v18\n\t" \
    "v_max3_f32 v209, v19, v20, v21\n\t" \
    "v_max3_f32 v208, v208, v22, v23\n\t" \
    "v_max3_f32 v209, v209, v24, v25\n\t" \
    "v_max3_f32 v208, v208, v26, v27\n\t" \
    "v_max3_f32 v209, v209, v28, v29\n\t" \
    "v_max3_f32 v208, v208, v30, v31\n\t" \
    "v_max_f32 v208, v208, v209\n\t" \
    "v_mov_b32 v209, v208\n\t" \
    "s_nop 1\n\t" \
    "v_permlane32_swap_b32 v208, v209\n\t" \
    "v_max_f32 v208, v208, v209\n\t" \
    "v_add_f32 v208, s56, v208\n\t" \
    "v_sub_f32 v80, 0, v208\n\t" \
    "v_sub_f32 v16, v16, v208\n\t" \
    "v_sub_f32 v81, 0, v208\n\t" \
    "v_sub_f32 v17, v17, v208\n\t" \
    "v_sub_f32 v82, 0, v208\n\t" \
    "v_sub_f32 v18, v18, v208\n\t" \
    "v_sub_f32 v83, 0, v208\n\t" \
    "v_sub_f32 v19, v19, v208\n\t" \
    "v_sub_f32 v84, 0, v208\n\t" \
    "v_sub_f32 v20, v20, v208\n\t" \
    "v_sub_f32 v85, 0, v208\n\t" \
    "v_sub_f32 v21, v21, v208\n\t" \
    "v_sub_f32 v86, 0, v208\n\t" \
    "v_sub_f32 v22, v22, v208\n\t" \
    "v_sub_f32 v87, 0, v208\n\t" \
    "v_sub_f32 v23, v23, v208\n\t" \
    "v_sub_f32 v88, 0, v208\n\t" \
    "v_sub_f32 v24, v24, v208\n\t" \
    "v_sub_f32 v89, 0, v208\n\t" \
    "v_sub_f32 v25, v25, v208\n\t" \
    "v_sub_f32 v90, 0, v208\n\t" \
    "v_sub_f32 v26, v26, v208\n\t" \
    "v_sub_f32 v91, 0, v208\n\t" \
    "v_sub_f32 v27, v27, v208\n\t" \
    "v_sub_f32 v92, 0, v208\n\t" \
    "v_sub_f32 v28, v28, v208\n\t" \
    "v_sub_f32 v93, 0, v208\n\t" \
    "v_sub_f32 v29, v29, v208\n\t" \
    "v_sub_f32 v94, 0, v208\n\t" \
    "v_sub_f32 v30, v30, v208\n\t" \
    "v_sub_f32 v95, 0, v208\n\t" \
    "v_sub_f32 v31, v31, v208\n\t" \
    "s_cmp_lg_u32 %[hf], 0\n\t" \
    "s_cbranch_scc0 Lnp_%=\n\t" \
    "s_setprio 1\n\t" \
    "Lnp_%=:\n\t" \
    "s_cmp_lg_u32 %[hf], 0\n\t" \
    "s_cbranch_scc1 Lloop1_%=\n\t" \
    "Lloop0_%=:\n\t" \
    "v_mfma_f32_16x16x32_bf16 v[180:183], v[184:187], v[120:123], v[176:179]\n\t" \
    "v_exp_f32 v16, v16\n\t" \
    "v_exp_f32 v17, v17\n\t" \
    "v_mfma_f32_16x16x32_bf16 v[180:183], v[184:187], v[124:127], v[180:183]\n\t" \
    "v_exp_f32 v18, v18\n\t" \
    "v_exp_f32 v19, v19\n\t" \
    "v_cvt_pk_bf16_f32 v112, v16, v17\n\t" \
    "v_mfma_f32_32x32x16_bf16 v[32:47], v[144:147], v[136:139], v[96:111]\n\t" \
    "v_exp_f32 v20, v20\n\t" \
    "v_exp_f32 v21, v21\n\t" \
    "v_cvt_pk_bf16_f32 v113, v18, v19\n\t" \
    "v_exp_f32 v22, v22\n\t" \
    "v_mfma_f32_32x32x16_bf16 v[32:47], v[148:151], v[140:143], v[32:47]\n\t" \
    "v_cmp_ngt_f32 vcc, s54, v180\n\t" \
    "v_exp_f32 v23, v23\n\t" \
    "v_cvt_pk_bf16_f32 v114, v20, v21\n\t" \
    "v_exp_f32 v24, v24\n\t" \
    "v_exp_f32 v25, v25\n\t" \
    "v_cvt_pk_bf16_f32 v115, v22, v23\n\t" \
    "s_cbranch_vccnz LrareA0v0_%=\n\t" \
    "LretA0v0_%=:\n\t" \
    "ds_read_b128 v[152:155], v205 offset:4096\n\t" \
    "ds_read_b128 v[156:159], v205 offset:5120\n\t" \
    "v_mfma_f32_32x32x16_bf16 v[64:79], v[160:163], v[120:123], v[64:79]\n\t" \
    "v_exp_f32 v26, v26\n\t" \
    "v_exp_f32 v27, v27\n\t" \
    "v_cvt_pk_bf16_f32 v116, v24, v25\n\t" \
    "v_exp_f32 v28, v28\n\t" \
    "v_mfma_f32_32x32x16_bf16 v[64:79], v[164:167], v[124:127], v[64:79]\n\t" \
    "ds_read_b128 v[160:163], v205 offset:2048\n\t" \
    "ds_read_b128 v[164:167], v205 offset:3072\n\t" \
    "v_exp_f32 v29, v29\n\t" \
    "v_cvt_pk_bf16_f32 v117, v26, v27\n\t" \
    "v_exp_f32 v30, v30\n\t" \
    "v_exp_f32 v31, v31\n\t" \
    "v_cvt_pk_bf16_f32 v118, v28, v29\n\t" \
    "v_cvt_pk_bf16_f32 v119, v30, v31\n\t" \
    "v_mfma_f32_16x16x32_bf16 v[172:175], v[184:187], v[112:115], v[168:171]\n\t" \
    "v_exp_f32 v32, v32\n\t" \
    "v_exp_f32 v33, v33\n\t" \
    "v_mfma_f32_16x16x32_bf16 v[172:175], v[184:187], v[116:119], v[172:175]\n\t" \
    "v_exp_f32 v34, v34\n\t" \
    "v_exp_f32 v35, v35\n\t" \
    "v_cvt_pk_bf16_f32 v120, v32, v33\n\t" \
    "s_waitcnt lgkmcnt(3)\n\t" \
    "v_mfma_f32_32x32x16_bf16 v[16:31], v[152:155], v[128:131], v[80:95]\n\t" \
    "v_exp_f32 v36, v36\n\t" \
    "v_exp_f32 v37, v37\n\t" \
    "v_cvt_pk_bf16_f32 v121, v34, v35\n\t" \
    "v_exp_f32 v38, v38\n\t" \
    "s_waitcnt lgkmcnt(2)\n\t" \
    "v_mfma_f32_32x32x16_bf16 v[16:31], v[156:159], v[132:135], v[16:31]\n\t" \
    "v_cmp_ngt_f32 vcc, s54, v172\n\t" \
    "v_exp_f32 v39, v39\n\t" \
    "v_cvt_pk_bf16_f32 v122, v36, v37\n\t" \
    "v_exp_f32 v40, v40\n\t" \
    "v_exp_f32 v41, v41\n\t" \
    "v_cvt_pk_bf16_f32 v123, v38, v39\n\t" \
    "s_cbranch_vccnz LrareB0v0_%=\n\t" \
    "LretB0v0_%=:\n\t" \
    "s_add_u32 m0, s52, 4096\n\t" \
    "s_add_u32 s53, s50, 4096\n\t" \
    "buffer_load_dwordx4 v207, s[40:43], s53 offen sc1 lds\n\t" \
    "buffer_load_dwordx4 v[212:215], v211, s[60:63], 0 offen\n\t" \
    "v_add_u32 v211, s58, v211\n\t" \
    "s_waitcnt lgkmcnt(1)\n\t" \
    "v_mfma_f32_32x32x16_bf16 v[48:63], v[160:163], v[112:115], v[48:63]\n\t" \
    "v_exp_f32 v42, v42\n\t" \
    "v_exp_f32 v43, v43\n\t" \
    "v_cvt_pk_bf16_f32 v124, v40, v41\n\t" \
    "v_exp_f32 v44, v44\n\t" \
    "s_waitcnt lgkmcnt(0)\n\t" \
    "v_mfma_f32_32x32x16_bf16 v[48:63], v[164:167], v[116:119], v[48:63]\n\t" \
    "v_exp_f32 v45, v45\n\t" \
    "v_cvt_pk_bf16_f32 v125, v42, v43\n\t" \
    "v_exp_f32 v46, v46\n\t" \
    "v_exp_f32 v47, v47\n\t" \
    "v_cvt_pk_bf16_f32 v126, v44, v45\n\t" \
    "v_cvt_pk_bf16_f32 v127, v46, v47\n\t" \
    "v_mfma_f32_16x16x32_bf16 v[176:179], v[184:187], v[120:123], v[180:183]\n\t" \
    "v_exp_f32 v16, v16\n\t" \
    "v_exp_f32 v17, v17\n\t" \
    "v_mfma_f32_16x16x32_bf16 v[176:179], v[184:187], v[124:127], v[176:179]\n\t" \
    "v_exp_f32 v18, v18\n\t" \
    "v_exp_f32 v19, v19\n\t" \
    "v_cvt_pk_bf16_f32 v112, v16, v17\n\t" \
    "v_mfma_f32_32x32x16_bf16 v[32:47], v[152:155], v[136:139], v[96:111]\n\t" \
    "v_exp_f32 v20, v20\n\t" \
    "v_exp_f32 v21, v21\n\t" \
    "v_cvt_pk_bf16_f32 v113, v18, v19\n\t" \
    "v_exp_f32 v22, v22\n\t" \
    "v_mfma_f32_32x32x16_bf16 v[32:47], v[156:159], v[140:143], v[32:47]\n\t" \
    "v_cmp_ngt_f32 vcc, s54, v176\n\t" \
    "v_exp_f32 v23, v23\n\t" \
    "v_cvt_pk_bf16_f32 v114, v20, v21\n\t" \
    "v_exp_f32 v24, v24\n\t" \
    "v_exp_f32 v25, v25\n\t" \
    "v_cvt_pk_bf16_f32 v115, v22, v23\n\t" \
    "s_cbranch_vccnz LrareA1v0_%=\n\t" \
    "LretA1v0_%=:\n\t" \
    "ds_read_b128 v[144:147], v205 offset:8192\n\t" \
    "ds_read_b128 v[148:151], v205 offset:9216\n\t" \
    "v_mfma_f32_32x32x16_bf16 v[64:79], v[160:163], v[120:123], v[64:79]\n\t" \
    "v_exp_f32 v26, v26\n\t" \
    "v_exp_f32 v27, v27\n\t" \
    "v_cvt_pk_bf16_f32 v116, v24, v25\n\t" \
    "v_exp_f32 v28, v28\n\t" \
    "v_mfma_f32_32x32x16_bf16 v[64:79], v[164:167], v[124:127], v[64:79]\n\t" \
    "ds_read_b128 v[160:163], v205 offset:6144\n\t" \
    "ds_read_b128 v[164:167], v205 offset:7168\n\t" \
    "v_exp_f32 v29, v29\n\t" \
    "v_cvt_pk_bf16_f32 v117, v26, v27\n\t" \
    "v_exp_f32 v30, v30\n\t" \
    "v_exp_f32 v31, v31\n\t" \
    "v_cvt_pk_bf16_f32 v118, v28, v29\n\t" \
    "v_cvt_pk_bf16_f32 v119, v30, v31\n\t" \
    "v_mfma_f32_16x16x32_bf16 v[168:171], v[184:187], v[112:115], v[172:175]\n\t" \
    "v_exp_f32 v32, v32\n\t" \
    "v_exp_f32 v33, v33\n\t" \
    "v_mfma_f32_16x16x32_bf16 v[168:171], v[184:187], v[116:119], v[168:171]\n\t" \
    "v_exp_f32 v34, v34\n\t" \
    "v_exp_f32 v35, v35\n\t" \
    "v_cvt_pk_bf16_f32 v120, v32, v33\n\t" \
    "s_waitcnt lgkmcnt(3)\n\t" \
    "v_mfma_f32_32x32x16_bf16 v[16:31], v[144:147], v[128:131], v[80:95]\n\t" \
    "v_exp_f32 v36, v36\n\t" \
    "v_exp_f32 v37, v37\n\t" \
    "v_cvt_pk_bf16_f32 v121, v34, v35\n\t" \
    "v_exp_f32 v38, v38\n\t" \
    "s_waitcnt lgkmcnt(2)\n\t" \
    "v_mfma_f32_32x32x16_bf16 v[16:31], v[148:151], v[132:135], v[16:31]\n\t" \
    "v_cmp_ngt_f32 vcc, s54, v168\n\t" \
    "v_exp_f32 v39, v39\n\t" \
    "v_cvt_pk_bf16_f32 v122, v36, v37\n\t" \
    "v_exp_f32 v40, v40\n\t" \
    "v_exp_f32 v41, v41\n\t" \
    "v_cvt_pk_bf16_f32 v123, v38, v39\n\t" \
    "s_cbranch_vccnz LrareB1v0_%=\n\t" \
    "LretB1v0_%=:\n\t" \
    "s_add_u32 m0, s52, 8192\n\t" \
    "s_add_u32 s53, s50, 8192\n\t" \
    "buffer_load_dwordx4 v207, s[40:43], s53 offen sc1 lds\n\t" \
    "buffer_load_dwordx4 v[212:215], v216, s[72:75], 0 offen\n\t" \
    "v_add_u32 v216, s57, v216\n\t" \
    "s_waitcnt lgkmcnt(1)\n\t" \
    "v_mfma_f32_32x32x16_bf16 v[48:63], v[160:163], v[112:115], v[48:63]\n\t" \
    "v_exp_f32 v42, v42\n\t" \
    "v_exp_f32 v43, v43\n\t" \
    "v_cvt_pk_bf16_f32 v124, v40, v41\n\t" \
    "v_exp_f32 v44, v44\n\t" \
    "s_waitcnt lgkmcnt(0)\n\t" \
    "v_mfma_f32_32x32x16_bf16 v[48:63], v[164:167], v[116:119], v[48:63]\n\t" \
    "v_exp_f32 v45, v45\n\t" \
    "v_cvt_pk_bf16_f32 v125, v42, v43\n\t" \
    "v_exp_f32 v46, v46\n\t" \
    "v_exp_f32 v47, v47\n\t" \
    "v_cvt_pk_bf16_f32 v126, v44, v45\n\t" \
    "v_cvt_pk_bf16_f32 v127, v46, v47\n\t" \
    "v_mfma_f32_16x16x32_bf16 v[180:183], v[184:187], v[120:123], v[176:179]\n\t" \
    "v_exp_f32 v16, v16\n\t" \
    "v_exp_f32 v17, v17\n\t" \
    "v_mfma_f32_16x16x32_bf16 v[180:183], v[184:187], v[124:127], v[180:183]\n\t" \
    "v_exp_f32 v18, v18\n\t" \
    "v_exp_f32 v19, v19\n\t" \
    "v_cvt_pk_bf16_f32 v112, v16, v17\n\t" \
    "v_mfma_f32_32x32x16_bf16 v[32:47], v[144:147], v[136:139], v[96:111]\n\t" \
    "v_exp_f32 v20, v20\n\t" \
    "v_exp_f32 v21, v21\n\t" \
    "v_cvt_pk_bf16_f32 v113, v18, v19\n\t" \
    "v_exp_f32 v22, v22\n\t" \
    "v_mfma_f32_32x32x16_bf16 v[32:47], v[148:151], v[140:143], v[32:47]\n\t" \
    "v_cmp_ngt_f32 vcc, s54, v180\n\t" \
    "v_exp_f32 v23, v23\n\t" \
    "v_cvt_pk_bf16_f32 v114, v20, v21\n\t" \
    "v_exp_f32 v24, v24\n\t" \
    "v_exp_f32 v25, v25\n\t" \
    "v_cvt_pk_bf16_f32 v115, v22, v23\n\t" \
    "s_cbranch_vccnz LrareA2v0_%=\n\t" \
    "LretA2v0_%=:\n\t" \
    "ds_read_b128 v[152:155], v205 offset:12288\n\t" \
    "ds_read_b128 v[156:159], v205 offset:13312\n\t" \
    "v_mfma_f32_32x32x16_bf16 v[64:79], v[160:163], v[120:123], v[64:79]\n\t" \
    "v_exp_f32 v26, v26\n\t" \
    "v_exp_f32 v27, v27\n\t" \
    "v_cvt_pk_bf16_f32 v116, v24, v25\n\t" \
    "v_exp_f32 v28, v28\n\t" \
    "v_mfma_f32_32x32x16_bf16 v[64:79], v[164:167], v[124:127], v[64:79]\n\t" \
    "ds_read_b128 v[160:163], v205 offset:10240\n\t" \
    "ds_read_b128 v[164:167], v205 offset:11264\n\t" \
    "v_exp_f32 v29, v29\n\t" \
    "v_cvt_pk_bf16_f32 v117, v26, v27\n\t" \
    "v_exp_f32 v30, v30\n\t" \
    "v_exp_f32 v31, v31\n\t" \
    "v_cvt_pk_bf16_f32 v118, v28, v29\n\t" \
    "v_cvt_pk_bf16_f32 v119, v30, v31\n\t" \
    "v_mfma_f32_16x16x32_bf16 v[172:175], v[184:187], v[112:115], v[168:171]\n\t" \
    "v_exp_f32 v32, v32\n\t" \
    "v_exp_f32 v33, v33\n\t" \
    "v_mfma_f32_16x16x32_bf16 v[172:175], v[184:187], v[116:119], v[172:175]\n\t" \
    "v_exp_f32 v34, v34\n\t" \
    "v_exp_f32 v35, v35\n\t" \
    "v_cvt_pk_bf16_f32 v120, v32, v33\n\t" \
    "s_waitcnt lgkmcnt(3)\n\t" \
    "v_mfma_f32_32x32x16_bf16 v[16:31], v[152:155], v[128:131], v[80:95]\n\t" \
    "v_exp_f32 v36, v36\n\t" \
    "v_exp_f32 v37, v37\n\t" \
    "v_cvt_pk_bf16_f32 v121, v34, v35\n\t" \
    "v_exp_f32 v38, v38\n\t" \
    "s_waitcnt lgkmcnt(2)\n\t" \
    "v_mfma_f32_32x32x16_bf16 v[16:31], v[156:159], v[132:135], v[16:31]\n\t" \
    "v_cmp_ngt_f32 vcc, s54, v172\n\t" \
    "v_exp_f32 v39, v39\n\t" \
    "v_cvt_pk_bf16_f32 v122, v36, v37\n\t" \
    "v_exp_f32 v40, v40\n\t" \
    "v_exp_f32 v41, v41\n\t" \
    "v_cvt_pk_bf16_f32 v123, v38, v39\n\t" \
    "s_cbranch_vccnz LrareB2v0_%=\n\t" \
    "LretB2v0_%=:\n\t" \
    "s_add_u32 m0, s52, 12288\n\t" \
    "s_add_u32 s53, s50, 12288\n\t" \
    "buffer_load_dwordx4 v207, s[40:43], s53 offen sc1 lds\n\t" \
    "buffer_load_dwordx4 v[212:215], v211, s[60:63], 0 offen\n\t" \
    "v_add_u32 v211, s58, v211\n\t" \
    "s_waitcnt lgkmcnt(1)\n\t" \
    "v_mfma_f32_32x32x16_bf16 v[48:63], v[160:163], v[112:115], v[48:63]\n\t" \
    "v_exp_f32 v42, v42\n\t" \
    "v_exp_f32 v43, v43\n\t" \
    "v_cvt_pk_bf16_f32 v124, v40, v41\n\t" \
    "v_exp_f32 v44, v44\n\t" \
    "s_waitcnt lgkmcnt(0)\n\t" \
    "v_mfma_f32_32x32x16_bf16 v[48:63], v[164:167], v[116:119], v[48:63]\n\t" \
    "v_exp_f32 v45, v45\n\t" \
    "v_cvt_pk_bf16_f32 v125, v42, v43\n\t" \
    "v_exp_f32 v46, v46\n\t" \
    "v_exp_f32 v47, v47\n\t" \
    "v_cvt_pk_bf16_f32 v126, v44, v45\n\t" \
    "v_cvt_pk_bf16_f32 v127, v46, v47\n\t" \
    "v_mfma_f32_16x16x32_bf16 v[176:179], v[184:187], v[120:123], v[180:183]\n\t" \
    "v_exp_f32 v16, v16\n\t" \
    "v_exp_f32 v17, v17\n\t" \
    "v_mfma_f32_16x16x32_bf16 v[176:179], v[184:187], v[124:127], v[176:179]\n\t" \
    "v_exp_f32 v18, v18\n\t" \
    "v_exp_f32 v19, v19\n\t" \
    "v_cvt_pk_bf16_f32 v112, v16, v17\n\t" \
    "v_mfma_f32_32x32x16_bf16 v[32:47], v[152:155], v[136:139], v[96:111]\n\t" \
    "v_exp_f32 v20, v20\n\t" \
    "v_exp_f32 v21, v21\n\t" \
    "v_cvt_pk_bf16_f32 v113, v18, v19\n\t" \
    "v_exp_f32 v22, v22\n\t" \
    "v_mfma_f32_32x32x16_bf16 v[32:47], v[156:159], v[140:143], v[32:47]\n\t" \
    "v_cmp_ngt_f32 vcc, s54, v176\n\t" \
    "v_exp_f32 v23, v23\n\t" \
    "v_cvt_pk_bf16_f32 v114, v20, v21\n\t" \
    "v_exp_f32 v24, v24\n\t" \
    "v_exp_f32 v25, v25\n\t" \
    "v_cvt_pk_bf16_f32 v115, v22, v23\n\t" \
    "s_cbranch_vccnz LrareA3v0_%=\n\t" \
    "LretA3v0_%=:\n\t" \
    "s_waitcnt vmcnt(4)\n\t" \
    "s_barrier\n\t" \
    "ds_read_b128 v[144:147], v206 offset:0\n\t" \
    "ds_read_b128 v[148:151], v206 offset:1024\n\t" \
    "v_mfma_f32_32x32x16_bf16 v[64:79], v[160:163], v[120:123], v[64:79]\n\t" \
    "v_exp_f32 v26, v26\n\t" \
    "v_exp_f32 v27, v27\n\t" \
    "v_cvt_pk_bf16_f32 v116, v24, v25\n\t" \
    "v_exp_f32 v28, v28\n\t" \
    "v_mfma_f32_32x32x16_bf16 v[64:79], v[164:167], v[124:127], v[64:79]\n\t" \
    "ds_read_b128 v[160:163], v205 offset:14336\n\t" \
    "ds_read_b128 v[164:167], v205 offset:15360\n\t" \
    "v_exp_f32 v29, v29\n\t" \
    "v_cvt_pk_bf16_f32 v117, v26, v27\n\t" \
    "v_exp_f32 v30, v30\n\t" \
    "v_exp_f32 v31, v31\n\t" \
    "v_cvt_pk_bf16_f32 v118, v28, v29\n\t" \
    "v_cvt_pk_bf16_f32 v119, v30, v31\n\t" \
    "v_mfma_f32_16x16x32_bf16 v[168:171], v[184:187], v[112:115], v[172:175]\n\t" \
    "v_exp_f32 v32, v32\n\t" \
    "v_exp_f32 v33, v33\n\t" \
    "v_mfma_f32_16x16x32_bf16 v[168:171], v[184:187], v[116:119], v[168:171]\n\t" \
    "v_exp_f32 v34, v34\n\t" \
    "v_exp_f32 v35, v35\n\t" \
    "v_cvt_pk_bf16_f32 v120, v32, v33\n\t" \
    "s_waitcnt lgkmcnt(3)\n\t" \
    "v_mfma_f32_32x32x16_bf16 v[16:31], v[144:147], v[128:131], v[80:95]\n\t" \
    "v_exp_f32 v36, v36\n\t" \
    "v_exp_f32 v37, v37\n\t" \
    "v_cvt_pk_bf16_f32 v121, v34, v35\n\t" \
    "v_exp_f32 v38, v38\n\t" \
    "s_waitcnt lgkmcnt(2)\n\t" \
    "v_mfma_f32_32x32x16_bf16 v[16:31], v[148:151], v[132:135], v[16:31]\n\t" \
    "v_cmp_ngt_f32 vcc, s54, v168\n\t" \
    "v_exp_f32 v39, v39\n\t" \
    "v_cvt_pk_bf16_f32 v122, v36, v37\n\t" \
    "v_exp_f32 v40, v40\n\t" \
    "v_exp_f32 v41, v41\n\t" \
    "v_cvt_pk_bf16_f32 v123, v38, v39\n\t" \
    "s_cbranch_vccnz LrareB3v0_%=\n\t" \
    "LretB3v0_%=:\n\t" \
    "s_add_u32 s50, s50, 16384\n\t" \
    "s_add_u32 s51, s51, 32768\n\t" \
    "s_and_b32 s51, s51, 131071\n\t" \
    "s_cmp_lt_u32 s50, 131072\n\t" \
    "s_cselect_b32 s42, 131072, 0\n\t" \
    "s_add_u32 s52, s48, s51\n\t" \
    "s_add_u32 m0, s52, 0\n\t" \
    "s_add_u32 s53, s50, 0\n\t" \
    "buffer_load_dwordx4 v207, s[40:43], s53 offen sc1 lds\n\t" \
    "buffer_load_dwordx4 v[212:215], v216, s[72:75], 0 offen\n\t" \
    "v_add_u32 v216, s57, v216\n\t" \
    "s_waitcnt lgkmcnt(1)\n\t" \
    "v_mfma_f32_32x32x16_bf16 v[48:63], v[160:163], v[112:115], v[48:63]\n\t" \
    "v_exp_f32 v42, v42\n\t" \
    "v_exp_f32 v43, v43\n\t" \
    "v_cvt_pk_bf16_f32 v124, v40, v41\n\t" \
    "v_exp_f32 v44, v44\n\t" \
    "s_waitcnt lgkmcnt(0)\n\t" \
    "v_mfma_f32_32x32x16_bf16 v[48:63], v[164:167], v[116:119], v[48:63]\n\t" \
    "v_exp_f32 v45, v45\n\t" \
    "v_cvt_pk_bf16_f32 v125, v42, v43\n\t" \
    "v_exp_f32 v46, v46\n\t" \
    "v_exp_f32 v47, v47\n\t" \
    "v_cvt_pk_bf16_f32 v126, v44, v45\n\t" \
    "v_mov_b32 v205, v206\n\t" \
    "s_add_u32 s55, s55, 32768\n\t" \
    "s_and_b32 s55, s55, 131071\n\t" \
    "v_add_u32 v206, s55, v204\n\t" \
    "s_sub_u32 s49, s49, 1\n\t" \
    "s_cmp_lg_u32 s49, 0\n\t" \
    "v_cvt_pk_bf16_f32 v127, v46, v47\n\t" \
    "s_cbranch_scc1 Lloop0_%=\n\t" \
    "s_branch Ldrain_%=\n\t" \
    "Lloop1_%=:\n\t" \
    "v_mfma_f32_16x16x32_bf16 v[180:183], v[184:187], v[120:123], v[176:179]\n\t" \
    "v_exp_f32 v16, v16\n\t" \
    "v_exp_f32 v17, v17\n\t" \
    "v_mfma_f32_16x16x32_bf16 v[180:183], v[184:187], v[124:127], v[180:183]\n\t" \
    "v_exp_f32 v18, v18\n\t" \
    "v_exp_f32 v19, v19\n\t" \
    "v_cvt_pk_bf16_f32 v112, v16, v17\n\t" \
    "v_mfma_f32_32x32x16_bf16 v[32:47], v[144:147], v[136:139], v[96:111]\n\t" \
    "v_exp_f32 v20, v20\n\t" \
    "v_exp_f32 v21, v21\n\t" \
    "v_cvt_pk_bf16_f32 v113, v18, v19\n\t" \
    "v_exp_f32 v22, v22\n\t" \
    "v_mfma_f32_32x32x16_bf16 v[32:47], v[148:151], v[140:143], v[32:47]\n\t" \
    "v_cmp_ngt_f32 vcc, s54, v180\n\t" \
    "v_exp_f32 v23, v23\n\t" \
    "v_cvt_pk_bf16_f32 v114, v20, v21\n\t" \
    "v_exp_f32 v24, v24\n\t" \
    "v_exp_f32 v25, v25\n\t" \
    "v_cvt_pk_bf16_f32 v115, v22, v23\n\t" \
    "s_cbranch_vccnz LrareA0v1_%=\n\t" \
    "LretA0v1_%=:\n\t" \
    "ds_read_b128 v[152:155], v205 offset:4096\n\t" \
    "ds_read_b128 v[156:159], v205 offset:5120\n\t" \
    "v_mfma_f32_32x32x16_bf16 v[64:79], v[160:163], v[120:123], v[64:79]\n\t" \
    "v_exp_f32 v26, v26\n\t" \
    "v_exp_f32 v27, v27\n\t" \
    "v_cvt_pk_bf16_f32 v116, v24, v25\n\t" \
    "v_exp_f32 v28, v28\n\t" \
    "v_mfma_f32_32x32x16_bf16 v[64:79], v[164:167], v[124:127], v[64:79]\n\t" \
    "ds_read_b128 v[160:163], v205 offset:2048\n\t" \
    "ds_read_b128 v[164:167], v205 offset:3072\n\t" \
    "v_exp_f32 v29, v29\n\t" \
    "v_cvt_pk_bf16_f32 v117, v26, v27\n\t" \
    "v_exp_f32 v30, v30\n\t" \
    "v_exp_f32 v31, v31\n\t" \
    "v_cvt_pk_bf16_f32 v118, v28, v29\n\t" \
    "v_cvt_pk_bf16_f32 v119, v30, v31\n\t" \
    "v_mfma_f32_16x16x32_bf16 v[172:175], v[184:187], v[112:115], v[168:171]\n\t" \
    "v_exp_f32 v32, v32\n\t" \
    "v_exp_f32 v33, v33\n\t" \
    "v_mfma_f32_16x16x32_bf16 v[172:175], v[184:187], v[116:119], v[172:175]\n\t" \
    "v_exp_f32 v34, v34\n\t" \
    "v_exp_f32 v35, v35\n\t" \
    "v_cvt_pk_bf16_f32 v120, v32, v33\n\t" \
    "s_waitcnt lgkmcnt(3)\n\t" \
    "v_mfma_f32_32x32x16_bf16 v[16:31], v[152:155], v[128:131], v[80:95]\n\t" \
    "v_exp_f32 v36, v36\n\t" \
    "v_exp_f32 v37, v37\n\t" \
    "v_cvt_pk_bf16_f32 v121, v34, v35\n\t" \
    "v_exp_f32 v38, v38\n\t" \
    "s_waitcnt lgkmcnt(2)\n\t" \
    "v_mfma_f32_32x32x16_bf16 v[16:31], v[156:159], v[132:135], v[16:31]\n\t" \
    "v_cmp_ngt_f32 vcc, s54, v172\n\t" \
    "v_exp_f32 v39, v39\n\t" \
    "v_cvt_pk_bf16_f32 v122, v36, v37\n\t" \
    "v_exp_f32 v40, v40\n\t" \
    "v_exp_f32 v41, v41\n\t" \
    "v_cvt_pk_bf16_f32 v123, v38, v39\n\t" \
    "s_cbranch_vccnz LrareB0v1_%=\n\t" \
    "LretB0v1_%=:\n\t" \
    "s_add_u32 m0, s52, 4096\n\t" \
    "s_add_u32 s53, s50, 4096\n\t" \
    "buffer_load_dwordx4 v207, s[40:43], s53 offen sc1 lds\n\t" \
    "buffer_load_dwordx4 v[212:215], v211, s[60:63], 0 offen\n\t" \
    "v_add_u32 v211, s58, v211\n\t" \
    "s_waitcnt lgkmcnt(1)\n\t" \
    "v_mfma_f32_32x32x16_bf16 v[48:63], v[160:163], v[112:115], v[48:63]\n\t" \
    "v_exp_f32 v42, v42\n\t" \
    "v_exp_f32 v43, v43\n\t" \
    "v_cvt_pk_bf16_f32 v124, v40, v41\n\t" \
    "v_exp_f32 v44, v44\n\t" \
    "s_waitcnt lgkmcnt(0)\n\t" \
    "v_mfma_f32_32x32x16_bf16 v[48:63], v[164:167], v[116:119], v[48:63]\n\t" \
    "v_exp_f32 v45, v45\n\t" \
    "v_cvt_pk_bf16_f32 v125, v42, v43\n\t" \
    "v_exp_f32 v46, v46\n\t" \
    "v_exp_f32 v47, v47\n\t" \
    "v_cvt_pk_bf16_f32 v126, v44, v45\n\t" \
    "v_cvt_pk_bf16_f32 v127, v46, v47\n\t" \
    "v_mfma_f32_16x16x32_bf16 v[176:179], v[184:187], v[120:123], v[180:183]\n\t" \
    "v_exp_f32 v16, v16\n\t" \
    "v_exp_f32 v17, v17\n\t" \
    "v_mfma_f32_16x16x32_bf16 v[176:179], v[184:187], v[124:127], v[176:179]\n\t" \
    "v_exp_f32 v18, v18\n\t" \
    "v_exp_f32 v19, v19\n\t" \
    "v_cvt_pk_bf16_f32 v112, v16, v17\n\t" \
    "v_mfma_f32_32x32x16_bf16 v[32:47], v[152:155], v[136:139], v[96:111]\n\t" \
    "v_exp_f32 v20, v20\n\t" \
    "v_exp_f32 v21, v21\n\t" \
    "v_cvt_pk_bf16_f32 v113, v18, v19\n\t" \
    "v_exp_f32 v22, v22\n\t" \
    "v_mfma_f32_32x32x16_bf16 v[32:47], v[156:159], v[140:143], v[32:47]\n\t" \
    "v_cmp_ngt_f32 vcc, s54, v176\n\t" \
    "v_exp_f32 v23, v23\n\t" \
    "v_cvt_pk_bf16_f32 v114, v20, v21\n\t" \
    "v_exp_f32 v24, v24\n\t" \
    "v_exp_f32 v25, v25\n\t" \
    "v_cvt_pk_bf16_f32 v115, v22, v23\n\t" \
    "s_cbranch_vccnz LrareA1v1_%=\n\t" \
    "LretA1v1_%=:\n\t" \
    "ds_read_b128 v[144:147], v205 offset:8192\n\t" \
    "ds_read_b128 v[148:151], v205 offset:9216\n\t" \
    "v_mfma_f32_32x32x16_bf16 v[64:79], v[160:163], v[120:123], v[64:79]\n\t" \
    "v_exp_f32 v26, v26\n\t" \
    "v_exp_f32 v27, v27\n\t" \
    "v_cvt_pk_bf16_f32 v116, v24, v25\n\t" \
    "v_exp_f32 v28, v28\n\t" \
    "v_mfma_f32_32x32x16_bf16 v[64:79], v[164:167], v[124:127], v[64:79]\n\t" \
    "ds_read_b128 v[160:163], v205 offset:6144\n\t" \
    "ds_read_b128 v[164:167], v205 offset:7168\n\t" \
    "v_exp_f32 v29, v29\n\t" \
    "v_cvt_pk_bf16_f32 v117, v26, v27\n\t" \
    "v_exp_f32 v30, v30\n\t" \
    "v_exp_f32 v31, v31\n\t" \
    "v_cvt_pk_bf16_f32 v118, v28, v29\n\t" \
    "v_cvt_pk_bf16_f32 v119, v30, v31\n\t" \
    "v_mfma_f32_16x16x32_bf16 v[168:171], v[184:187], v[112:115], v[172:175]\n\t" \
    "v_exp_f32 v32, v32\n\t" \
    "v_exp_f32 v33, v33\n\t" \
    "v_mfma_f32_16x16x32_bf16 v[168:171], v[184:187], v[116:119], v[168:171]\n\t" \
    "v_exp_f32 v34, v34\n\t" \
    "v_exp_f32 v35, v35\n\t" \
    "v_cvt_pk_bf16_f32 v120, v32, v33\n\t" \
    "s_waitcnt lgkmcnt(3)\n\t" \
    "v_mfma_f32_32x32x16_bf16 v[16:31], v[144:147], v[128:131], v[80:95]\n\t" \
    "v_exp_f32 v36, v36\n\t" \
    "v_exp_f32 v37, v37\n\t" \
    "v_cvt_pk_bf16_f32 v121, v34, v35\n\t" \
    "v_exp_f32 v38, v38\n\t" \
    "s_waitcnt lgkmcnt(2)\n\t" \
    "v_mfma_f32_32x32x16_bf16 v[16:31], v[148:151], v[132:135], v[16:31]\n\t" \
    "v_cmp_ngt_f32 vcc, s54, v168\n\t" \
    "v_exp_f32 v39, v39\n\t" \
    "v_cvt_pk_bf16_f32 v122, v36, v37\n\t" \
    "v_exp_f32 v40, v40\n\t" \
    "v_exp_f32 v41, v41\n\t" \
    "v_cvt_pk_bf16_f32 v123, v38, v39\n\t" \
    "s_cbranch_vccnz LrareB1v1_%=\n\t" \
    "LretB1v1_%=:\n\t" \
    "s_add_u32 m0, s52, 8192\n\t" \
    "s_add_u32 s53, s50, 8192\n\t" \
    "buffer_load_dwordx4 v207, s[40:43], s53 offen sc1 lds\n\t" \
    "buffer_load_dwordx4 v[212:215], v216, s[72:75], 0 offen\n\t" \
    "v_add_u32 v216, s57, v216\n\t" \
    "s_waitcnt lgkmcnt(1)\n\t" \
    "v_mfma_f32_32x32x16_bf16 v[48:63], v[160:163], v[112:115], v[48:63]\n\t" \
    "v_exp_f32 v42, v42\n\t" \
    "v_exp_f32 v43, v43\n\t" \
    "v_cvt_pk_bf16_f32 v124, v40, v41\n\t" \
    "v_exp_f32 v44, v44\n\t" \
    "s_waitcnt lgkmcnt(0)\n\t" \
    "v_mfma_f32_32x32x16_bf16 v[48:63], v[164:167], v[116:119], v[48:63]\n\t" \
    "v_exp_f32 v45, v45\n\t" \
    "v_cvt_pk_bf16_f32 v125, v42, v43\n\t" \
    "v_exp_f32 v46, v46\n\t" \
    "v_exp_f32 v47, v47\n\t" \
    "v_cvt_pk_bf16_f32 v126, v44, v45\n\t" \
    "v_cvt_pk_bf16_f32 v127, v46, v47\n\t" \
    "v_mfma_f32_16x16x32_bf16 v[180:183], v[184:187], v[120:123], v[176:179]\n\t" \
    "v_exp_f32 v16, v16\n\t" \
    "v_exp_f32 v17, v17\n\t" \
    "v_mfma_f32_16x16x32_bf16 v[180:183], v[184:187], v[124:127], v[180:183]\n\t" \
    "v_exp_f32 v18, v18\n\t" \
    "v_exp_f32 v19, v19\n\t" \
    "v_cvt_pk_bf16_f32 v112, v16, v17\n\t" \
    "v_mfma_f32_32x32x16_bf16 v[32:47], v[144:147], v[136:139], v[96:111]\n\t" \
    "v_exp_f32 v20, v20\n\t" \
    "v_exp_f32 v21, v21\n\t" \
    "v_cvt_pk_bf16_f32 v113, v18, v19\n\t" \
    "v_exp_f32 v22, v22\n\t" \
    "v_mfma_f32_32x32x16_bf16 v[32:47], v[148:151], v[140:143], v[32:47]\n\t" \
    "v_cmp_ngt_f32 vcc, s54, v180\n\t" \
    "v_exp_f32 v23, v23\n\t" \
    "v_cvt_pk_bf16_f32 v114, v20, v21\n\t" \
    "v_exp_f32 v24, v24\n\t" \
    "v_exp_f32 v25, v25\n\t" \
    "v_cvt_pk_bf16_f32 v115, v22, v23\n\t" \
    "s_cbranch_vccnz LrareA2v1_%=\n\t" \
    "LretA2v1_%=:\n\t" \
    "ds_read_b128 v[152:155], v205 offset:12288\n\t" \
    "ds_read_b128 v[156:159], v205 offset:13312\n\t" \
    "v_mfma_f32_32x32x16_bf16 v[64:79], v[160:163], v[120:123], v[64:79]\n\t" \
    "v_exp_f32 v26, v26\n\t" \
    "v_exp_f32 v27, v27\n\t" \
    "v_cvt_pk_bf16_f32 v116, v24, v25\n\t" \
    "v_exp_f32 v28, v28\n\t" \
    "v_mfma_f32_32x32x16_bf16 v[64:79], v[164:167], v[124:127], v[64:79]\n\t" \
    "ds_read_b128 v[160:163], v205 offset:10240\n\t" \
    "ds_read_b128 v[164:167], v205 offset:11264\n\t" \
    "v_exp_f32 v29, v29\n\t" \
    "v_cvt_pk_bf16_f32 v117, v26, v27\n\t" \
    "v_exp_f32 v30, v30\n\t" \
    "v_exp_f32 v31, v31\n\t" \
    "v_cvt_pk_bf16_f32 v118, v28, v29\n\t" \
    "v_cvt_pk_bf16_f32 v119, v30, v31\n\t" \
    "v_mfma_f32_16x16x32_bf16 v[172:175], v[184:187], v[112:115], v[168:171]\n\t" \
    "v_exp_f32 v32, v32\n\t" \
    "v_exp_f32 v33, v33\n\t" \
    "v_mfma_f32_16x16x32_bf16 v[172:175], v[184:187], v[116:119], v[172:175]\n\t" \
    "v_exp_f32 v34, v34\n\t" \
    "v_exp_f32 v35, v35\n\t" \
    "v_cvt_pk_bf16_f32 v120, v32, v33\n\t" \
    "s_waitcnt lgkmcnt(3)\n\t" \
    "v_mfma_f32_32x32x16_bf16 v[16:31], v[152:155], v[128:131], v[80:95]\n\t" \
    "v_exp_f32 v36, v36\n\t" \
    "v_exp_f32 v37, v37\n\t" \
    "v_cvt_pk_bf16_f32 v121, v34, v35\n\t" \
    "v_exp_f32 v38, v38\n\t" \
    "s_waitcnt lgkmcnt(2)\n\t" \
    "v_mfma_f32_32x32x16_bf16 v[16:31], v[156:159], v[132:135], v[16:31]\n\t" \
    "v_cmp_ngt_f32 vcc, s54, v172\n\t" \
    "v_exp_f32 v39, v39\n\t" \
    "v_cvt_pk_bf16_f32 v122, v36, v37\n\t" \
    "v_exp_f32 v40, v40\n\t" \
    "v_exp_f32 v41, v41\n\t" \
    "v_cvt_pk_bf16_f32 v123, v38, v39\n\t" \
    "s_cbranch_vccnz LrareB2v1_%=\n\t" \
    "LretB2v1_%=:\n\t" \
    "s_add_u32 m0, s52, 12288\n\t" \
    "s_add_u32 s53, s50, 12288\n\t" \
    "buffer_load_dwordx4 v207, s[40:43], s53 offen sc1 lds\n\t" \
    "buffer_load_dwordx4 v[212:215], v211, s[60:63], 0 offen\n\t" \
    "v_add_u32 v211, s58, v211\n\t" \
    "s_waitcnt lgkmcnt(1)\n\t" \
    "v_mfma_f32_32x32x16_bf16 v[48:63], v[160:163], v[112:115], v[48:63]\n\t" \
    "v_exp_f32 v42, v42\n\t" \
    "v_exp_f32 v43, v43\n\t" \
    "v_cvt_pk_bf16_f32 v124, v40, v41\n\t" \
    "v_exp_f32 v44, v44\n\t" \
    "s_waitcnt lgkmcnt(0)\n\t" \
    "v_mfma_f32_32x32x16_bf16 v[48:63], v[164:167], v[116:119], v[48:63]\n\t" \
    "v_exp_f32 v45, v45\n\t" \
    "v_cvt_pk_bf16_f32 v125, v42, v43\n\t" \
    "v_exp_f32 v46, v46\n\t" \
    "v_exp_f32 v47, v47\n\t" \
    "v_cvt_pk_bf16_f32 v126, v44, v45\n\t" \
    "v_cvt_pk_bf16_f32 v127, v46, v47\n\t" \
    "s_waitcnt vmcnt(4)\n\t" \
    "s_barrier\n\t" \
    "v_mfma_f32_16x16x32_bf16 v[176:179], v[184:187], v[120:123], v[180:183]\n\t" \
    "v_exp_f32 v16, v16\n\t" \
    "v_exp_f32 v17, v17\n\t" \
    "v_mfma_f32_16x16x32_bf16 v[176:179], v[184:187], v[124:127], v[176:179]\n\t" \
    "v_exp_f32 v18, v18\n\t" \
    "v_exp_f32 v19, v19\n\t" \
    "v_cvt_pk_bf16_f32 v112, v16, v17\n\t" \
    "v_mfma_f32_32x32x16_bf16 v[32:47], v[152:155], v[136:139], v[96:111]\n\t" \
    "v_exp_f32 v20, v20\n\t" \
    "v_exp_f32 v21, v21\n\t" \
    "v_cvt_pk_bf16_f32 v113, v18, v19\n\t" \
    "v_exp_f32 v22, v22\n\t" \
    "v_mfma_f32_32x32x16_bf16 v[32:47], v[156:159], v[140:143], v[32:47]\n\t" \
    "v_cmp_ngt_f32 vcc, s54, v176\n\t" \
    "v_exp_f32 v23, v23\n\t" \
    "v_cvt_pk_bf16_f32 v114, v20, v21\n\t" \
    "v_exp_f32 v24, v24\n\t" \
    "v_exp_f32 v25, v25\n\t" \
    "v_cvt_pk_bf16_f32 v115, v22, v23\n\t" \
    "s_cbranch_vccnz LrareA3v1_%=\n\t" \
    "LretA3v1_%=:\n\t" \
    "ds_read_b128 v[144:147], v206 offset:0\n\t" \
    "ds_read_b128 v[148:151], v206 offset:1024\n\t" \
    "v_mfma_f32_32x32x16_bf16 v[64:79], v[160:163], v[120:123], v[64:79]\n\t" \
    "v_exp_f32 v26, v26\n\t" \
    "v_exp_f32 v27, v27\n\t" \
    "v_cvt_pk_bf16_f32 v116, v24, v25\n\t" \
    "v_exp_f32 v28, v28\n\t" \
    "v_mfma_f32_32x32x16_bf16 v[64:79], v[164:167], v[124:127], v[64:79]\n\t" \
    "ds_read_b128 v[160:163], v205 offset:14336\n\t" \
    "ds_read_b128 v[164:167], v205 offset:15360\n\t" \
    "v_exp_f32 v29, v29\n\t" \
    "v_cvt_pk_bf16_f32 v117, v26, v27\n\t" \
    "v_exp_f32 v30, v30\n\t" \
    "v_exp_f32 v31, v31\n\t" \
    "v_cvt_pk_bf16_f32 v118, v28, v29\n\t" \
    "v_cvt_pk_bf16_f32 v119, v30, v31\n\t" \
    "v_mfma_f32_16x16x32_bf16 v[168:171], v[184:187], v[112:115], v[172:175]\n\t" \
    "v_exp_f32 v32, v32\n\t" \
    "v_exp_f32 v33, v33\n\t" \
    "v_mfma_f32_16x16x32_bf16 v[168:171], v[184:187], v[116:119], v[168:171]\n\t" \
    "v_exp_f32 v34, v34\n\t" \
    "v_exp_f32 v35, v35\n\t" \
    "v_cvt_pk_bf16_f32 v120, v32, v33\n\t" \
    "s_waitcnt lgkmcnt(3)\n\t" \
    "v_mfma_f32_32x32x16_bf16 v[16:31], v[144:147], v[128:131], v[80:95]\n\t" \
    "v_exp_f32 v36, v36\n\t" \
    "v_exp_f32 v37, v37\n\t" \
    "v_cvt_pk_bf16_f32 v121, v34, v35\n\t" \
    "v_exp_f32 v38, v38\n\t" \
    "s_waitcnt lgkmcnt(2)\n\t" \
    "v_mfma_f32_32x32x16_bf16 v[16:31], v[148:151], v[132:135], v[16:31]\n\t" \
    "v_cmp_ngt_f32 vcc, s54, v168\n\t" \
    "v_exp_f32 v39, v39\n\t" \
    "v_cvt_pk_bf16_f32 v122, v36, v37\n\t" \
    "v_exp_f32 v40, v40\n\t" \
    "v_exp_f32 v41, v41\n\t" \
    "v_cvt_pk_bf16_f32 v123, v38, v39\n\t" \
    "s_cbranch_vccnz LrareB3v1_%=\n\t" \
    "LretB3v1_%=:\n\t" \
    "s_add_u32 s50, s50, 16384\n\t" \
    "s_add_u32 s51, s51, 32768\n\t" \
    "s_and_b32 s51, s51, 131071\n\t" \
    "s_cmp_lt_u32 s50, 131072\n\t" \
    "s_cselect_b32 s42, 131072, 0\n\t" \
    "s_add_u32 s52, s48, s51\n\t" \
    "s_add_u32 m0, s52, 0\n\t" \
    "s_add_u32 s53, s50, 0\n\t" \
    "buffer_load_dwordx4 v207, s[40:43], s53 offen sc1 lds\n\t" \
    "buffer_load_dwordx4 v[212:215], v216, s[72:75], 0 offen\n\t" \
    "v_add_u32 v216, s57, v216\n\t" \
    "s_waitcnt lgkmcnt(1)\n\t" \
    "v_mfma_f32_32x32x16_bf16 v[48:63], v[160:163], v[112:115], v[48:63]\n\t" \
    "v_exp_f32 v42, v42\n\t" \
    "v_exp_f32 v43, v43\n\t" \
    "v_cvt_pk_bf16_f32 v124, v40, v41\n\t" \
    "v_exp_f32 v44, v44\n\t" \
    "s_waitcnt lgkmcnt(0)\n\t" \
    "v_mfma_f32_32x32x16_bf16 v[48:63], v[164:167], v[116:119], v[48:63]\n\t" \
    "v_exp_f32 v45, v45\n\t" \
    "v_cvt_pk_bf16_f32 v125, v42, v43\n\t" \
    "v_exp_f32 v46, v46\n\t" \
    "v_exp_f32 v47, v47\n\t" \
    "v_cvt_pk_bf16_f32 v126, v44, v45\n\t" \
    "v_mov_b32 v205, v206\n\t" \
    "s_add_u32 s55, s55, 32768\n\t" \
    "s_and_b32 s55, s55, 131071\n\t" \
    "v_add_u32 v206, s55, v204\n\t" \
    "s_sub_u32 s49, s49, 1\n\t" \
    "s_cmp_lg_u32 s49, 0\n\t" \
    "v_cvt_pk_bf16_f32 v127, v46, v47\n\t" \
    "s_cbranch_scc1 Lloop1_%=\n\t" \
    "Ldrain_%=:\n\t" \
    "s_waitcnt lgkmcnt(0)\n\t" \
    "v_mfma_f32_16x16x32_bf16 v[180:183], v[184:187], v[120:123], v[176:179]\n\t" \
    "v_mfma_f32_16x16x32_bf16 v[180:183], v[184:187], v[124:127], v[180:183]\n\t" \
    "s_nop 7\n\t" \
    "v_cmp_ngt_f32 vcc, s54, v180\n\t" \
    "s_cbranch_vccnz LrareZ_%=\n\t" \
    "LretZ_%=:\n\t" \
    "v_mfma_f32_32x32x16_bf16 v[64:79], v[160:163], v[120:123], v[64:79]\n\t" \
    "v_mfma_f32_32x32x16_bf16 v[64:79], v[164:167], v[124:127], v[64:79]\n\t" \
    "s_waitcnt vmcnt(0) lgkmcnt(0)\n\t" \
    "s_barrier\n\t" \
    "s_cmp_lg_u32 %[hf], 0\n\t" \
    "s_cbranch_scc1 Lhf1_%=\n\t" \
    "s_nop 7\n\t" \
    "v_mov_b32 v188, v64\n\t" \
    "v_mov_b32 v16, v48\n\t" \
    "v_mov_b32 v189, v65\n\t" \
    "v_mov_b32 v17, v49\n\t" \
    "v_mov_b32 v190, v66\n\t" \
    "v_mov_b32 v18, v50\n\t" \
    "v_mov_b32 v191, v67\n\t" \
    "v_mov_b32 v19, v51\n\t" \
    "v_mov_b32 v192, v68\n\t" \
    "v_mov_b32 v20, v52\n\t" \
    "v_mov_b32 v193, v69\n\t" \
    "v_mov_b32 v21, v53\n\t" \
    "v_mov_b32 v194, v70\n\t" \
    "v_mov_b32 v22, v54\n\t" \
    "v_mov_b32 v195, v71\n\t" \
    "v_mov_b32 v23, v55\n\t" \
    "v_mov_b32 v196, v72\n\t" \
    "v_mov_b32 v24, v56\n\t" \
    "v_mov_b32 v197, v73\n\t" \
    "v_mov_b32 v25, v57\n\t" \
    "v_mov_b32 v198, v74\n\t" \
    "v_mov_b32 v26, v58\n\t" \
    "v_mov_b32 v199, v75\n\t" \
    "v_mov_b32 v27, v59\n\t" \
    "v_mov_b32 v200, v76\n\t" \
    "v_mov_b32 v28, v60\n\t" \
    "v_mov_b32 v201, v77\n\t" \
    "v_mov_b32 v29, v61\n\t" \
    "v_mov_b32 v202, v78\n\t" \
    "v_mov_b32 v30, v62\n\t" \
    "v_mov_b32 v203, v79\n\t" \
    "v_mov_b32 v31, v63\n\t" \
    "v_mov_b32 v116, v180\n\t" \
    "v_mov_b32 v117, v96\n\t" \
    "v_mov_b32 v112, v168\n\t" \
    "v_mov_b32 v113, v80\n\t" \
    "s_branch Lhf2_%=\n\t" \
    "Lhf1_%=:\n\t" \
    "s_nop 11\n\t" \
    "v_mov_b32 v188, v48\n\t" \
    "v_mov_b32 v16, v64\n\t" \
    "v_mov_b32 v189, v49\n\t" \
    "v_mov_b32 v17, v65\n\t" \
    "v_mov_b32 v190, v50\n\t" \
    "v_mov_b32 v18, v66\n\t" \
    "v_mov_b32 v191, v51\n\t" \
    "v_mov_b32 v19, v67\n\t" \
    "v_mov_b32 v192, v52\n\t" \
    "v_mov_b32 v20, v68\n\t" \
    "v_mov_b32 v193, v53\n\t" \
    "v_mov_b32 v21, v69\n\t" \
    "v_mov_b32 v194, v54\n\t" \
    "v_mov_b32 v22, v70\n\t" \
    "v_mov_b32 v195, v55\n\t" \
    "v_mov_b32 v23, v71\n\t" \
    "v_mov_b32 v196, v56\n\t" \
    "v_mov_b32 v24, v72\n\t" \
    "v_mov_b32 v197, v57\n\t" \
    "v_mov_b32 v25, v73\n\t" \
    "v_mov_b32 v198, v58\n\t" \
    "v_mov_b32 v26, v74\n\t" \
    "v_mov_b32 v199, v59\n\t" \
    "v_mov_b32 v27, v75\n\t" \
    "v_mov_b32 v200, v60\n\t" \
    "v_mov_b32 v28, v76\n\t" \
    "v_mov_b32 v201, v61\n\t" \
    "v_mov_b32 v29, v77\n\t" \
    "v_mov_b32 v202, v62\n\t" \
    "v_mov_b32 v30, v78\n\t" \
    "v_mov_b32 v203, v63\n\t" \
    "v_mov_b32 v31, v79\n\t" \
    "v_mov_b32 v116, v168\n\t" \
    "v_mov_b32 v117, v80\n\t" \
    "v_mov_b32 v112, v180\n\t" \
    "v_mov_b32 v113, v96\n\t" \
    "Lhf2_%=:\n\t" \
    "s_nop 1\n\t" \
    "v_lshlrev_b32 v208, 4, %[lane]\n\t" \
    "v_add_u32 v211, %[xw], v208\n\t" \
    "v_add_u32 v208, %[xr], v208\n\t" \
    "ds_write_b128 v211, v[188:191] offset:0\n\t" \
    "ds_write_b128 v211, v[192:195] offset:1024\n\t" \
    "ds_write_b128 v211, v[196:199] offset:2048\n\t" \
    "ds_write_b128 v211, v[200:203] offset:3072\n\t" \
    "ds_write_b64 v211, v[116:117] offset:4096\n\t" \
    "s_waitcnt lgkmcnt(0)\n\t" \
    "s_barrier\n\t" \
    "ds_read_b128 v[188:191], v208 offset:0\n\t" \
    "ds_read_b128 v[192:195], v208 offset:1024\n\t" \
    "ds_read_b128 v[196:199], v208 offset:2048\n\t" \
    "ds_read_b128 v[200:203], v208 offset:3072\n\t" \
    "ds_read_b64 v[114:115], v208 offset:4096\n\t" \
    "s_waitcnt lgkmcnt(0)\n\t" \
    "v_min_f32 v209, v113, v115\n\t" \
    "v_sub_f32 v113, v209, v113\n\t" \
    "v_sub_f32 v115, v209, v115\n\t" \
    "v_exp_f32 v113, v113\n\t" \
    "v_exp_f32 v115, v115\n\t" \
    "v_mul_f32 v112, v112, v113\n\t" \
    "v_fmac_f32 v112, v114, v115\n\t" \
    "v_rcp_f32 v112, v112\n\t" \
    "s_nop 0\n\t" \
    "v_mul_f32 v113, v113, v112\n\t" \
    "v_mul_f32 v115, v115, v112\n\t" \
    "v_mul_f32 v16, v16, v113\n\t" \
    "v_fmac_f32 v16, v188, v115\n\t" \
    "v_mul_f32 v17, v17, v113\n\t" \
    "v_fmac_f32 v17, v189, v115\n\t" \
    "v_mul_f32 v18, v18, v113\n\t" \
    "v_fmac_f32 v18, v190, v115\n\t" \
    "v_mul_f32 v19, v19, v113\n\t" \
    "v_fmac_f32 v19, v191, v115\n\t" \
    "v_mul_f32 v20, v20, v113\n\t" \
    "v_fmac_f32 v20, v192, v115\n\t" \
    "v_mul_f32 v21, v21, v113\n\t" \
    "v_fmac_f32 v21, v193, v115\n\t" \
    "v_mul_f32 v22, v22, v113\n\t" \
    "v_fmac_f32 v22, v194, v115\n\t" \
    "v_mul_f32 v23, v23, v113\n\t" \
    "v_fmac_f32 v23, v195, v115\n\t" \
    "v_mul_f32 v24, v24, v113\n\t" \
    "v_fmac_f32 v24, v196, v115\n\t" \
    "v_mul_f32 v25, v25, v113\n\t" \
    "v_fmac_f32 v25, v197, v115\n\t" \
    "v_mul_f32 v26, v26, v113\n\t" \
    "v_fmac_f32 v26, v198, v115\n\t" \
    "v_mul_f32 v27, v27, v113\n\t" \
    "v_fmac_f32 v27, v199, v115\n\t" \
    "v_mul_f32 v28, v28, v113\n\t" \
    "v_fmac_f32 v28, v200, v115\n\t" \
    "v_mul_f32 v29, v29, v113\n\t" \
    "v_fmac_f32 v29, v201, v115\n\t" \
    "v_mul_f32 v30, v30, v113\n\t" \
    "v_fmac_f32 v30, v202, v115\n\t" \
    "v_mul_f32 v31, v31, v113\n\t" \
    "v_fmac_f32 v31, v203, v115\n\t" \
    "v_cvt_pk_bf16_f32 v120, v16, v17\n\t" \
    "v_cvt_pk_bf16_f32 v121, v18, v19\n\t" \
    "v_cvt_pk_bf16_f32 v122, v20, v21\n\t" \
    "v_cvt_pk_bf16_f32 v123, v22, v23\n\t" \
    "v_cvt_pk_bf16_f32 v124, v24, v25\n\t" \
    "v_cvt_pk_bf16_f32 v125, v26, v27\n\t" \
    "v_cvt_pk_bf16_f32 v126, v28, v29\n\t" \
    "v_cvt_pk_bf16_f32 v127, v30, v31\n\t" \
    "v_permlane16_swap_b32 v120, v124\n\t" \
    "v_permlane16_swap_b32 v121, v125\n\t" \
    "v_permlane16_swap_b32 v122, v126\n\t" \
    "v_permlane16_swap_b32 v123, v127\n\t" \
    "v_lshlrev_b32 v208, 4, %[lane]\n\t" \
    "v_add_u32 v211, 32768, v208\n\t" \
    "global_store_dwordx4 v208, v[120:123], s[46:47] offset:0 sc1\n\t" \
    "global_store_dwordx4 v211, v[124:127], s[46:47] offset:0 sc1\n\t" \
    "s_waitcnt vmcnt(0)\n\t" \
    "s_branch Lend_%=\n\t" \
    "LrareA0v0_%=:\n\t" \
    "s_nop 15\n\t" \
    "v_mfma_f32_32x32x16_bf16 v[188:203], v[152:155], v[136:139], v[96:111]\n\t" \
    "v_mfma_f32_32x32x16_bf16 v[188:203], v[156:159], v[140:143], v[188:203]\n\t" \
    "s_nop 11\n\t" \
    "v_max3_f32 v208, v188, v189, v190\n\t" \
    "v_max3_f32 v209, v191, v192, v193\n\t" \
    "v_max3_f32 v208, v208, v194, v195\n\t" \
    "v_max3_f32 v209, v209, v196, v197\n\t" \
    "v_max3_f32 v208, v208, v198, v199\n\t" \
    "v_max3_f32 v209, v209, v200, v201\n\t" \
    "v_max3_f32 v208, v208, v202, v203\n\t" \
    "v_max_f32 v208, v208, v209\n\t" \
    "v_mov_b32 v209, v208\n\t" \
    "s_nop 1\n\t" \
    "v_permlane32_swap_b32 v208, v209\n\t" \
    "v_max_f32 v208, v208, v209\n\t" \
    "v_log_f32 v210, v176\n\t" \
    "s_nop 0\n\t" \
    "v_max_f32 v208, v208, v210\n\t" \
    "v_add_f32 v208, s56, v208\n\t" \
    "v_sub_f32 v210, 0, v208\n\t" \
    "v_exp_f32 v210, v210\n\t" \
    "v_sub_f32 v188, v188, v208\n\t" \
    "v_exp_f32 v188, v188\n\t" \
    "v_sub_f32 v96, v96, v208\n\t" \
    "v_sub_f32 v32, v32, v208\n\t" \
    "v_mul_f32 v64, v64, v210\n\t" \
    "v_sub_f32 v189, v189, v208\n\t" \
    "v_exp_f32 v189, v189\n\t" \
    "v_sub_f32 v97, v97, v208\n\t" \
    "v_sub_f32 v33, v33, v208\n\t" \
    "v_mul_f32 v65, v65, v210\n\t" \
    "v_sub_f32 v190, v190, v208\n\t" \
    "v_exp_f32 v190, v190\n\t" \
    "v_sub_f32 v98, v98, v208\n\t" \
    "v_sub_f32 v34, v34, v208\n\t" \
    "v_mul_f32 v66, v66, v210\n\t" \
    "v_sub_f32 v191, v191, v208\n\t" \
    "v_exp_f32 v191, v191\n\t" \
    "v_sub_f32 v99, v99, v208\n\t" \
    "v_sub_f32 v35, v35, v208\n\t" \
    "v_mul_f32 v67, v67, v210\n\t" \
    "v_sub_f32 v192, v192, v208\n\t" \
    "v_exp_f32 v192, v192\n\t" \
    "v_sub_f32 v100, v100, v208\n\t" \
    "v_sub_f32 v36, v36, v208\n\t" \
    "v_mul_f32 v68, v68, v210\n\t" \
    "v_sub_f32 v193, v193, v208\n\t" \
    "v_exp_f32 v193, v193\n\t" \
    "v_sub_f32 v101, v101, v208\n\t" \
    "v_sub_f32 v37, v37, v208\n\t" \
    "v_mul_f32 v69, v69, v210\n\t" \
    "v_sub_f32 v194, v194, v208\n\t" \
    "v_exp_f32 v194, v194\n\t" \
    "v_sub_f32 v102, v102, v208\n\t" \
    "v_sub_f32 v38, v38, v208\n\t" \
    "v_mul_f32 v70, v70, v210\n\t" \
    "v_sub_f32 v195, v195, v208\n\t" \
    "v_exp_f32 v195, v195\n\t" \
    "v_sub_f32 v103, v103, v208\n\t" \
    "v_sub_f32 v39, v39, v208\n\t" \
    "v_mul_f32 v71, v71, v210\n\t" \
    "v_sub_f32 v196, v196, v208\n\t" \
    "v_exp_f32 v196, v196\n\t" \
    "v_sub_f32 v104, v104, v208\n\t" \
    "v_sub_f32 v40, v40, v208\n\t" \
    "v_mul_f32 v72, v72, v210\n\t" \
    "v_sub_f32 v197, v197, v208\n\t" \
    "v_exp_f32 v197, v197\n\t" \
    "v_sub_f32 v105, v105, v208\n\t" \
    "v_sub_f32 v41, v41, v208\n\t" \
    "v_mul_f32 v73, v73, v210\n\t" \
    "v_sub_f32 v198, v198, v208\n\t" \
    "v_exp_f32 v198, v198\n\t" \
    "v_sub_f32 v106, v106, v208\n\t" \
    "v_sub_f32 v42, v42, v208\n\t" \
    "v_mul_f32 v74, v74, v210\n\t" \
    "v_sub_f32 v199, v199, v208\n\t" \
    "v_exp_f32 v199, v199\n\t" \
    "v_sub_f32 v107, v107, v208\n\t" \
    "v_sub_f32 v43, v43, v208\n\t" \
    "v_mul_f32 v75, v75, v210\n\t" \
    "v_sub_f32 v200, v200, v208\n\t" \
    "v_exp_f32 v200, v200\n\t" \
    "v_sub_f32 v108, v108, v208\n\t" \
    "v_sub_f32 v44, v44, v208\n\t" \
    "v_mul_f32 v76, v76, v210\n\t" \
    "v_sub_f32 v201, v201, v208\n\t" \
    "v_exp_f32 v201, v201\n\t" \
    "v_sub_f32 v109, v109, v208\n\t" \
    "v_sub_f32 v45, v45, v208\n\t" \
    "v_mul_f32 v77, v77, v210\n\t" \
    "v_sub_f32 v202, v202, v208\n\t" \
    "v_exp_f32 v202, v202\n\t" \
    "v_sub_f32 v110, v110, v208\n\t" \
    "v_sub_f32 v46, v46, v208\n\t" \
    "v_mul_f32 v78, v78, v210\n\t" \
    "v_sub_f32 v203, v203, v208\n\t" \
    "v_exp_f32 v203, v203\n\t" \
    "v_sub_f32 v111, v111, v208\n\t" \
    "v_sub_f32 v47, v47, v208\n\t" \
    "v_mul_f32 v79, v79, v210\n\t" \
    "v_mul_f32 v176, v176, v210\n\t" \
    "v_cvt_pk_bf16_f32 v120, v188, v189\n\t" \
    "v_cvt_pk_bf16_f32 v121, v190, v191\n\t" \
    "v_cvt_pk_bf16_f32 v122, v192, v193\n\t" \
    "v_cvt_pk_bf16_f32 v123, v194, v195\n\t" \
    "v_cvt_pk_bf16_f32 v124, v196, v197\n\t" \
    "v_cvt_pk_bf16_f32 v125, v198, v199\n\t" \
    "v_cvt_pk_bf16_f32 v126, v200, v201\n\t" \
    "v_cvt_pk_bf16_f32 v127, v202, v203\n\t" \
    "v_mfma_f32_16x16x32_bf16 v[180:183], v[184:187], v[120:123], v[176:179]\n\t" \
    "s_nop 0\n\t" \
    "v_mfma_f32_16x16x32_bf16 v[180:183], v[184:187], v[124:127], v[180:183]\n\t" \
    "s_nop 15\n\t" \
    "s_branch LretA0v0_%=\n\t" \
    "LrareB0v0_%=:\n\t" \
    "s_nop 15\n\t" \
    "v_mfma_f32_32x32x16_bf16 v[188:203], v[144:147], v[128:131], v[80:95]\n\t" \
    "v_mfma_f32_32x32x16_bf16 v[188:203], v[148:151], v[132:135], v[188:203]\n\t" \
    "s_nop 11\n\t" \
    "v_max3_f32 v208, v188, v189, v190\n\t" \
    "v_max3_f32 v209, v191, v192, v193\n\t" \
    "v_max3_f32 v208, v208, v194, v195\n\t" \
    "v_max3_f32 v209, v209, v196, v197\n\t" \
    "v_max3_f32 v208, v208, v198, v199\n\t" \
    "v_max3_f32 v209, v209, v200, v201\n\t" \
    "v_max3_f32 v208, v208, v202, v203\n\t" \
    "v_max_f32 v208, v208, v209\n\t" \
    "v_mov_b32 v209, v208\n\t" \
    "s_nop 1\n\t" \
    "v_permlane32_swap_b32 v208, v209\n\t" \
    "v_max_f32 v208, v208, v209\n\t" \
    "v_log_f32 v210, v168\n\t" \
    "s_nop 0\n\t" \
    "v_max_f32 v208, v208, v210\n\t" \
    "v_add_f32 v208, s56, v208\n\t" \
    "v_sub_f32 v210, 0, v208\n\t" \
    "v_exp_f32 v210, v210\n\t" \
    "v_sub_f32 v188, v188, v208\n\t" \
    "v_exp_f32 v188, v188\n\t" \
    "v_sub_f32 v80, v80, v208\n\t" \
    "v_sub_f32 v16, v16, v208\n\t" \
    "v_mul_f32 v48, v48, v210\n\t" \
    "v_sub_f32 v189, v189, v208\n\t" \
    "v_exp_f32 v189, v189\n\t" \
    "v_sub_f32 v81, v81, v208\n\t" \
    "v_sub_f32 v17, v17, v208\n\t" \
    "v_mul_f32 v49, v49, v210\n\t" \
    "v_sub_f32 v190, v190, v208\n\t" \
    "v_exp_f32 v190, v190\n\t" \
    "v_sub_f32 v82, v82, v208\n\t" \
    "v_sub_f32 v18, v18, v208\n\t" \
    "v_mul_f32 v50, v50, v210\n\t" \
    "v_sub_f32 v191, v191, v208\n\t" \
    "v_exp_f32 v191, v191\n\t" \
    "v_sub_f32 v83, v83, v208\n\t" \
    "v_sub_f32 v19, v19, v208\n\t" \
    "v_mul_f32 v51, v51, v210\n\t" \
    "v_sub_f32 v192, v192, v208\n\t" \
    "v_exp_f32 v192, v192\n\t" \
    "v_sub_f32 v84, v84, v208\n\t" \
    "v_sub_f32 v20, v20, v208\n\t" \
    "v_mul_f32 v52, v52, v210\n\t" \
    "v_sub_f32 v193, v193, v208\n\t" \
    "v_exp_f32 v193, v193\n\t" \
    "v_sub_f32 v85, v85, v208\n\t" \
    "v_sub_f32 v21, v21, v208\n\t" \
    "v_mul_f32 v53, v53, v210\n\t" \
    "v_sub_f32 v194, v194, v208\n\t" \
    "v_exp_f32 v194, v194\n\t" \
    "v_sub_f32 v86, v86, v208\n\t" \
    "v_sub_f32 v22, v22, v208\n\t" \
    "v_mul_f32 v54, v54, v210\n\t" \
    "v_sub_f32 v195, v195, v208\n\t" \
    "v_exp_f32 v195, v195\n\t" \
    "v_sub_f32 v87, v87, v208\n\t" \
    "v_sub_f32 v23, v23, v208\n\t" \
    "v_mul_f32 v55, v55, v210\n\t" \
    "v_sub_f32 v196, v196, v208\n\t" \
    "v_exp_f32 v196, v196\n\t" \
    "v_sub_f32 v88, v88, v208\n\t" \
    "v_sub_f32 v24, v24, v208\n\t" \
    "v_mul_f32 v56, v56, v210\n\t" \
    "v_sub_f32 v197, v197, v208\n\t" \
    "v_exp_f32 v197, v197\n\t" \
    "v_sub_f32 v89, v89, v208\n\t" \
    "v_sub_f32 v25, v25, v208\n\t" \
    "v_mul_f32 v57, v57, v210\n\t" \
    "v_sub_f32 v198, v198, v208\n\t" \
    "v_exp_f32 v198, v198\n\t" \
    "v_sub_f32 v90, v90, v208\n\t" \
    "v_sub_f32 v26, v26, v208\n\t" \
    "v_mul_f32 v58, v58, v210\n\t" \
    "v_sub_f32 v199, v199, v208\n\t" \
    "v_exp_f32 v199, v199\n\t" \
    "v_sub_f32 v91, v91, v208\n\t" \
    "v_sub_f32 v27, v27, v208\n\t" \
    "v_mul_f32 v59, v59, v210\n\t" \
    "v_sub_f32 v200, v200, v208\n\t" \
    "v_exp_f32 v200, v200\n\t" \
    "v_sub_f32 v92, v92, v208\n\t" \
    "v_sub_f32 v28, v28, v208\n\t" \
    "v_mul_f32 v60, v60, v210\n\t" \
    "v_sub_f32 v201, v201, v208\n\t" \
    "v_exp_f32 v201, v201\n\t" \
    "v_sub_f32 v93, v93, v208\n\t" \
    "v_sub_f32 v29, v29, v208\n\t" \
    "v_mul_f32 v61, v61, v210\n\t" \
    "v_sub_f32 v202, v202, v208\n\t" \
    "v_exp_f32 v202, v202\n\t" \
    "v_sub_f32 v94, v94, v208\n\t" \
    "v_sub_f32 v30, v30, v208\n\t" \
    "v_mul_f32 v62, v62, v210\n\t" \
    "v_sub_f32 v203, v203, v208\n\t" \
    "v_exp_f32 v203, v203\n\t" \
    "v_sub_f32 v95, v95, v208\n\t" \
    "v_sub_f32 v31, v31, v208\n\t" \
    "v_mul_f32 v63, v63, v210\n\t" \
    "v_mul_f32 v168, v168, v210\n\t" \
    "v_cvt_pk_bf16_f32 v112, v188, v189\n\t" \
    "v_cvt_pk_bf16_f32 v113, v190, v191\n\t" \
    "v_cvt_pk_bf16_f32 v114, v192, v193\n\t" \
    "v_cvt_pk_bf16_f32 v115, v194, v195\n\t" \
    "v_cvt_pk_bf16_f32 v116, v196, v197\n\t" \
    "v_cvt_pk_bf16_f32 v117, v198, v199\n\t" \
    "v_cvt_pk_bf16_f32 v118, v200, v201\n\t" \
    "v_cvt_pk_bf16_f32 v119, v202, v203\n\t" \
    "v_mfma_f32_16x16x32_bf16 v[172:175], v[184:187], v[112:115], v[168:171]\n\t" \
    "s_nop 0\n\t" \
    "v_mfma_f32_16x16x32_bf16 v[172:175], v[184:187], v[116:119], v[172:175]\n\t" \
    "s_nop 15\n\t" \
    "s_branch LretB0v0_%=\n\t" \
    "LrareA1v0_%=:\n\t" \
    "s_nop 15\n\t" \
    "v_mfma_f32_32x32x16_bf16 v[188:203], v[144:147], v[136:139], v[96:111]\n\t" \
    "v_mfma_f32_32x32x16_bf16 v[188:203], v[148:151], v[140:143], v[188:203]\n\t" \
    "s_nop 11\n\t" \
    "v_max3_f32 v208, v188, v189, v190\n\t" \
    "v_max3_f32 v209, v191, v192, v193\n\t" \
    "v_max3_f32 v208, v208, v194, v195\n\t" \
    "v_max3_f32 v209, v209, v196, v197\n\t" \
    "v_max3_f32 v208, v208, v198, v199\n\t" \
    "v_max3_f32 v209, v209, v200, v201\n\t" \
    "v_max3_f32 v208, v208, v202, v203\n\t" \
    "v_max_f32 v208, v208, v209\n\t" \
    "v_mov_b32 v209, v208\n\t" \
    "s_nop 1\n\t" \
    "v_permlane32_swap_b32 v208, v209\n\t" \
    "v_max_f32 v208, v208, v209\n\t" \
    "v_log_f32 v210, v180\n\t" \
    "s_nop 0\n\t" \
    "v_max_f32 v208, v208, v210\n\t" \
    "v_add_f32 v208, s56, v208\n\t" \
    "v_sub_f32 v210, 0, v208\n\t" \
    "v_exp_f32 v210, v210\n\t" \
    "v_sub_f32 v188, v188, v208\n\t" \
    "v_exp_f32 v188, v188\n\t" \
    "v_sub_f32 v96, v96, v208\n\t" \
    "v_sub_f32 v32, v32, v208\n\t" \
    "v_mul_f32 v64, v64, v210\n\t" \
    "v_sub_f32 v189, v189, v208\n\t" \
    "v_exp_f32 v189, v189\n\t" \
    "v_sub_f32 v97, v97, v208\n\t" \
    "v_sub_f32 v33, v33, v208\n\t" \
    "v_mul_f32 v65, v65, v210\n\t" \
    "v_sub_f32 v190, v190, v208\n\t" \
    "v_exp_f32 v190, v190\n\t" \
    "v_sub_f32 v98, v98, v208\n\t" \
    "v_sub_f32 v34, v34, v208\n\t" \
    "v_mul_f32 v66, v66, v210\n\t" \
    "v_sub_f32 v191, v191, v208\n\t" \
    "v_exp_f32 v191, v191\n\t" \
    "v_sub_f32 v99, v99, v208\n\t" \
    "v_sub_f32 v35, v35, v208\n\t" \
    "v_mul_f32 v67, v67, v210\n\t" \
    "v_sub_f32 v192, v192, v208\n\t" \
    "v_exp_f32 v192, v192\n\t" \
    "v_sub_f32 v100, v100, v208\n\t" \
    "v_sub_f32 v36, v36, v208\n\t" \
    "v_mul_f32 v68, v68, v210\n\t" \
    "v_sub_f32 v193, v193, v208\n\t" \
    "v_exp_f32 v193, v193\n\t" \
    "v_sub_f32 v101, v101, v208\n\t" \
    "v_sub_f32 v37, v37, v208\n\t" \
    "v_mul_f32 v69, v69, v210\n\t" \
    "v_sub_f32 v194, v194, v208\n\t" \
    "v_exp_f32 v194, v194\n\t" \
    "v_sub_f32 v102, v102, v208\n\t" \
    "v_sub_f32 v38, v38, v208\n\t" \
    "v_mul_f32 v70, v70, v210\n\t" \
    "v_sub_f32 v195, v195, v208\n\t" \
    "v_exp_f32 v195, v195\n\t" \
    "v_sub_f32 v103, v103, v208\n\t" \
    "v_sub_f32 v39, v39, v208\n\t" \
    "v_mul_f32 v71, v71, v210\n\t" \
    "v_sub_f32 v196, v196, v208\n\t" \
    "v_exp_f32 v196, v196\n\t" \
    "v_sub_f32 v104, v104, v208\n\t" \
    "v_sub_f32 v40, v40, v208\n\t" \
    "v_mul_f32 v72, v72, v210\n\t" \
    "v_sub_f32 v197, v197, v208\n\t" \
    "v_exp_f32 v197, v197\n\t" \
    "v_sub_f32 v105, v105, v208\n\t" \
    "v_sub_f32 v41, v41, v208\n\t" \
    "v_mul_f32 v73, v73, v210\n\t" \
    "v_sub_f32 v198, v198, v208\n\t" \
    "v_exp_f32 v198, v198\n\t" \
    "v_sub_f32 v106, v106, v208\n\t" \
    "v_sub_f32 v42, v42, v208\n\t" \
    "v_mul_f32 v74, v74, v210\n\t" \
    "v_sub_f32 v199, v199, v208\n\t" \
    "v_exp_f32 v199, v199\n\t" \
    "v_sub_f32 v107, v107, v208\n\t" \
    "v_sub_f32 v43, v43, v208\n\t" \
    "v_mul_f32 v75, v75, v210\n\t" \
    "v_sub_f32 v200, v200, v208\n\t" \
    "v_exp_f32 v200, v200\n\t" \
    "v_sub_f32 v108, v108, v208\n\t" \
    "v_sub_f32 v44, v44, v208\n\t" \
    "v_mul_f32 v76, v76, v210\n\t" \
    "v_sub_f32 v201, v201, v208\n\t" \
    "v_exp_f32 v201, v201\n\t" \
    "v_sub_f32 v109, v109, v208\n\t" \
    "v_sub_f32 v45, v45, v208\n\t" \
    "v_mul_f32 v77, v77, v210\n\t" \
    "v_sub_f32 v202, v202, v208\n\t" \
    "v_exp_f32 v202, v202\n\t" \
    "v_sub_f32 v110, v110, v208\n\t" \
    "v_sub_f32 v46, v46, v208\n\t" \
    "v_mul_f32 v78, v78, v210\n\t" \
    "v_sub_f32 v203, v203, v208\n\t" \
    "v_exp_f32 v203, v203\n\t" \
    "v_sub_f32 v111, v111, v208\n\t" \
    "v_sub_f32 v47, v47, v208\n\t" \
    "v_mul_f32 v79, v79, v210\n\t" \
    "v_mul_f32 v180, v180, v210\n\t" \
    "v_cvt_pk_bf16_f32 v120, v188, v189\n\t" \
    "v_cvt_pk_bf16_f32 v121, v190, v191\n\t" \
    "v_cvt_pk_bf16_f32 v122, v192, v193\n\t" \
    "v_cvt_pk_bf16_f32 v123, v194, v195\n\t" \
    "v_cvt_pk_bf16_f32 v124, v196, v197\n\t" \
    "v_cvt_pk_bf16_f32 v125, v198, v199\n\t" \
    "v_cvt_pk_bf16_f32 v126, v200, v201\n\t" \
    "v_cvt_pk_bf16_f32 v127, v202, v203\n\t" \
    "v_mfma_f32_16x16x32_bf16 v[176:179], v[184:187], v[120:123], v[180:183]\n\t" \
    "s_nop 0\n\t" \
    "v_mfma_f32_16x16x32_bf16 v[176:179], v[184:187], v[124:127], v[176:179]\n\t" \
    "s_nop 15\n\t" \
    "s_branch LretA1v0_%=\n\t" \
    "LrareB1v0_%=:\n\t" \
    "s_nop 15\n\t" \
    "v_mfma_f32_32x32x16_bf16 v[188:203], v[152:155], v[128:131], v[80:95]\n\t" \
    "v_mfma_f32_32x32x16_bf16 v[188:203], v[156:159], v[132:135], v[188:203]\n\t" \
    "s_nop 11\n\t" \
    "v_max3_f32 v208, v188, v189, v190\n\t" \
    "v_max3_f32 v209, v191, v192, v193\n\t" \
    "v_max3_f32 v208, v208, v194, v195\n\t" \
    "v_max3_f32 v209, v209, v196, v197\n\t" \
    "v_max3_f32 v208, v208, v198, v199\n\t" \
    "v_max3_f32 v209, v209, v200, v201\n\t" \
    "v_max3_f32 v208, v208, v202, v203\n\t" \
    "v_max_f32 v208, v208, v209\n\t" \
    "v_mov_b32 v209, v208\n\t" \
    "s_nop 1\n\t" \
    "v_permlane32_swap_b32 v208, v209\n\t" \
    "v_max_f32 v208, v208, v209\n\t" \
    "v_log_f32 v210, v172\n\t" \
    "s_nop 0\n\t" \
    "v_max_f32 v208, v208, v210\n\t" \
    "v_add_f32 v208, s56, v208\n\t" \
    "v_sub_f32 v210, 0, v208\n\t" \
    "v_exp_f32 v210, v210\n\t" \
    "v_sub_f32 v188, v188, v208\n\t" \
    "v_exp_f32 v188, v188\n\t" \
    "v_sub_f32 v80, v80, v208\n\t" \
    "v_sub_f32 v16, v16, v208\n\t" \
    "v_mul_f32 v48, v48, v210\n\t" \
    "v_sub_f32 v189, v189, v208\n\t" \
    "v_exp_f32 v189, v189\n\t" \
    "v_sub_f32 v81, v81, v208\n\t" \
    "v_sub_f32 v17, v17, v208\n\t" \
    "v_mul_f32 v49, v49, v210\n\t" \
    "v_sub_f32 v190, v190, v208\n\t" \
    "v_exp_f32 v190, v190\n\t" \
    "v_sub_f32 v82, v82, v208\n\t" \
    "v_sub_f32 v18, v18, v208\n\t" \
    "v_mul_f32 v50, v50, v210\n\t" \
    "v_sub_f32 v191, v191, v208\n\t" \
    "v_exp_f32 v191, v191\n\t" \
    "v_sub_f32 v83, v83, v208\n\t" \
    "v_sub_f32 v19, v19, v208\n\t" \
    "v_mul_f32 v51, v51, v210\n\t" \
    "v_sub_f32 v192, v192, v208\n\t" \
    "v_exp_f32 v192, v192\n\t" \
    "v_sub_f32 v84, v84, v208\n\t" \
    "v_sub_f32 v20, v20, v208\n\t" \
    "v_mul_f32 v52, v52, v210\n\t" \
    "v_sub_f32 v193, v193, v208\n\t" \
    "v_exp_f32 v193, v193\n\t" \
    "v_sub_f32 v85, v85, v208\n\t" \
    "v_sub_f32 v21, v21, v208\n\t" \
    "v_mul_f32 v53, v53, v210\n\t" \
    "v_sub_f32 v194, v194, v208\n\t" \
    "v_exp_f32 v194, v194\n\t" \
    "v_sub_f32 v86, v86, v208\n\t" \
    "v_sub_f32 v22, v22, v208\n\t" \
    "v_mul_f32 v54, v54, v210\n\t" \
    "v_sub_f32 v195, v195, v208\n\t" \
    "v_exp_f32 v195, v195\n\t" \
    "v_sub_f32 v87, v87, v208\n\t" \
    "v_sub_f32 v23, v23, v208\n\t" \
    "v_mul_f32 v55, v55, v210\n\t" \
    "v_sub_f32 v196, v196, v208\n\t" \
    "v_exp_f32 v196, v196\n\t" \
    "v_sub_f32 v88, v88, v208\n\t" \
    "v_sub_f32 v24, v24, v208\n\t" \
    "v_mul_f32 v56, v56, v210\n\t" \
    "v_sub_f32 v197, v197, v208\n\t" \
    "v_exp_f32 v197, v197\n\t" \
    "v_sub_f32 v89, v89, v208\n\t" \
    "v_sub_f32 v25, v25, v208\n\t" \
    "v_mul_f32 v57, v57, v210\n\t" \
    "v_sub_f32 v198, v198, v208\n\t" \
    "v_exp_f32 v198, v198\n\t" \
    "v_sub_f32 v90, v90, v208\n\t" \
    "v_sub_f32 v26, v26, v208\n\t" \
    "v_mul_f32 v58, v58, v210\n\t" \
    "v_sub_f32 v199, v199, v208\n\t" \
    "v_exp_f32 v199, v199\n\t" \
    "v_sub_f32 v91, v91, v208\n\t" \
    "v_sub_f32 v27, v27, v208\n\t" \
    "v_mul_f32 v59, v59, v210\n\t" \
    "v_sub_f32 v200, v200, v208\n\t" \
    "v_exp_f32 v200, v200\n\t" \
    "v_sub_f32 v92, v92, v208\n\t" \
    "v_sub_f32 v28, v28, v208\n\t" \
    "v_mul_f32 v60, v60, v210\n\t" \
    "v_sub_f32 v201, v201, v208\n\t" \
    "v_exp_f32 v201, v201\n\t" \
    "v_sub_f32 v93, v93, v208\n\t" \
    "v_sub_f32 v29, v29, v208\n\t" \
    "v_mul_f32 v61, v61, v210\n\t" \
    "v_sub_f32 v202, v202, v208\n\t" \
    "v_exp_f32 v202, v202\n\t" \
    "v_sub_f32 v94, v94, v208\n\t" \
    "v_sub_f32 v30, v30, v208\n\t" \
    "v_mul_f32 v62, v62, v210\n\t" \
    "v_sub_f32 v203, v203, v208\n\t" \
    "v_exp_f32 v203, v203\n\t" \
    "v_sub_f32 v95, v95, v208\n\t" \
    "v_sub_f32 v31, v31, v208\n\t" \
    "v_mul_f32 v63, v63, v210\n\t" \
    "v_mul_f32 v172, v172, v210\n\t" \
    "v_cvt_pk_bf16_f32 v112, v188, v189\n\t" \
    "v_cvt_pk_bf16_f32 v113, v190, v191\n\t" \
    "v_cvt_pk_bf16_f32 v114, v192, v193\n\t" \
    "v_cvt_pk_bf16_f32 v115, v194, v195\n\t" \
    "v_cvt_pk_bf16_f32 v116, v196, v197\n\t" \
    "v_cvt_pk_bf16_f32 v117, v198, v199\n\t" \
    "v_cvt_pk_bf16_f32 v118, v200, v201\n\t" \
    "v_cvt_pk_bf16_f32 v119, v202, v203\n\t" \
    "v_mfma_f32_16x16x32_bf16 v[168:171], v[184:187], v[112:115], v[172:175]\n\t" \
    "s_nop 0\n\t" \
    "v_mfma_f32_16x16x32_bf16 v[168:171], v[184:187], v[116:119], v[168:171]\n\t" \
    "s_nop 15\n\t" \
    "s_branch LretB1v0_%=\n\t" \
    "LrareA2v0_%=:\n\t" \
    "s_nop 15\n\t" \
    "v_mfma_f32_32x32x16_bf16 v[188:203], v[152:155], v[136:139], v[96:111]\n\t" \
    "v_mfma_f32_32x32x16_bf16 v[188:203], v[156:159], v[140:143], v[188:203]\n\t" \
    "s_nop 11\n\t" \
    "v_max3_f32 v208, v188, v189, v190\n\t" \
    "v_max3_f32 v209, v191, v192, v193\n\t" \
    "v_max3_f32 v208, v208, v194, v195\n\t" \
    "v_max3_f32 v209, v209, v196, v197\n\t" \
    "v_max3_f32 v208, v208, v198, v199\n\t" \
    "v_max3_f32 v209, v209, v200, v201\n\t" \
    "v_max3_f32 v208, v208, v202, v203\n\t" \
    "v_max_f32 v208, v208, v209\n\t" \
    "v_mov_b32 v209, v208\n\t" \
    "s_nop 1\n\t" \
    "v_permlane32_swap_b32 v208, v209\n\t" \
    "v_max_f32 v208, v208, v209\n\t" \
    "v_log_f32 v210, v176\n\t" \
    "s_nop 0\n\t" \
    "v_max_f32 v208, v208, v210\n\t" \
    "v_add_f32 v208, s56, v208\n\t" \
    "v_sub_f32 v210, 0, v208\n\t" \
    "v_exp_f32 v210, v210\n\t" \
    "v_sub_f32 v188, v188, v208\n\t" \
    "v_exp_f32 v188, v188\n\t" \
    "v_sub_f32 v96, v96, v208\n\t" \
    "v_sub_f32 v32, v32, v208\n\t" \
    "v_mul_f32 v64, v64, v210\n\t" \
    "v_sub_f32 v189, v189, v208\n\t" \
    "v_exp_f32 v189, v189\n\t" \
    "v_sub_f32 v97, v97, v208\n\t" \
    "v_sub_f32 v33, v33, v208\n\t" \
    "v_mul_f32 v65, v65, v210\n\t" \
    "v_sub_f32 v190, v190, v208\n\t" \
    "v_exp_f32 v190, v190\n\t" \
    "v_sub_f32 v98, v98, v208\n\t" \
    "v_sub_f32 v34, v34, v208\n\t" \
    "v_mul_f32 v66, v66, v210\n\t" \
    "v_sub_f32 v191, v191, v208\n\t" \
    "v_exp_f32 v191, v191\n\t" \
    "v_sub_f32 v99, v99, v208\n\t" \
    "v_sub_f32 v35, v35, v208\n\t" \
    "v_mul_f32 v67, v67, v210\n\t" \
    "v_sub_f32 v192, v192, v208\n\t" \
    "v_exp_f32 v192, v192\n\t" \
    "v_sub_f32 v100, v100, v208\n\t" \
    "v_sub_f32 v36, v36, v208\n\t" \
    "v_mul_f32 v68, v68, v210\n\t" \
    "v_sub_f32 v193, v193, v208\n\t" \
    "v_exp_f32 v193, v193\n\t" \
    "v_sub_f32 v101, v101, v208\n\t" \
    "v_sub_f32 v37, v37, v208\n\t" \
    "v_mul_f32 v69, v69, v210\n\t" \
    "v_sub_f32 v194, v194, v208\n\t" \
    "v_exp_f32 v194, v194\n\t" \
    "v_sub_f32 v102, v102, v208\n\t" \
    "v_sub_f32 v38, v38, v208\n\t" \
    "v_mul_f32 v70, v70, v210\n\t" \
    "v_sub_f32 v195, v195, v208\n\t" \
    "v_exp_f32 v195, v195\n\t" \
    "v_sub_f32 v103, v103, v208\n\t" \
    "v_sub_f32 v39, v39, v208\n\t" \
    "v_mul_f32 v71, v71, v210\n\t" \
    "v_sub_f32 v196, v196, v208\n\t" \
    "v_exp_f32 v196, v196\n\t" \
    "v_sub_f32 v104, v104, v208\n\t" \
    "v_sub_f32 v40, v40, v208\n\t" \
    "v_mul_f32 v72, v72, v210\n\t" \
    "v_sub_f32 v197, v197, v208\n\t" \
    "v_exp_f32 v197, v197\n\t" \
    "v_sub_f32 v105, v105, v208\n\t" \
    "v_sub_f32 v41, v41, v208\n\t" \
    "v_mul_f32 v73, v73, v210\n\t" \
    "v_sub_f32 v198, v198, v208\n\t" \
    "v_exp_f32 v198, v198\n\t" \
    "v_sub_f32 v106, v106, v208\n\t" \
    "v_sub_f32 v42, v42, v208\n\t" \
    "v_mul_f32 v74, v74, v210\n\t" \
    "v_sub_f32 v199, v199, v208\n\t" \
    "v_exp_f32 v199, v199\n\t" \
    "v_sub_f32 v107, v107, v208\n\t" \
    "v_sub_f32 v43, v43, v208\n\t" \
    "v_mul_f32 v75, v75, v210\n\t" \
    "v_sub_f32 v200, v200, v208\n\t" \
    "v_exp_f32 v200, v200\n\t" \
    "v_sub_f32 v108, v108, v208\n\t" \
    "v_sub_f32 v44, v44, v208\n\t" \
    "v_mul_f32 v76, v76, v210\n\t" \
    "v_sub_f32 v201, v201, v208\n\t" \
    "v_exp_f32 v201, v201\n\t" \
    "v_sub_f32 v109, v109, v208\n\t" \
    "v_sub_f32 v45, v45, v208\n\t" \
    "v_mul_f32 v77, v77, v210\n\t" \
    "v_sub_f32 v202, v202, v208\n\t" \
    "v_exp_f32 v202, v202\n\t" \
    "v_sub_f32 v110, v110, v208\n\t" \
    "v_sub_f32 v46, v46, v208\n\t" \
    "v_mul_f32 v78, v78, v210\n\t" \
    "v_sub_f32 v203, v203, v208\n\t" \
    "v_exp_f32 v203, v203\n\t" \
    "v_sub_f32 v111, v111, v208\n\t" \
    "v_sub_f32 v47, v47, v208\n\t" \
    "v_mul_f32 v79, v79, v210\n\t" \
    "v_mul_f32 v176, v176, v210\n\t" \
    "v_cvt_pk_bf16_f32 v120, v188, v189\n\t" \
    "v_cvt_pk_bf16_f32 v121, v190, v191\n\t" \
    "v_cvt_pk_bf16_f32 v122, v192, v193\n\t" \
    "v_cvt_pk_bf16_f32 v123, v194, v195\n\t" \
    "v_cvt_pk_bf16_f32 v124, v196, v197\n\t" \
    "v_cvt_pk_bf16_f32 v125, v198, v199\n\t" \
    "v_cvt_pk_bf16_f32 v126, v200, v201\n\t" \
    "v_cvt_pk_bf16_f32 v127, v202, v203\n\t" \
    "v_mfma_f32_16x16x32_bf16 v[180:183], v[184:187], v[120:123], v[176:179]\n\t" \
    "s_nop 0\n\t" \
    "v_mfma_f32_16x16x32_bf16 v[180:183], v[184:187], v[124:127], v[180:183]\n\t" \
    "s_nop 15\n\t" \
    "s_branch LretA2v0_%=\n\t" \
    "LrareB2v0_%=:\n\t" \
    "s_nop 15\n\t" \
    "v_mfma_f32_32x32x16_bf16 v[188:203], v[144:147], v[128:131], v[80:95]\n\t" \
    "v_mfma_f32_32x32x16_bf16 v[188:203], v[148:151], v[132:135], v[188:203]\n\t" \
    "s_nop 11\n\t" \
    "v_max3_f32 v208, v188, v189, v190\n\t" \
    "v_max3_f32 v209, v191, v192, v193\n\t" \
    "v_max3_f32 v208, v208, v194, v195\n\t" \
    "v_max3_f32 v209, v209, v196, v197\n\t" \
    "v_max3_f32 v208, v208, v198, v199\n\t" \
    "v_max3_f32 v209, v209, v200, v201\n\t" \
    "v_max3_f32 v208, v208, v202, v203\n\t" \
    "v_max_f32 v208, v208, v209\n\t" \
    "v_mov_b32 v209, v208\n\t" \
    "s_nop 1\n\t" \
    "v_permlane32_swap_b32 v208, v209\n\t" \
    "v_max_f32 v208, v208, v209\n\t" \
    "v_log_f32 v210, v168\n\t" \
    "s_nop 0\n\t" \
    "v_max_f32 v208, v208, v210\n\t" \
    "v_add_f32 v208, s56, v208\n\t" \
    "v_sub_f32 v210, 0, v208\n\t" \
    "v_exp_f32 v210, v210\n\t" \
    "v_sub_f32 v188, v188, v208\n\t" \
    "v_exp_f32 v188, v188\n\t" \
    "v_sub_f32 v80, v80, v208\n\t" \
    "v_sub_f32 v16, v16, v208\n\t" \
    "v_mul_f32 v48, v48, v210\n\t" \
    "v_sub_f32 v189, v189, v208\n\t" \
    "v_exp_f32 v189, v189\n\t" \
    "v_sub_f32 v81, v81, v208\n\t" \
    "v_sub_f32 v17, v17, v208\n\t" \
    "v_mul_f32 v49, v49, v210\n\t" \
    "v_sub_f32 v190, v190, v208\n\t" \
    "v_exp_f32 v190, v190\n\t" \
    "v_sub_f32 v82, v82, v208\n\t" \
    "v_sub_f32 v18, v18, v208\n\t" \
    "v_mul_f32 v50, v50, v210\n\t" \
    "v_sub_f32 v191, v191, v208\n\t" \
    "v_exp_f32 v191, v191\n\t" \
    "v_sub_f32 v83, v83, v208\n\t" \
    "v_sub_f32 v19, v19, v208\n\t" \
    "v_mul_f32 v51, v51, v210\n\t" \
    "v_sub_f32 v192, v192, v208\n\t" \
    "v_exp_f32 v192, v192\n\t" \
    "v_sub_f32 v84, v84, v208\n\t" \
    "v_sub_f32 v20, v20, v208\n\t" \
    "v_mul_f32 v52, v52, v210\n\t" \
    "v_sub_f32 v193, v193, v208\n\t" \
    "v_exp_f32 v193, v193\n\t" \
    "v_sub_f32 v85, v85, v208\n\t" \
    "v_sub_f32 v21, v21, v208\n\t" \
    "v_mul_f32 v53, v53, v210\n\t" \
    "v_sub_f32 v194, v194, v208\n\t" \
    "v_exp_f32 v194, v194\n\t" \
    "v_sub_f32 v86, v86, v208\n\t" \
    "v_sub_f32 v22, v22, v208\n\t" \
    "v_mul_f32 v54, v54, v210\n\t" \
    "v_sub_f32 v195, v195, v208\n\t" \
    "v_exp_f32 v195, v195\n\t" \
    "v_sub_f32 v87, v87, v208\n\t" \
    "v_sub_f32 v23, v23, v208\n\t" \
    "v_mul_f32 v55, v55, v210\n\t" \
    "v_sub_f32 v196, v196, v208\n\t" \
    "v_exp_f32 v196, v196\n\t" \
    "v_sub_f32 v88, v88, v208\n\t" \
    "v_sub_f32 v24, v24, v208\n\t" \
    "v_mul_f32 v56, v56, v210\n\t" \
    "v_sub_f32 v197, v197, v208\n\t" \
    "v_exp_f32 v197, v197\n\t" \
    "v_sub_f32 v89, v89, v208\n\t" \
    "v_sub_f32 v25, v25, v208\n\t" \
    "v_mul_f32 v57, v57, v210\n\t" \
    "v_sub_f32 v198, v198, v208\n\t" \
    "v_exp_f32 v198, v198\n\t" \
    "v_sub_f32 v90, v90, v208\n\t" \
    "v_sub_f32 v26, v26, v208\n\t" \
    "v_mul_f32 v58, v58, v210\n\t" \
    "v_sub_f32 v199, v199, v208\n\t" \
    "v_exp_f32 v199, v199\n\t" \
    "v_sub_f32 v91, v91, v208\n\t" \
    "v_sub_f32 v27, v27, v208\n\t" \
    "v_mul_f32 v59, v59, v210\n\t" \
    "v_sub_f32 v200, v200, v208\n\t" \
    "v_exp_f32 v200, v200\n\t" \
    "v_sub_f32 v92, v92, v208\n\t" \
    "v_sub_f32 v28, v28, v208\n\t" \
    "v_mul_f32 v60, v60, v210\n\t" \
    "v_sub_f32 v201, v201, v208\n\t" \
    "v_exp_f32 v201, v201\n\t" \
    "v_sub_f32 v93, v93, v208\n\t" \
    "v_sub_f32 v29, v29, v208\n\t" \
    "v_mul_f32 v61, v61, v210\n\t" \
    "v_sub_f32 v202, v202, v208\n\t" \
    "v_exp_f32 v202, v202\n\t" \
    "v_sub_f32 v94, v94, v208\n\t" \
    "v_sub_f32 v30, v30, v208\n\t" \
    "v_mul_f32 v62, v62, v210\n\t" \
    "v_sub_f32 v203, v203, v208\n\t" \
    "v_exp_f32 v203, v203\n\t" \
    "v_sub_f32 v95, v95, v208\n\t" \
    "v_sub_f32 v31, v31, v208\n\t" \
    "v_mul_f32 v63, v63, v210\n\t" \
    "v_mul_f32 v168, v168, v210\n\t" \
    "v_cvt_pk_bf16_f32 v112, v188, v189\n\t" \
    "v_cvt_pk_bf16_f32 v113, v190, v191\n\t" \
    "v_cvt_pk_bf16_f32 v114, v192, v193\n\t" \
    "v_cvt_pk_bf16_f32 v115, v194, v195\n\t" \
    "v_cvt_pk_bf16_f32 v116, v196, v197\n\t" \
    "v_cvt_pk_bf16_f32 v117, v198, v199\n\t" \
    "v_cvt_pk_bf16_f32 v118, v200, v201\n\t" \
    "v_cvt_pk_bf16_f32 v119, v202, v203\n\t" \
    "v_mfma_f32_16x16x32_bf16 v[172:175], v[184:187], v[112:115], v[168:171]\n\t" \
    "s_nop 0\n\t" \
    "v_mfma_f32_16x16x32_bf16 v[172:175], v[184:187], v[116:119], v[172:175]\n\t" \
    "s_nop 15\n\t" \
    "s_branch LretB2v0_%=\n\t" \
    "LrareA3v0_%=:\n\t" \
    "s_nop 15\n\t" \
    "v_mfma_f32_32x32x16_bf16 v[188:203], v[144:147], v[136:139], v[96:111]\n\t" \
    "v_mfma_f32_32x32x16_bf16 v[188:203], v[148:151], v[140:143], v[188:203]\n\t" \
    "s_nop 11\n\t" \
    "v_max3_f32 v208, v188, v189, v190\n\t" \
    "v_max3_f32 v209, v191, v192, v193\n\t" \
    "v_max3_f32 v208, v208, v194, v195\n\t" \
    "v_max3_f32 v209, v209, v196, v197\n\t" \
    "v_max3_f32 v208, v208, v198, v199\n\t" \
    "v_max3_f32 v209, v209, v200, v201\n\t" \
    "v_max3_f32 v208, v208, v202, v203\n\t" \
    "v_max_f32 v208, v208, v209\n\t" \
    "v_mov_b32 v209, v208\n\t" \
    "s_nop 1\n\t" \
    "v_permlane32_swap_b32 v208, v209\n\t" \
    "v_max_f32 v208, v208, v209\n\t" \
    "v_log_f32 v210, v180\n\t" \
    "s_nop 0\n\t" \
    "v_max_f32 v208, v208, v210\n\t" \
    "v_add_f32 v208, s56, v208\n\t" \
    "v_sub_f32 v210, 0, v208\n\t" \
    "v_exp_f32 v210, v210\n\t" \
    "v_sub_f32 v188, v188, v208\n\t" \
    "v_exp_f32 v188, v188\n\t" \
    "v_sub_f32 v96, v96, v208\n\t" \
    "v_sub_f32 v32, v32, v208\n\t" \
    "v_mul_f32 v64, v64, v210\n\t" \
    "v_sub_f32 v189, v189, v208\n\t" \
    "v_exp_f32 v189, v189\n\t" \
    "v_sub_f32 v97, v97, v208\n\t" \
    "v_sub_f32 v33, v33, v208\n\t" \
    "v_mul_f32 v65, v65, v210\n\t" \
    "v_sub_f32 v190, v190, v208\n\t" \
    "v_exp_f32 v190, v190\n\t" \
    "v_sub_f32 v98, v98, v208\n\t" \
    "v_sub_f32 v34, v34, v208\n\t" \
    "v_mul_f32 v66, v66, v210\n\t" \
    "v_sub_f32 v191, v191, v208\n\t" \
    "v_exp_f32 v191, v191\n\t" \
    "v_sub_f32 v99, v99, v208\n\t" \
    "v_sub_f32 v35, v35, v208\n\t" \
    "v_mul_f32 v67, v67, v210\n\t" \
    "v_sub_f32 v192, v192, v208\n\t" \
    "v_exp_f32 v192, v192\n\t" \
    "v_sub_f32 v100, v100, v208\n\t" \
    "v_sub_f32 v36, v36, v208\n\t" \
    "v_mul_f32 v68, v68, v210\n\t" \
    "v_sub_f32 v193, v193, v208\n\t" \
    "v_exp_f32 v193, v193\n\t" \
    "v_sub_f32 v101, v101, v208\n\t" \
    "v_sub_f32 v37, v37, v208\n\t" \
    "v_mul_f32 v69, v69, v210\n\t" \
    "v_sub_f32 v194, v194, v208\n\t" \
    "v_exp_f32 v194, v194\n\t" \
    "v_sub_f32 v102, v102, v208\n\t" \
    "v_sub_f32 v38, v38, v208\n\t" \
    "v_mul_f32 v70, v70, v210\n\t" \
    "v_sub_f32 v195, v195, v208\n\t" \
    "v_exp_f32 v195, v195\n\t" \
    "v_sub_f32 v103, v103, v208\n\t" \
    "v_sub_f32 v39, v39, v208\n\t" \
    "v_mul_f32 v71, v71, v210\n\t" \
    "v_sub_f32 v196, v196, v208\n\t" \
    "v_exp_f32 v196, v196\n\t" \
    "v_sub_f32 v104, v104, v208\n\t" \
    "v_sub_f32 v40, v40, v208\n\t" \
    "v_mul_f32 v72, v72, v210\n\t" \
    "v_sub_f32 v197, v197, v208\n\t" \
    "v_exp_f32 v197, v197\n\t" \
    "v_sub_f32 v105, v105, v208\n\t" \
    "v_sub_f32 v41, v41, v208\n\t" \
    "v_mul_f32 v73, v73, v210\n\t" \
    "v_sub_f32 v198, v198, v208\n\t" \
    "v_exp_f32 v198, v198\n\t" \
    "v_sub_f32 v106, v106, v208\n\t" \
    "v_sub_f32 v42, v42, v208\n\t" \
    "v_mul_f32 v74, v74, v210\n\t" \
    "v_sub_f32 v199, v199, v208\n\t" \
    "v_exp_f32 v199, v199\n\t" \
    "v_sub_f32 v107, v107, v208\n\t" \
    "v_sub_f32 v43, v43, v208\n\t" \
    "v_mul_f32 v75, v75, v210\n\t" \
    "v_sub_f32 v200, v200, v208\n\t" \
    "v_exp_f32 v200, v200\n\t" \
    "v_sub_f32 v108, v108, v208\n\t" \
    "v_sub_f32 v44, v44, v208\n\t" \
    "v_mul_f32 v76, v76, v210\n\t" \
    "v_sub_f32 v201, v201, v208\n\t" \
    "v_exp_f32 v201, v201\n\t" \
    "v_sub_f32 v109, v109, v208\n\t" \
    "v_sub_f32 v45, v45, v208\n\t" \
    "v_mul_f32 v77, v77, v210\n\t" \
    "v_sub_f32 v202, v202, v208\n\t" \
    "v_exp_f32 v202, v202\n\t" \
    "v_sub_f32 v110, v110, v208\n\t" \
    "v_sub_f32 v46, v46, v208\n\t" \
    "v_mul_f32 v78, v78, v210\n\t" \
    "v_sub_f32 v203, v203, v208\n\t" \
    "v_exp_f32 v203, v203\n\t" \
    "v_sub_f32 v111, v111, v208\n\t" \
    "v_sub_f32 v47, v47, v208\n\t" \
    "v_mul_f32 v79, v79, v210\n\t" \
    "v_mul_f32 v180, v180, v210\n\t" \
    "v_cvt_pk_bf16_f32 v120, v188, v189\n\t" \
    "v_cvt_pk_bf16_f32 v121, v190, v191\n\t" \
    "v_cvt_pk_bf16_f32 v122, v192, v193\n\t" \
    "v_cvt_pk_bf16_f32 v123, v194, v195\n\t" \
    "v_cvt_pk_bf16_f32 v124, v196, v197\n\t" \
    "v_cvt_pk_bf16_f32 v125, v198, v199\n\t" \
    "v_cvt_pk_bf16_f32 v126, v200, v201\n\t" \
    "v_cvt_pk_bf16_f32 v127, v202, v203\n\t" \
    "v_mfma_f32_16x16x32_bf16 v[176:179], v[184:187], v[120:123], v[180:183]\n\t" \
    "s_nop 0\n\t" \
    "v_mfma_f32_16x16x32_bf16 v[176:179], v[184:187], v[124:127], v[176:179]\n\t" \
    "s_nop 15\n\t" \
    "s_branch LretA3v0_%=\n\t" \
    "LrareB3v0_%=:\n\t" \
    "s_nop 15\n\t" \
    "v_mfma_f32_32x32x16_bf16 v[188:203], v[152:155], v[128:131], v[80:95]\n\t" \
    "v_mfma_f32_32x32x16_bf16 v[188:203], v[156:159], v[132:135], v[188:203]\n\t" \
    "s_nop 11\n\t" \
    "v_max3_f32 v208, v188, v189, v190\n\t" \
    "v_max3_f32 v209, v191, v192, v193\n\t" \
    "v_max3_f32 v208, v208, v194, v195\n\t" \
    "v_max3_f32 v209, v209, v196, v197\n\t" \
    "v_max3_f32 v208, v208, v198, v199\n\t" \
    "v_max3_f32 v209, v209, v200, v201\n\t" \
    "v_max3_f32 v208, v208, v202, v203\n\t" \
    "v_max_f32 v208, v208, v209\n\t" \
    "v_mov_b32 v209, v208\n\t" \
    "s_nop 1\n\t" \
    "v_permlane32_swap_b32 v208, v209\n\t" \
    "v_max_f32 v208, v208, v209\n\t" \
    "v_log_f32 v210, v172\n\t" \
    "s_nop 0\n\t" \
    "v_max_f32 v208, v208, v210\n\t" \
    "v_add_f32 v208, s56, v208\n\t" \
    "v_sub_f32 v210, 0, v208\n\t" \
    "v_exp_f32 v210, v210\n\t" \
    "v_sub_f32 v188, v188, v208\n\t" \
    "v_exp_f32 v188, v188\n\t" \
    "v_sub_f32 v80, v80, v208\n\t" \
    "v_sub_f32 v16, v16, v208\n\t" \
    "v_mul_f32 v48, v48, v210\n\t" \
    "v_sub_f32 v189, v189, v208\n\t" \
    "v_exp_f32 v189, v189\n\t" \
    "v_sub_f32 v81, v81, v208\n\t" \
    "v_sub_f32 v17, v17, v208\n\t" \
    "v_mul_f32 v49, v49, v210\n\t" \
    "v_sub_f32 v190, v190, v208\n\t" \
    "v_exp_f32 v190, v190\n\t" \
    "v_sub_f32 v82, v82, v208\n\t" \
    "v_sub_f32 v18, v18, v208\n\t" \
    "v_mul_f32 v50, v50, v210\n\t" \
    "v_sub_f32 v191, v191, v208\n\t" \
    "v_exp_f32 v191, v191\n\t" \
    "v_sub_f32 v83, v83, v208\n\t" \
    "v_sub_f32 v19, v19, v208\n\t" \
    "v_mul_f32 v51, v51, v210\n\t" \
    "v_sub_f32 v192, v192, v208\n\t" \
    "v_exp_f32 v192, v192\n\t" \
    "v_sub_f32 v84, v84, v208\n\t" \
    "v_sub_f32 v20, v20, v208\n\t" \
    "v_mul_f32 v52, v52, v210\n\t" \
    "v_sub_f32 v193, v193, v208\n\t" \
    "v_exp_f32 v193, v193\n\t" \
    "v_sub_f32 v85, v85, v208\n\t" \
    "v_sub_f32 v21, v21, v208\n\t" \
    "v_mul_f32 v53, v53, v210\n\t" \
    "v_sub_f32 v194, v194, v208\n\t" \
    "v_exp_f32 v194, v194\n\t" \
    "v_sub_f32 v86, v86, v208\n\t" \
    "v_sub_f32 v22, v22, v208\n\t" \
    "v_mul_f32 v54, v54, v210\n\t" \
    "v_sub_f32 v195, v195, v208\n\t" \
    "v_exp_f32 v195, v195\n\t" \
    "v_sub_f32 v87, v87, v208\n\t" \
    "v_sub_f32 v23, v23, v208\n\t" \
    "v_mul_f32 v55, v55, v210\n\t" \
    "v_sub_f32 v196, v196, v208\n\t" \
    "v_exp_f32 v196, v196\n\t" \
    "v_sub_f32 v88, v88, v208\n\t" \
    "v_sub_f32 v24, v24, v208\n\t" \
    "v_mul_f32 v56, v56, v210\n\t" \
    "v_sub_f32 v197, v197, v208\n\t" \
    "v_exp_f32 v197, v197\n\t" \
    "v_sub_f32 v89, v89, v208\n\t" \
    "v_sub_f32 v25, v25, v208\n\t" \
    "v_mul_f32 v57, v57, v210\n\t" \
    "v_sub_f32 v198, v198, v208\n\t" \
    "v_exp_f32 v198, v198\n\t" \
    "v_sub_f32 v90, v90, v208\n\t" \
    "v_sub_f32 v26, v26, v208\n\t" \
    "v_mul_f32 v58, v58, v210\n\t" \
    "v_sub_f32 v199, v199, v208\n\t" \
    "v_exp_f32 v199, v199\n\t" \
    "v_sub_f32 v91, v91, v208\n\t" \
    "v_sub_f32 v27, v27, v208\n\t" \
    "v_mul_f32 v59, v59, v210\n\t" \
    "v_sub_f32 v200, v200, v208\n\t" \
    "v_exp_f32 v200, v200\n\t" \
    "v_sub_f32 v92, v92, v208\n\t" \
    "v_sub_f32 v28, v28, v208\n\t" \
    "v_mul_f32 v60, v60, v210\n\t" \
    "v_sub_f32 v201, v201, v208\n\t" \
    "v_exp_f32 v201, v201\n\t" \
    "v_sub_f32 v93, v93, v208\n\t" \
    "v_sub_f32 v29, v29, v208\n\t" \
    "v_mul_f32 v61, v61, v210\n\t" \
    "v_sub_f32 v202, v202, v208\n\t" \
    "v_exp_f32 v202, v202\n\t" \
    "v_sub_f32 v94, v94, v208\n\t" \
    "v_sub_f32 v30, v30, v208\n\t" \
    "v_mul_f32 v62, v62, v210\n\t" \
    "v_sub_f32 v203, v203, v208\n\t" \
    "v_exp_f32 v203, v203\n\t" \
    "v_sub_f32 v95, v95, v208\n\t" \
    "v_sub_f32 v31, v31, v208\n\t" \
    "v_mul_f32 v63, v63, v210\n\t" \
    "v_mul_f32 v172, v172, v210\n\t" \
    "v_cvt_pk_bf16_f32 v112, v188, v189\n\t" \
    "v_cvt_pk_bf16_f32 v113, v190, v191\n\t" \
    "v_cvt_pk_bf16_f32 v114, v192, v193\n\t" \
    "v_cvt_pk_bf16_f32 v115, v194, v195\n\t" \
    "v_cvt_pk_bf16_f32 v116, v196, v197\n\t" \
    "v_cvt_pk_bf16_f32 v117, v198, v199\n\t" \
    "v_cvt_pk_bf16_f32 v118, v200, v201\n\t" \
    "v_cvt_pk_bf16_f32 v119, v202, v203\n\t" \
    "v_mfma_f32_16x16x32_bf16 v[168:171], v[184:187], v[112:115], v[172:175]\n\t" \
    "s_nop 0\n\t" \
    "v_mfma_f32_16x16x32_bf16 v[168:171], v[184:187], v[116:119], v[168:171]\n\t" \
    "s_nop 15\n\t" \
    "s_branch LretB3v0_%=\n\t" \
    "LrareA0v1_%=:\n\t" \
    "s_nop 15\n\t" \
    "v_mfma_f32_32x32x16_bf16 v[188:203], v[152:155], v[136:139], v[96:111]\n\t" \
    "v_mfma_f32_32x32x16_bf16 v[188:203], v[156:159], v[140:143], v[188:203]\n\t" \
    "s_nop 11\n\t" \
    "v_max3_f32 v208, v188, v189, v190\n\t" \
    "v_max3_f32 v209, v191, v192, v193\n\t" \
    "v_max3_f32 v208, v208, v194, v195\n\t" \
    "v_max3_f32 v209, v209, v196, v197\n\t" \
    "v_max3_f32 v208, v208, v198, v199\n\t" \
    "v_max3_f32 v209, v209, v200, v201\n\t" \
    "v_max3_f32 v208, v208, v202, v203\n\t" \
    "v_max_f32 v208, v208, v209\n\t" \
    "v_mov_b32 v209, v208\n\t" \
    "s_nop 1\n\t" \
    "v_permlane32_swap_b32 v208, v209\n\t" \
    "v_max_f32 v208, v208, v209\n\t" \
    "v_log_f32 v210, v176\n\t" \
    "s_nop 0\n\t" \
    "v_max_f32 v208, v208, v210\n\t" \
    "v_add_f32 v208, s56, v208\n\t" \
    "v_sub_f32 v210, 0, v208\n\t" \
    "v_exp_f32 v210, v210\n\t" \
    "v_sub_f32 v188, v188, v208\n\t" \
    "v_exp_f32 v188, v188\n\t" \
    "v_sub_f32 v96, v96, v208\n\t" \
    "v_sub_f32 v32, v32, v208\n\t" \
    "v_mul_f32 v64, v64, v210\n\t" \
    "v_sub_f32 v189, v189, v208\n\t" \
    "v_exp_f32 v189, v189\n\t" \
    "v_sub_f32 v97, v97, v208\n\t" \
    "v_sub_f32 v33, v33, v208\n\t" \
    "v_mul_f32 v65, v65, v210\n\t" \
    "v_sub_f32 v190, v190, v208\n\t" \
    "v_exp_f32 v190, v190\n\t" \
    "v_sub_f32 v98, v98, v208\n\t" \
    "v_sub_f32 v34, v34, v208\n\t" \
    "v_mul_f32 v66, v66, v210\n\t" \
    "v_sub_f32 v191, v191, v208\n\t" \
    "v_exp_f32 v191, v191\n\t" \
    "v_sub_f32 v99, v99, v208\n\t" \
    "v_sub_f32 v35, v35, v208\n\t" \
    "v_mul_f32 v67, v67, v210\n\t" \
    "v_sub_f32 v192, v192, v208\n\t" \
    "v_exp_f32 v192, v192\n\t" \
    "v_sub_f32 v100, v100, v208\n\t" \
    "v_sub_f32 v36, v36, v208\n\t" \
    "v_mul_f32 v68, v68, v210\n\t" \
    "v_sub_f32 v193, v193, v208\n\t" \
    "v_exp_f32 v193, v193\n\t" \
    "v_sub_f32 v101, v101, v208\n\t" \
    "v_sub_f32 v37, v37, v208\n\t" \
    "v_mul_f32 v69, v69, v210\n\t" \
    "v_sub_f32 v194, v194, v208\n\t" \
    "v_exp_f32 v194, v194\n\t" \
    "v_sub_f32 v102, v102, v208\n\t" \
    "v_sub_f32 v38, v38, v208\n\t" \
    "v_mul_f32 v70, v70, v210\n\t" \
    "v_sub_f32 v195, v195, v208\n\t" \
    "v_exp_f32 v195, v195\n\t" \
    "v_sub_f32 v103, v103, v208\n\t" \
    "v_sub_f32 v39, v39, v208\n\t" \
    "v_mul_f32 v71, v71, v210\n\t" \
    "v_sub_f32 v196, v196, v208\n\t" \
    "v_exp_f32 v196, v196\n\t" \
    "v_sub_f32 v104, v104, v208\n\t" \
    "v_sub_f32 v40, v40, v208\n\t" \
    "v_mul_f32 v72, v72, v210\n\t" \
    "v_sub_f32 v197, v197, v208\n\t" \
    "v_exp_f32 v197, v197\n\t" \
    "v_sub_f32 v105, v105, v208\n\t" \
    "v_sub_f32 v41, v41, v208\n\t" \
    "v_mul_f32 v73, v73, v210\n\t" \
    "v_sub_f32 v198, v198, v208\n\t" \
    "v_exp_f32 v198, v198\n\t" \
    "v_sub_f32 v106, v106, v208\n\t" \
    "v_sub_f32 v42, v42, v208\n\t" \
    "v_mul_f32 v74, v74, v210\n\t" \
    "v_sub_f32 v199, v199, v208\n\t" \
    "v_exp_f32 v199, v199\n\t" \
    "v_sub_f32 v107, v107, v208\n\t" \
    "v_sub_f32 v43, v43, v208\n\t" \
    "v_mul_f32 v75, v75, v210\n\t" \
    "v_sub_f32 v200, v200, v208\n\t" \
    "v_exp_f32 v200, v200\n\t" \
    "v_sub_f32 v108, v108, v208\n\t" \
    "v_sub_f32 v44, v44, v208\n\t" \
    "v_mul_f32 v76, v76, v210\n\t" \
    "v_sub_f32 v201, v201, v208\n\t" \
    "v_exp_f32 v201, v201\n\t" \
    "v_sub_f32 v109, v109, v208\n\t" \
    "v_sub_f32 v45, v45, v208\n\t" \
    "v_mul_f32 v77, v77, v210\n\t" \
    "v_sub_f32 v202, v202, v208\n\t" \
    "v_exp_f32 v202, v202\n\t" \
    "v_sub_f32 v110, v110, v208\n\t" \
    "v_sub_f32 v46, v46, v208\n\t" \
    "v_mul_f32 v78, v78, v210\n\t" \
    "v_sub_f32 v203, v203, v208\n\t" \
    "v_exp_f32 v203, v203\n\t" \
    "v_sub_f32 v111, v111, v208\n\t" \
    "v_sub_f32 v47, v47, v208\n\t" \
    "v_mul_f32 v79, v79, v210\n\t" \
    "v_mul_f32 v176, v176, v210\n\t" \
    "v_cvt_pk_bf16_f32 v120, v188, v189\n\t" \
    "v_cvt_pk_bf16_f32 v121, v190, v191\n\t" \
    "v_cvt_pk_bf16_f32 v122, v192, v193\n\t" \
    "v_cvt_pk_bf16_f32 v123, v194, v195\n\t" \
    "v_cvt_pk_bf16_f32 v124, v196, v197\n\t" \
    "v_cvt_pk_bf16_f32 v125, v198, v199\n\t" \
    "v_cvt_pk_bf16_f32 v126, v200, v201\n\t" \
    "v_cvt_pk_bf16_f32 v127, v202, v203\n\t" \
    "v_mfma_f32_16x16x32_bf16 v[180:183], v[184:187], v[120:123], v[176:179]\n\t" \
    "s_nop 0\n\t" \
    "v_mfma_f32_16x16x32_bf16 v[180:183], v[184:187], v[124:127], v[180:183]\n\t" \
    "s_nop 15\n\t" \
    "s_branch LretA0v1_%=\n\t" \
    "LrareB0v1_%=:\n\t" \
    "s_nop 15\n\t" \
    "v_mfma_f32_32x32x16_bf16 v[188:203], v[144:147], v[128:131], v[80:95]\n\t" \
    "v_mfma_f32_32x32x16_bf16 v[188:203], v[148:151], v[132:135], v[188:203]\n\t" \
    "s_nop 11\n\t" \
    "v_max3_f32 v208, v188, v189, v190\n\t" \
    "v_max3_f32 v209, v191, v192, v193\n\t" \
    "v_max3_f32 v208, v208, v194, v195\n\t" \
    "v_max3_f32 v209, v209, v196, v197\n\t" \
    "v_max3_f32 v208, v208, v198, v199\n\t" \
    "v_max3_f32 v209, v209, v200, v201\n\t" \
    "v_max3_f32 v208, v208, v202, v203\n\t" \
    "v_max_f32 v208, v208, v209\n\t" \
    "v_mov_b32 v209, v208\n\t" \
    "s_nop 1\n\t" \
    "v_permlane32_swap_b32 v208, v209\n\t" \
    "v_max_f32 v208, v208, v209\n\t" \
    "v_log_f32 v210, v168\n\t" \
    "s_nop 0\n\t" \
    "v_max_f32 v208, v208, v210\n\t" \
    "v_add_f32 v208, s56, v208\n\t" \
    "v_sub_f32 v210, 0, v208\n\t" \
    "v_exp_f32 v210, v210\n\t" \
    "v_sub_f32 v188, v188, v208\n\t" \
    "v_exp_f32 v188, v188\n\t" \
    "v_sub_f32 v80, v80, v208\n\t" \
    "v_sub_f32 v16, v16, v208\n\t" \
    "v_mul_f32 v48, v48, v210\n\t" \
    "v_sub_f32 v189, v189, v208\n\t" \
    "v_exp_f32 v189, v189\n\t" \
    "v_sub_f32 v81, v81, v208\n\t" \
    "v_sub_f32 v17, v17, v208\n\t" \
    "v_mul_f32 v49, v49, v210\n\t" \
    "v_sub_f32 v190, v190, v208\n\t" \
    "v_exp_f32 v190, v190\n\t" \
    "v_sub_f32 v82, v82, v208\n\t" \
    "v_sub_f32 v18, v18, v208\n\t" \
    "v_mul_f32 v50, v50, v210\n\t" \
    "v_sub_f32 v191, v191, v208\n\t" \
    "v_exp_f32 v191, v191\n\t" \
    "v_sub_f32 v83, v83, v208\n\t" \
    "v_sub_f32 v19, v19, v208\n\t" \
    "v_mul_f32 v51, v51, v210\n\t" \
    "v_sub_f32 v192, v192, v208\n\t" \
    "v_exp_f32 v192, v192\n\t" \
    "v_sub_f32 v84, v84, v208\n\t" \
    "v_sub_f32 v20, v20, v208\n\t" \
    "v_mul_f32 v52, v52, v210\n\t" \
    "v_sub_f32 v193, v193, v208\n\t" \
    "v_exp_f32 v193, v193\n\t" \
    "v_sub_f32 v85, v85, v208\n\t" \
    "v_sub_f32 v21, v21, v208\n\t" \
    "v_mul_f32 v53, v53, v210\n\t" \
    "v_sub_f32 v194, v194, v208\n\t" \
    "v_exp_f32 v194, v194\n\t" \
    "v_sub_f32 v86, v86, v208\n\t" \
    "v_sub_f32 v22, v22, v208\n\t" \
    "v_mul_f32 v54, v54, v210\n\t" \
    "v_sub_f32 v195, v195, v208\n\t" \
    "v_exp_f32 v195, v195\n\t" \
    "v_sub_f32 v87, v87, v208\n\t" \
    "v_sub_f32 v23, v23, v208\n\t" \
    "v_mul_f32 v55, v55, v210\n\t" \
    "v_sub_f32 v196, v196, v208\n\t" \
    "v_exp_f32 v196, v196\n\t" \
    "v_sub_f32 v88, v88, v208\n\t" \
    "v_sub_f32 v24, v24, v208\n\t" \
    "v_mul_f32 v56, v56, v210\n\t" \
    "v_sub_f32 v197, v197, v208\n\t" \
    "v_exp_f32 v197, v197\n\t" \
    "v_sub_f32 v89, v89, v208\n\t" \
    "v_sub_f32 v25, v25, v208\n\t" \
    "v_mul_f32 v57, v57, v210\n\t" \
    "v_sub_f32 v198, v198, v208\n\t" \
    "v_exp_f32 v198, v198\n\t" \
    "v_sub_f32 v90, v90, v208\n\t" \
    "v_sub_f32 v26, v26, v208\n\t" \
    "v_mul_f32 v58, v58, v210\n\t" \
    "v_sub_f32 v199, v199, v208\n\t" \
    "v_exp_f32 v199, v199\n\t" \
    "v_sub_f32 v91, v91, v208\n\t" \
    "v_sub_f32 v27, v27, v208\n\t" \
    "v_mul_f32 v59, v59, v210\n\t" \
    "v_sub_f32 v200, v200, v208\n\t" \
    "v_exp_f32 v200, v200\n\t" \
    "v_sub_f32 v92, v92, v208\n\t" \
    "v_sub_f32 v28, v28, v208\n\t" \
    "v_mul_f32 v60, v60, v210\n\t" \
    "v_sub_f32 v201, v201, v208\n\t" \
    "v_exp_f32 v201, v201\n\t" \
    "v_sub_f32 v93, v93, v208\n\t" \
    "v_sub_f32 v29, v29, v208\n\t" \
    "v_mul_f32 v61, v61, v210\n\t" \
    "v_sub_f32 v202, v202, v208\n\t" \
    "v_exp_f32 v202, v202\n\t" \
    "v_sub_f32 v94, v94, v208\n\t" \
    "v_sub_f32 v30, v30, v208\n\t" \
    "v_mul_f32 v62, v62, v210\n\t" \
    "v_sub_f32 v203, v203, v208\n\t" \
    "v_exp_f32 v203, v203\n\t" \
    "v_sub_f32 v95, v95, v208\n\t" \
    "v_sub_f32 v31, v31, v208\n\t" \
    "v_mul_f32 v63, v63, v210\n\t" \
    "v_mul_f32 v168, v168, v210\n\t" \
    "v_cvt_pk_bf16_f32 v112, v188, v189\n\t" \
    "v_cvt_pk_bf16_f32 v113, v190, v191\n\t" \
    "v_cvt_pk_bf16_f32 v114, v192, v193\n\t" \
    "v_cvt_pk_bf16_f32 v115, v194, v195\n\t" \
    "v_cvt_pk_bf16_f32 v116, v196, v197\n\t" \
    "v_cvt_pk_bf16_f32 v117, v198, v199\n\t" \
    "v_cvt_pk_bf16_f32 v118, v200, v201\n\t" \
    "v_cvt_pk_bf16_f32 v119, v202, v203\n\t" \
    "v_mfma_f32_16x16x32_bf16 v[172:175], v[184:187], v[112:115], v[168:171]\n\t" \
    "s_nop 0\n\t" \
    "v_mfma_f32_16x16x32_bf16 v[172:175], v[184:187], v[116:119], v[172:175]\n\t" \
    "s_nop 15\n\t" \
    "s_branch LretB0v1_%=\n\t" \
    "LrareA1v1_%=:\n\t" \
    "s_nop 15\n\t" \
    "v_mfma_f32_32x32x16_bf16 v[188:203], v[144:147], v[136:139], v[96:111]\n\t" \
    "v_mfma_f32_32x32x16_bf16 v[188:203], v[148:151], v[140:143], v[188:203]\n\t" \
    "s_nop 11\n\t" \
    "v_max3_f32 v208, v188, v189, v190\n\t" \
    "v_max3_f32 v209, v191, v192, v193\n\t" \
    "v_max3_f32 v208, v208, v194, v195\n\t" \
    "v_max3_f32 v209, v209, v196, v197\n\t" \
    "v_max3_f32 v208, v208, v198, v199\n\t" \
    "v_max3_f32 v209, v209, v200, v201\n\t" \
    "v_max3_f32 v208, v208, v202, v203\n\t" \
    "v_max_f32 v208, v208, v209\n\t" \
    "v_mov_b32 v209, v208\n\t" \
    "s_nop 1\n\t" \
    "v_permlane32_swap_b32 v208, v209\n\t" \
    "v_max_f32 v208, v208, v209\n\t" \
    "v_log_f32 v210, v180\n\t" \
    "s_nop 0\n\t" \
    "v_max_f32 v208, v208, v210\n\t" \
    "v_add_f32 v208, s56, v208\n\t" \
    "v_sub_f32 v210, 0, v208\n\t" \
    "v_exp_f32 v210, v210\n\t" \
    "v_sub_f32 v188, v188, v208\n\t" \
    "v_exp_f32 v188, v188\n\t" \
    "v_sub_f32 v96, v96, v208\n\t" \
    "v_sub_f32 v32, v32, v208\n\t" \
    "v_mul_f32 v64, v64, v210\n\t" \
    "v_sub_f32 v189, v189, v208\n\t" \
    "v_exp_f32 v189, v189\n\t" \
    "v_sub_f32 v97, v97, v208\n\t" \
    "v_sub_f32 v33, v33, v208\n\t" \
    "v_mul_f32 v65, v65, v210\n\t" \
    "v_sub_f32 v190, v190, v208\n\t" \
    "v_exp_f32 v190, v190\n\t" \
    "v_sub_f32 v98, v98, v208\n\t" \
    "v_sub_f32 v34, v34, v208\n\t" \
    "v_mul_f32 v66, v66, v210\n\t" \
    "v_sub_f32 v191, v191, v208\n\t" \
    "v_exp_f32 v191, v191\n\t" \
    "v_sub_f32 v99, v99, v208\n\t" \
    "v_sub_f32 v35, v35, v208\n\t" \
    "v_mul_f32 v67, v67, v210\n\t" \
    "v_sub_f32 v192, v192, v208\n\t" \
    "v_exp_f32 v192, v192\n\t" \
    "v_sub_f32 v100, v100, v208\n\t" \
    "v_sub_f32 v36, v36, v208\n\t" \
    "v_mul_f32 v68, v68, v210\n\t" \
    "v_sub_f32 v193, v193, v208\n\t" \
    "v_exp_f32 v193, v193\n\t" \
    "v_sub_f32 v101, v101, v208\n\t" \
    "v_sub_f32 v37, v37, v208\n\t" \
    "v_mul_f32 v69, v69, v210\n\t" \
    "v_sub_f32 v194, v194, v208\n\t" \
    "v_exp_f32 v194, v194\n\t" \
    "v_sub_f32 v102, v102, v208\n\t" \
    "v_sub_f32 v38, v38, v208\n\t" \
    "v_mul_f32 v70, v70, v210\n\t" \
    "v_sub_f32 v195, v195, v208\n\t" \
    "v_exp_f32 v195, v195\n\t" \
    "v_sub_f32 v103, v103, v208\n\t" \
    "v_sub_f32 v39, v39, v208\n\t" \
    "v_mul_f32 v71, v71, v210\n\t" \
    "v_sub_f32 v196, v196, v208\n\t" \
    "v_exp_f32 v196, v196\n\t" \
    "v_sub_f32 v104, v104, v208\n\t" \
    "v_sub_f32 v40, v40, v208\n\t" \
    "v_mul_f32 v72, v72, v210\n\t" \
    "v_sub_f32 v197, v197, v208\n\t" \
    "v_exp_f32 v197, v197\n\t" \
    "v_sub_f32 v105, v105, v208\n\t" \
    "v_sub_f32 v41, v41, v208\n\t" \
    "v_mul_f32 v73, v73, v210\n\t" \
    "v_sub_f32 v198, v198, v208\n\t" \
    "v_exp_f32 v198, v198\n\t" \
    "v_sub_f32 v106, v106, v208\n\t" \
    "v_sub_f32 v42, v42, v208\n\t" \
    "v_mul_f32 v74, v74, v210\n\t" \
    "v_sub_f32 v199, v199, v208\n\t" \
    "v_exp_f32 v199, v199\n\t" \
    "v_sub_f32 v107, v107, v208\n\t" \
    "v_sub_f32 v43, v43, v208\n\t" \
    "v_mul_f32 v75, v75, v210\n\t" \
    "v_sub_f32 v200, v200, v208\n\t" \
    "v_exp_f32 v200, v200\n\t" \
    "v_sub_f32 v108, v108, v208\n\t" \
    "v_sub_f32 v44, v44, v208\n\t" \
    "v_mul_f32 v76, v76, v210\n\t" \
    "v_sub_f32 v201, v201, v208\n\t" \
    "v_exp_f32 v201, v201\n\t" \
    "v_sub_f32 v109, v109, v208\n\t" \
    "v_sub_f32 v45, v45, v208\n\t" \
    "v_mul_f32 v77, v77, v210\n\t" \
    "v_sub_f32 v202, v202, v208\n\t" \
    "v_exp_f32 v202, v202\n\t" \
    "v_sub_f32 v110, v110, v208\n\t" \
    "v_sub_f32 v46, v46, v208\n\t" \
    "v_mul_f32 v78, v78, v210\n\t" \
    "v_sub_f32 v203, v203, v208\n\t" \
    "v_exp_f32 v203, v203\n\t" \
    "v_sub_f32 v111, v111, v208\n\t" \
    "v_sub_f32 v47, v47, v208\n\t" \
    "v_mul_f32 v79, v79, v210\n\t" \
    "v_mul_f32 v180, v180, v210\n\t" \
    "v_cvt_pk_bf16_f32 v120, v188, v189\n\t" \
    "v_cvt_pk_bf16_f32 v121, v190, v191\n\t" \
    "v_cvt_pk_bf16_f32 v122, v192, v193\n\t" \
    "v_cvt_pk_bf16_f32 v123, v194, v195\n\t" \
    "v_cvt_pk_bf16_f32 v124, v196, v197\n\t" \
    "v_cvt_pk_bf16_f32 v125, v198, v199\n\t" \
    "v_cvt_pk_bf16_f32 v126, v200, v201\n\t" \
    "v_cvt_pk_bf16_f32 v127, v202, v203\n\t" \
    "v_mfma_f32_16x16x32_bf16 v[176:179], v[184:187], v[120:123], v[180:183]\n\t" \
    "s_nop 0\n\t" \
    "v_mfma_f32_16x16x32_bf16 v[176:179], v[184:187], v[124:127], v[176:179]\n\t" \
    "s_nop 15\n\t" \
    "s_branch LretA1v1_%=\n\t" \
    "LrareB1v1_%=:\n\t" \
    "s_nop 15\n\t" \
    "v_mfma_f32_32x32x16_bf16 v[188:203], v[152:155], v[128:131], v[80:95]\n\t" \
    "v_mfma_f32_32x32x16_bf16 v[188:203], v[156:159], v[132:135], v[188:203]\n\t" \
    "s_nop 11\n\t" \
    "v_max3_f32 v208, v188, v189, v190\n\t" \
    "v_max3_f32 v209, v191, v192, v193\n\t" \
    "v_max3_f32 v208, v208, v194, v195\n\t" \
    "v_max3_f32 v209, v209, v196, v197\n\t" \
    "v_max3_f32 v208, v208, v198, v199\n\t" \
    "v_max3_f32 v209, v209, v200, v201\n\t" \
    "v_max3_f32 v208, v208, v202, v203\n\t" \
    "v_max_f32 v208, v208, v209\n\t" \
    "v_mov_b32 v209, v208\n\t" \
    "s_nop 1\n\t" \
    "v_permlane32_swap_b32 v208, v209\n\t" \
    "v_max_f32 v208, v208, v209\n\t" \
    "v_log_f32 v210, v172\n\t" \
    "s_nop 0\n\t" \
    "v_max_f32 v208, v208, v210\n\t" \
    "v_add_f32 v208, s56, v208\n\t" \
    "v_sub_f32 v210, 0, v208\n\t" \
    "v_exp_f32 v210, v210\n\t" \
    "v_sub_f32 v188, v188, v208\n\t" \
    "v_exp_f32 v188, v188\n\t" \
    "v_sub_f32 v80, v80, v208\n\t" \
    "v_sub_f32 v16, v16, v208\n\t" \
    "v_mul_f32 v48, v48, v210\n\t" \
    "v_sub_f32 v189, v189, v208\n\t" \
    "v_exp_f32 v189, v189\n\t" \
    "v_sub_f32 v81, v81, v208\n\t" \
    "v_sub_f32 v17, v17, v208\n\t" \
    "v_mul_f32 v49, v49, v210\n\t" \
    "v_sub_f32 v190, v190, v208\n\t" \
    "v_exp_f32 v190, v190\n\t" \
    "v_sub_f32 v82, v82, v208\n\t" \
    "v_sub_f32 v18, v18, v208\n\t" \
    "v_mul_f32 v50, v50, v210\n\t" \
    "v_sub_f32 v191, v191, v208\n\t" \
    "v_exp_f32 v191, v191\n\t" \
    "v_sub_f32 v83, v83, v208\n\t" \
    "v_sub_f32 v19, v19, v208\n\t" \
    "v_mul_f32 v51, v51, v210\n\t" \
    "v_sub_f32 v192, v192, v208\n\t" \
    "v_exp_f32 v192, v192\n\t" \
    "v_sub_f32 v84, v84, v208\n\t" \
    "v_sub_f32 v20, v20, v208\n\t" \
    "v_mul_f32 v52, v52, v210\n\t" \
    "v_sub_f32 v193, v193, v208\n\t" \
    "v_exp_f32 v193, v193\n\t" \
    "v_sub_f32 v85, v85, v208\n\t" \
    "v_sub_f32 v21, v21, v208\n\t" \
    "v_mul_f32 v53, v53, v210\n\t" \
    "v_sub_f32 v194, v194, v208\n\t" \
    "v_exp_f32 v194, v194\n\t" \
    "v_sub_f32 v86, v86, v208\n\t" \
    "v_sub_f32 v22, v22, v208\n\t" \
    "v_mul_f32 v54, v54, v210\n\t" \
    "v_sub_f32 v195, v195, v208\n\t" \
    "v_exp_f32 v195, v195\n\t" \
    "v_sub_f32 v87, v87, v208\n\t" \
    "v_sub_f32 v23, v23, v208\n\t" \
    "v_mul_f32 v55, v55, v210\n\t" \
    "v_sub_f32 v196, v196, v208\n\t" \
    "v_exp_f32 v196, v196\n\t" \
    "v_sub_f32 v88, v88, v208\n\t" \
    "v_sub_f32 v24, v24, v208\n\t" \
    "v_mul_f32 v56, v56, v210\n\t" \
    "v_sub_f32 v197, v197, v208\n\t" \
    "v_exp_f32 v197, v197\n\t" \
    "v_sub_f32 v89, v89, v208\n\t" \
    "v_sub_f32 v25, v25, v208\n\t" \
    "v_mul_f32 v57, v57, v210\n\t" \
    "v_sub_f32 v198, v198, v208\n\t" \
    "v_exp_f32 v198, v198\n\t" \
    "v_sub_f32 v90, v90, v208\n\t" \
    "v_sub_f32 v26, v26, v208\n\t" \
    "v_mul_f32 v58, v58, v210\n\t" \
    "v_sub_f32 v199, v199, v208\n\t" \
    "v_exp_f32 v199, v199\n\t" \
    "v_sub_f32 v91, v91, v208\n\t" \
    "v_sub_f32 v27, v27, v208\n\t" \
    "v_mul_f32 v59, v59, v210\n\t" \
    "v_sub_f32 v200, v200, v208\n\t" \
    "v_exp_f32 v200, v200\n\t" \
    "v_sub_f32 v92, v92, v208\n\t" \
    "v_sub_f32 v28, v28, v208\n\t" \
    "v_mul_f32 v60, v60, v210\n\t" \
    "v_sub_f32 v201, v201, v208\n\t" \
    "v_exp_f32 v201, v201\n\t" \
    "v_sub_f32 v93, v93, v208\n\t" \
    "v_sub_f32 v29, v29, v208\n\t" \
    "v_mul_f32 v61, v61, v210\n\t" \
    "v_sub_f32 v202, v202, v208\n\t" \
    "v_exp_f32 v202, v202\n\t" \
    "v_sub_f32 v94, v94, v208\n\t" \
    "v_sub_f32 v30, v30, v208\n\t" \
    "v_mul_f32 v62, v62, v210\n\t" \
    "v_sub_f32 v203, v203, v208\n\t" \
    "v_exp_f32 v203, v203\n\t" \
    "v_sub_f32 v95, v95, v208\n\t" \
    "v_sub_f32 v31, v31, v208\n\t" \
    "v_mul_f32 v63, v63, v210\n\t" \
    "v_mul_f32 v172, v172, v210\n\t" \
    "v_cvt_pk_bf16_f32 v112, v188, v189\n\t" \
    "v_cvt_pk_bf16_f32 v113, v190, v191\n\t" \
    "v_cvt_pk_bf16_f32 v114, v192, v193\n\t" \
    "v_cvt_pk_bf16_f32 v115, v194, v195\n\t" \
    "v_cvt_pk_bf16_f32 v116, v196, v197\n\t" \
    "v_cvt_pk_bf16_f32 v117, v198, v199\n\t" \
    "v_cvt_pk_bf16_f32 v118, v200, v201\n\t" \
    "v_cvt_pk_bf16_f32 v119, v202, v203\n\t" \
    "v_mfma_f32_16x16x32_bf16 v[168:171], v[184:187], v[112:115], v[172:175]\n\t" \
    "s_nop 0\n\t" \
    "v_mfma_f32_16x16x32_bf16 v[168:171], v[184:187], v[116:119], v[168:171]\n\t" \
    "s_nop 15\n\t" \
    "s_branch LretB1v1_%=\n\t" \
    "LrareA2v1_%=:\n\t" \
    "s_nop 15\n\t" \
    "v_mfma_f32_32x32x16_bf16 v[188:203], v[152:155], v[136:139], v[96:111]\n\t" \
    "v_mfma_f32_32x32x16_bf16 v[188:203], v[156:159], v[140:143], v[188:203]\n\t" \
    "s_nop 11\n\t" \
    "v_max3_f32 v208, v188, v189, v190\n\t" \
    "v_max3_f32 v209, v191, v192, v193\n\t" \
    "v_max3_f32 v208, v208, v194, v195\n\t" \
    "v_max3_f32 v209, v209, v196, v197\n\t" \
    "v_max3_f32 v208, v208, v198, v199\n\t" \
    "v_max3_f32 v209, v209, v200, v201\n\t" \
    "v_max3_f32 v208, v208, v202, v203\n\t" \
    "v_max_f32 v208, v208, v209\n\t" \
    "v_mov_b32 v209, v208\n\t" \
    "s_nop 1\n\t" \
    "v_permlane32_swap_b32 v208, v209\n\t" \
    "v_max_f32 v208, v208, v209\n\t" \
    "v_log_f32 v210, v176\n\t" \
    "s_nop 0\n\t" \
    "v_max_f32 v208, v208, v210\n\t" \
    "v_add_f32 v208, s56, v208\n\t" \
    "v_sub_f32 v210, 0, v208\n\t" \
    "v_exp_f32 v210, v210\n\t" \
    "v_sub_f32 v188, v188, v208\n\t" \
    "v_exp_f32 v188, v188\n\t" \
    "v_sub_f32 v96, v96, v208\n\t" \
    "v_sub_f32 v32, v32, v208\n\t" \
    "v_mul_f32 v64, v64, v210\n\t" \
    "v_sub_f32 v189, v189, v208\n\t" \
    "v_exp_f32 v189, v189\n\t" \
    "v_sub_f32 v97, v97, v208\n\t" \
    "v_sub_f32 v33, v33, v208\n\t" \
    "v_mul_f32 v65, v65, v210\n\t" \
    "v_sub_f32 v190, v190, v208\n\t" \
    "v_exp_f32 v190, v190\n\t" \
    "v_sub_f32 v98, v98, v208\n\t" \
    "v_sub_f32 v34, v34, v208\n\t" \
    "v_mul_f32 v66, v66, v210\n\t" \
    "v_sub_f32 v191, v191, v208\n\t" \
    "v_exp_f32 v191, v191\n\t" \
    "v_sub_f32 v99, v99, v208\n\t" \
    "v_sub_f32 v35, v35, v208\n\t" \
    "v_mul_f32 v67, v67, v210\n\t" \
    "v_sub_f32 v192, v192, v208\n\t" \
    "v_exp_f32 v192, v192\n\t" \
    "v_sub_f32 v100, v100, v208\n\t" \
    "v_sub_f32 v36, v36, v208\n\t" \
    "v_mul_f32 v68, v68, v210\n\t" \
    "v_sub_f32 v193, v193, v208\n\t" \
    "v_exp_f32 v193, v193\n\t" \
    "v_sub_f32 v101, v101, v208\n\t" \
    "v_sub_f32 v37, v37, v208\n\t" \
    "v_mul_f32 v69, v69, v210\n\t" \
    "v_sub_f32 v194, v194, v208\n\t" \
    "v_exp_f32 v194, v194\n\t" \
    "v_sub_f32 v102, v102, v208\n\t" \
    "v_sub_f32 v38, v38, v208\n\t" \
    "v_mul_f32 v70, v70, v210\n\t" \
    "v_sub_f32 v195, v195, v208\n\t" \
    "v_exp_f32 v195, v195\n\t" \
    "v_sub_f32 v103, v103, v208\n\t" \
    "v_sub_f32 v39, v39, v208\n\t" \
    "v_mul_f32 v71, v71, v210\n\t" \
    "v_sub_f32 v196, v196, v208\n\t" \
    "v_exp_f32 v196, v196\n\t" \
    "v_sub_f32 v104, v104, v208\n\t" \
    "v_sub_f32 v40, v40, v208\n\t" \
    "v_mul_f32 v72, v72, v210\n\t" \
    "v_sub_f32 v197, v197, v208\n\t" \
    "v_exp_f32 v197, v197\n\t" \
    "v_sub_f32 v105, v105, v208\n\t" \
    "v_sub_f32 v41, v41, v208\n\t" \
    "v_mul_f32 v73, v73, v210\n\t" \
    "v_sub_f32 v198, v198, v208\n\t" \
    "v_exp_f32 v198, v198\n\t" \
    "v_sub_f32 v106, v106, v208\n\t" \
    "v_sub_f32 v42, v42, v208\n\t" \
    "v_mul_f32 v74, v74, v210\n\t" \
    "v_sub_f32 v199, v199, v208\n\t" \
    "v_exp_f32 v199, v199\n\t" \
    "v_sub_f32 v107, v107, v208\n\t" \
    "v_sub_f32 v43, v43, v208\n\t" \
    "v_mul_f32 v75, v75, v210\n\t" \
    "v_sub_f32 v200, v200, v208\n\t" \
    "v_exp_f32 v200, v200\n\t" \
    "v_sub_f32 v108, v108, v208\n\t" \
    "v_sub_f32 v44, v44, v208\n\t" \
    "v_mul_f32 v76, v76, v210\n\t" \
    "v_sub_f32 v201, v201, v208\n\t" \
    "v_exp_f32 v201, v201\n\t" \
    "v_sub_f32 v109, v109, v208\n\t" \
    "v_sub_f32 v45, v45, v208\n\t" \
    "v_mul_f32 v77, v77, v210\n\t" \
    "v_sub_f32 v202, v202, v208\n\t" \
    "v_exp_f32 v202, v202\n\t" \
    "v_sub_f32 v110, v110, v208\n\t" \
    "v_sub_f32 v46, v46, v208\n\t" \
    "v_mul_f32 v78, v78, v210\n\t" \
    "v_sub_f32 v203, v203, v208\n\t" \
    "v_exp_f32 v203, v203\n\t" \
    "v_sub_f32 v111, v111, v208\n\t" \
    "v_sub_f32 v47, v47, v208\n\t" \
    "v_mul_f32 v79, v79, v210\n\t" \
    "v_mul_f32 v176, v176, v210\n\t" \
    "v_cvt_pk_bf16_f32 v120, v188, v189\n\t" \
    "v_cvt_pk_bf16_f32 v121, v190, v191\n\t" \
    "v_cvt_pk_bf16_f32 v122, v192, v193\n\t" \
    "v_cvt_pk_bf16_f32 v123, v194, v195\n\t" \
    "v_cvt_pk_bf16_f32 v124, v196, v197\n\t" \
    "v_cvt_pk_bf16_f32 v125, v198, v199\n\t" \
    "v_cvt_pk_bf16_f32 v126, v200, v201\n\t" \
    "v_cvt_pk_bf16_f32 v127, v202, v203\n\t" \
    "v_mfma_f32_16x16x32_bf16 v[180:183], v[184:187], v[120:123], v[176:179]\n\t" \
    "s_nop 0\n\t" \
    "v_mfma_f32_16x16x32_bf16 v[180:183], v[184:187], v[124:127], v[180:183]\n\t" \
    "s_nop 15\n\t" \
    "s_branch LretA2v1_%=\n\t" \
    "LrareB2v1_%=:\n\t" \
    "s_nop 15\n\t" \
    "v_mfma_f32_32x32x16_bf16 v[188:203], v[144:147], v[128:131], v[80:95]\n\t" \
    "v_mfma_f32_32x32x16_bf16 v[188:203], v[148:151], v[132:135], v[188:203]\n\t" \
    "s_nop 11\n\t" \
    "v_max3_f32 v208, v188, v189, v190\n\t" \
    "v_max3_f32 v209, v191, v192, v193\n\t" \
    "v_max3_f32 v208, v208, v194, v195\n\t" \
    "v_max3_f32 v209, v209, v196, v197\n\t" \
    "v_max3_f32 v208, v208, v198, v199\n\t" \
    "v_max3_f32 v209, v209, v200, v201\n\t" \
    "v_max3_f32 v208, v208, v202, v203\n\t" \
    "v_max_f32 v208, v208, v209\n\t" \
    "v_mov_b32 v209, v208\n\t" \
    "s_nop 1\n\t" \
    "v_permlane32_swap_b32 v208, v209\n\t" \
    "v_max_f32 v208, v208, v209\n\t" \
    "v_log_f32 v210, v168\n\t" \
    "s_nop 0\n\t" \
    "v_max_f32 v208, v208, v210\n\t" \
    "v_add_f32 v208, s56, v208\n\t" \
    "v_sub_f32 v210, 0, v208\n\t" \
    "v_exp_f32 v210, v210\n\t" \
    "v_sub_f32 v188, v188, v208\n\t" \
    "v_exp_f32 v188, v188\n\t" \
    "v_sub_f32 v80, v80, v208\n\t" \
    "v_sub_f32 v16, v16, v208\n\t" \
    "v_mul_f32 v48, v48, v210\n\t" \
    "v_sub_f32 v189, v189, v208\n\t" \
    "v_exp_f32 v189, v189\n\t" \
    "v_sub_f32 v81, v81, v208\n\t" \
    "v_sub_f32 v17, v17, v208\n\t" \
    "v_mul_f32 v49, v49, v210\n\t" \
    "v_sub_f32 v190, v190, v208\n\t" \
    "v_exp_f32 v190, v190\n\t" \
    "v_sub_f32 v82, v82, v208\n\t" \
    "v_sub_f32 v18, v18, v208\n\t" \
    "v_mul_f32 v50, v50, v210\n\t" \
    "v_sub_f32 v191, v191, v208\n\t" \
    "v_exp_f32 v191, v191\n\t" \
    "v_sub_f32 v83, v83, v208\n\t" \
    "v_sub_f32 v19, v19, v208\n\t" \
    "v_mul_f32 v51, v51, v210\n\t" \
    "v_sub_f32 v192, v192, v208\n\t" \
    "v_exp_f32 v192, v192\n\t" \
    "v_sub_f32 v84, v84, v208\n\t" \
    "v_sub_f32 v20, v20, v208\n\t" \
    "v_mul_f32 v52, v52, v210\n\t" \
    "v_sub_f32 v193, v193, v208\n\t" \
    "v_exp_f32 v193, v193\n\t" \
    "v_sub_f32 v85, v85, v208\n\t" \
    "v_sub_f32 v21, v21, v208\n\t" \
    "v_mul_f32 v53, v53, v210\n\t" \
    "v_sub_f32 v194, v194, v208\n\t" \
    "v_exp_f32 v194, v194\n\t" \
    "v_sub_f32 v86, v86, v208\n\t" \
    "v_sub_f32 v22, v22, v208\n\t" \
    "v_mul_f32 v54, v54, v210\n\t" \
    "v_sub_f32 v195, v195, v208\n\t" \
    "v_exp_f32 v195, v195\n\t" \
    "v_sub_f32 v87, v87, v208\n\t" \
    "v_sub_f32 v23, v23, v208\n\t" \
    "v_mul_f32 v55, v55, v210\n\t" \
    "v_sub_f32 v196, v196, v208\n\t" \
    "v_exp_f32 v196, v196\n\t" \
    "v_sub_f32 v88, v88, v208\n\t" \
    "v_sub_f32 v24, v24, v208\n\t" \
    "v_mul_f32 v56, v56, v210\n\t" \
    "v_sub_f32 v197, v197, v208\n\t" \
    "v_exp_f32 v197, v197\n\t" \
    "v_sub_f32 v89, v89, v208\n\t" \
    "v_sub_f32 v25, v25, v208\n\t" \
    "v_mul_f32 v57, v57, v210\n\t" \
    "v_sub_f32 v198, v198, v208\n\t" \
    "v_exp_f32 v198, v198\n\t" \
    "v_sub_f32 v90, v90, v208\n\t" \
    "v_sub_f32 v26, v26, v208\n\t" \
    "v_mul_f32 v58, v58, v210\n\t" \
    "v_sub_f32 v199, v199, v208\n\t" \
    "v_exp_f32 v199, v199\n\t" \
    "v_sub_f32 v91, v91, v208\n\t" \
    "v_sub_f32 v27, v27, v208\n\t" \
    "v_mul_f32 v59, v59, v210\n\t" \
    "v_sub_f32 v200, v200, v208\n\t" \
    "v_exp_f32 v200, v200\n\t" \
    "v_sub_f32 v92, v92, v208\n\t" \
    "v_sub_f32 v28, v28, v208\n\t" \
    "v_mul_f32 v60, v60, v210\n\t" \
    "v_sub_f32 v201, v201, v208\n\t" \
    "v_exp_f32 v201, v201\n\t" \
    "v_sub_f32 v93, v93, v208\n\t" \
    "v_sub_f32 v29, v29, v208\n\t" \
    "v_mul_f32 v61, v61, v210\n\t" \
    "v_sub_f32 v202, v202, v208\n\t" \
    "v_exp_f32 v202, v202\n\t" \
    "v_sub_f32 v94, v94, v208\n\t" \
    "v_sub_f32 v30, v30, v208\n\t" \
    "v_mul_f32 v62, v62, v210\n\t" \
    "v_sub_f32 v203, v203, v208\n\t" \
    "v_exp_f32 v203, v203\n\t" \
    "v_sub_f32 v95, v95, v208\n\t" \
    "v_sub_f32 v31, v31, v208\n\t" \
    "v_mul_f32 v63, v63, v210\n\t" \
    "v_mul_f32 v168, v168, v210\n\t" \
    "v_cvt_pk_bf16_f32 v112, v188, v189\n\t" \
    "v_cvt_pk_bf16_f32 v113, v190, v191\n\t" \
    "v_cvt_pk_bf16_f32 v114, v192, v193\n\t" \
    "v_cvt_pk_bf16_f32 v115, v194, v195\n\t" \
    "v_cvt_pk_bf16_f32 v116, v196, v197\n\t" \
    "v_cvt_pk_bf16_f32 v117, v198, v199\n\t" \
    "v_cvt_pk_bf16_f32 v118, v200, v201\n\t" \
    "v_cvt_pk_bf16_f32 v119, v202, v203\n\t" \
    "v_mfma_f32_16x16x32_bf16 v[172:175], v[184:187], v[112:115], v[168:171]\n\t" \
    "s_nop 0\n\t" \
    "v_mfma_f32_16x16x32_bf16 v[172:175], v[184:187], v[116:119], v[172:175]\n\t" \
    "s_nop 15\n\t" \
    "s_branch LretB2v1_%=\n\t" \
    "LrareA3v1_%=:\n\t" \
    "s_nop 15\n\t" \
    "v_mfma_f32_32x32x16_bf16 v[188:203], v[144:147], v[136:139], v[96:111]\n\t" \
    "v_mfma_f32_32x32x16_bf16 v[188:203], v[148:151], v[140:143], v[188:203]\n\t" \
    "s_nop 11\n\t" \
    "v_max3_f32 v208, v188, v189, v190\n\t" \
    "v_max3_f32 v209, v191, v192, v193\n\t" \
    "v_max3_f32 v208, v208, v194, v195\n\t" \
    "v_max3_f32 v209, v209, v196, v197\n\t" \
    "v_max3_f32 v208, v208, v198, v199\n\t" \
    "v_max3_f32 v209, v209, v200, v201\n\t" \
    "v_max3_f32 v208, v208, v202, v203\n\t" \
    "v_max_f32 v208, v208, v209\n\t" \
    "v_mov_b32 v209, v208\n\t" \
    "s_nop 1\n\t" \
    "v_permlane32_swap_b32 v208, v209\n\t" \
    "v_max_f32 v208, v208, v209\n\t" \
    "v_log_f32 v210, v180\n\t" \
    "s_nop 0\n\t" \
    "v_max_f32 v208, v208, v210\n\t" \
    "v_add_f32 v208, s56, v208\n\t" \
    "v_sub_f32 v210, 0, v208\n\t" \
    "v_exp_f32 v210, v210\n\t" \
    "v_sub_f32 v188, v188, v208\n\t" \
    "v_exp_f32 v188, v188\n\t" \
    "v_sub_f32 v96, v96, v208\n\t" \
    "v_sub_f32 v32, v32, v208\n\t" \
    "v_mul_f32 v64, v64, v210\n\t" \
    "v_sub_f32 v189, v189, v208\n\t" \
    "v_exp_f32 v189, v189\n\t" \
    "v_sub_f32 v97, v97, v208\n\t" \
    "v_sub_f32 v33, v33, v208\n\t" \
    "v_mul_f32 v65, v65, v210\n\t" \
    "v_sub_f32 v190, v190, v208\n\t" \
    "v_exp_f32 v190, v190\n\t" \
    "v_sub_f32 v98, v98, v208\n\t" \
    "v_sub_f32 v34, v34, v208\n\t" \
    "v_mul_f32 v66, v66, v210\n\t" \
    "v_sub_f32 v191, v191, v208\n\t" \
    "v_exp_f32 v191, v191\n\t" \
    "v_sub_f32 v99, v99, v208\n\t" \
    "v_sub_f32 v35, v35, v208\n\t" \
    "v_mul_f32 v67, v67, v210\n\t" \
    "v_sub_f32 v192, v192, v208\n\t" \
    "v_exp_f32 v192, v192\n\t" \
    "v_sub_f32 v100, v100, v208\n\t" \
    "v_sub_f32 v36, v36, v208\n\t" \
    "v_mul_f32 v68, v68, v210\n\t" \
    "v_sub_f32 v193, v193, v208\n\t" \
    "v_exp_f32 v193, v193\n\t" \
    "v_sub_f32 v101, v101, v208\n\t" \
    "v_sub_f32 v37, v37, v208\n\t" \
    "v_mul_f32 v69, v69, v210\n\t" \
    "v_sub_f32 v194, v194, v208\n\t" \
    "v_exp_f32 v194, v194\n\t" \
    "v_sub_f32 v102, v102, v208\n\t" \
    "v_sub_f32 v38, v38, v208\n\t" \
    "v_mul_f32 v70, v70, v210\n\t" \
    "v_sub_f32 v195, v195, v208\n\t" \
    "v_exp_f32 v195, v195\n\t" \
    "v_sub_f32 v103, v103, v208\n\t" \
    "v_sub_f32 v39, v39, v208\n\t" \
    "v_mul_f32 v71, v71, v210\n\t" \
    "v_sub_f32 v196, v196, v208\n\t" \
    "v_exp_f32 v196, v196\n\t" \
    "v_sub_f32 v104, v104, v208\n\t" \
    "v_sub_f32 v40, v40, v208\n\t" \
    "v_mul_f32 v72, v72, v210\n\t" \
    "v_sub_f32 v197, v197, v208\n\t" \
    "v_exp_f32 v197, v197\n\t" \
    "v_sub_f32 v105, v105, v208\n\t" \
    "v_sub_f32 v41, v41, v208\n\t" \
    "v_mul_f32 v73, v73, v210\n\t" \
    "v_sub_f32 v198, v198, v208\n\t" \
    "v_exp_f32 v198, v198\n\t" \
    "v_sub_f32 v106, v106, v208\n\t" \
    "v_sub_f32 v42, v42, v208\n\t" \
    "v_mul_f32 v74, v74, v210\n\t" \
    "v_sub_f32 v199, v199, v208\n\t" \
    "v_exp_f32 v199, v199\n\t" \
    "v_sub_f32 v107, v107, v208\n\t" \
    "v_sub_f32 v43, v43, v208\n\t" \
    "v_mul_f32 v75, v75, v210\n\t" \
    "v_sub_f32 v200, v200, v208\n\t" \
    "v_exp_f32 v200, v200\n\t" \
    "v_sub_f32 v108, v108, v208\n\t" \
    "v_sub_f32 v44, v44, v208\n\t" \
    "v_mul_f32 v76, v76, v210\n\t" \
    "v_sub_f32 v201, v201, v208\n\t" \
    "v_exp_f32 v201, v201\n\t" \
    "v_sub_f32 v109, v109, v208\n\t" \
    "v_sub_f32 v45, v45, v208\n\t" \
    "v_mul_f32 v77, v77, v210\n\t" \
    "v_sub_f32 v202, v202, v208\n\t" \
    "v_exp_f32 v202, v202\n\t" \
    "v_sub_f32 v110, v110, v208\n\t" \
    "v_sub_f32 v46, v46, v208\n\t" \
    "v_mul_f32 v78, v78, v210\n\t" \
    "v_sub_f32 v203, v203, v208\n\t" \
    "v_exp_f32 v203, v203\n\t" \
    "v_sub_f32 v111, v111, v208\n\t" \
    "v_sub_f32 v47, v47, v208\n\t" \
    "v_mul_f32 v79, v79, v210\n\t" \
    "v_mul_f32 v180, v180, v210\n\t" \
    "v_cvt_pk_bf16_f32 v120, v188, v189\n\t" \
    "v_cvt_pk_bf16_f32 v121, v190, v191\n\t" \
    "v_cvt_pk_bf16_f32 v122, v192, v193\n\t" \
    "v_cvt_pk_bf16_f32 v123, v194, v195\n\t" \
    "v_cvt_pk_bf16_f32 v124, v196, v197\n\t" \
    "v_cvt_pk_bf16_f32 v125, v198, v199\n\t" \
    "v_cvt_pk_bf16_f32 v126, v200, v201\n\t" \
    "v_cvt_pk_bf16_f32 v127, v202, v203\n\t" \
    "v_mfma_f32_16x16x32_bf16 v[176:179], v[184:187], v[120:123], v[180:183]\n\t" \
    "s_nop 0\n\t" \
    "v_mfma_f32_16x16x32_bf16 v[176:179], v[184:187], v[124:127], v[176:179]\n\t" \
    "s_nop 15\n\t" \
    "s_branch LretA3v1_%=\n\t" \
    "LrareB3v1_%=:\n\t" \
    "s_nop 15\n\t" \
    "v_mfma_f32_32x32x16_bf16 v[188:203], v[152:155], v[128:131], v[80:95]\n\t" \
    "v_mfma_f32_32x32x16_bf16 v[188:203], v[156:159], v[132:135], v[188:203]\n\t" \
    "s_nop 11\n\t" \
    "v_max3_f32 v208, v188, v189, v190\n\t" \
    "v_max3_f32 v209, v191, v192, v193\n\t" \
    "v_max3_f32 v208, v208, v194, v195\n\t" \
    "v_max3_f32 v209, v209, v196, v197\n\t" \
    "v_max3_f32 v208, v208, v198, v199\n\t" \
    "v_max3_f32 v209, v209, v200, v201\n\t" \
    "v_max3_f32 v208, v208, v202, v203\n\t" \
    "v_max_f32 v208, v208, v209\n\t" \
    "v_mov_b32 v209, v208\n\t" \
    "s_nop 1\n\t" \
    "v_permlane32_swap_b32 v208, v209\n\t" \
    "v_max_f32 v208, v208, v209\n\t" \
    "v_log_f32 v210, v172\n\t" \
    "s_nop 0\n\t" \
    "v_max_f32 v208, v208, v210\n\t" \
    "v_add_f32 v208, s56, v208\n\t" \
    "v_sub_f32 v210, 0, v208\n\t" \
    "v_exp_f32 v210, v210\n\t" \
    "v_sub_f32 v188, v188, v208\n\t" \
    "v_exp_f32 v188, v188\n\t" \
    "v_sub_f32 v80, v80, v208\n\t" \
    "v_sub_f32 v16, v16, v208\n\t" \
    "v_mul_f32 v48, v48, v210\n\t" \
    "v_sub_f32 v189, v189, v208\n\t" \
    "v_exp_f32 v189, v189\n\t" \
    "v_sub_f32 v81, v81, v208\n\t" \
    "v_sub_f32 v17, v17, v208\n\t" \
    "v_mul_f32 v49, v49, v210\n\t" \
    "v_sub_f32 v190, v190, v208\n\t" \
    "v_exp_f32 v190, v190\n\t" \
    "v_sub_f32 v82, v82, v208\n\t" \
    "v_sub_f32 v18, v18, v208\n\t" \
    "v_mul_f32 v50, v50, v210\n\t" \
    "v_sub_f32 v191, v191, v208\n\t" \
    "v_exp_f32 v191, v191\n\t" \
    "v_sub_f32 v83, v83, v208\n\t" \
    "v_sub_f32 v19, v19, v208\n\t" \
    "v_mul_f32 v51, v51, v210\n\t" \
    "v_sub_f32 v192, v192, v208\n\t" \
    "v_exp_f32 v192, v192\n\t" \
    "v_sub_f32 v84, v84, v208\n\t" \
    "v_sub_f32 v20, v20, v208\n\t" \
    "v_mul_f32 v52, v52, v210\n\t" \
    "v_sub_f32 v193, v193, v208\n\t" \
    "v_exp_f32 v193, v193\n\t" \
    "v_sub_f32 v85, v85, v208\n\t" \
    "v_sub_f32 v21, v21, v208\n\t" \
    "v_mul_f32 v53, v53, v210\n\t" \
    "v_sub_f32 v194, v194, v208\n\t" \
    "v_exp_f32 v194, v194\n\t" \
    "v_sub_f32 v86, v86, v208\n\t" \
    "v_sub_f32 v22, v22, v208\n\t" \
    "v_mul_f32 v54, v54, v210\n\t" \
    "v_sub_f32 v195, v195, v208\n\t" \
    "v_exp_f32 v195, v195\n\t" \
    "v_sub_f32 v87, v87, v208\n\t" \
    "v_sub_f32 v23, v23, v208\n\t" \
    "v_mul_f32 v55, v55, v210\n\t" \
    "v_sub_f32 v196, v196, v208\n\t" \
    "v_exp_f32 v196, v196\n\t" \
    "v_sub_f32 v88, v88, v208\n\t" \
    "v_sub_f32 v24, v24, v208\n\t" \
    "v_mul_f32 v56, v56, v210\n\t" \
    "v_sub_f32 v197, v197, v208\n\t" \
    "v_exp_f32 v197, v197\n\t" \
    "v_sub_f32 v89, v89, v208\n\t" \
    "v_sub_f32 v25, v25, v208\n\t" \
    "v_mul_f32 v57, v57, v210\n\t" \
    "v_sub_f32 v198, v198, v208\n\t" \
    "v_exp_f32 v198, v198\n\t" \
    "v_sub_f32 v90, v90, v208\n\t" \
    "v_sub_f32 v26, v26, v208\n\t" \
    "v_mul_f32 v58, v58, v210\n\t" \
    "v_sub_f32 v199, v199, v208\n\t" \
    "v_exp_f32 v199, v199\n\t" \
    "v_sub_f32 v91, v91, v208\n\t" \
    "v_sub_f32 v27, v27, v208\n\t" \
    "v_mul_f32 v59, v59, v210\n\t" \
    "v_sub_f32 v200, v200, v208\n\t" \
    "v_exp_f32 v200, v200\n\t" \
    "v_sub_f32 v92, v92, v208\n\t" \
    "v_sub_f32 v28, v28, v208\n\t" \
    "v_mul_f32 v60, v60, v210\n\t" \
    "v_sub_f32 v201, v201, v208\n\t" \
    "v_exp_f32 v201, v201\n\t" \
    "v_sub_f32 v93, v93, v208\n\t" \
    "v_sub_f32 v29, v29, v208\n\t" \
    "v_mul_f32 v61, v61, v210\n\t" \
    "v_sub_f32 v202, v202, v208\n\t" \
    "v_exp_f32 v202, v202\n\t" \
    "v_sub_f32 v94, v94, v208\n\t" \
    "v_sub_f32 v30, v30, v208\n\t" \
    "v_mul_f32 v62, v62, v210\n\t" \
    "v_sub_f32 v203, v203, v208\n\t" \
    "v_exp_f32 v203, v203\n\t" \
    "v_sub_f32 v95, v95, v208\n\t" \
    "v_sub_f32 v31, v31, v208\n\t" \
    "v_mul_f32 v63, v63, v210\n\t" \
    "v_mul_f32 v172, v172, v210\n\t" \
    "v_cvt_pk_bf16_f32 v112, v188, v189\n\t" \
    "v_cvt_pk_bf16_f32 v113, v190, v191\n\t" \
    "v_cvt_pk_bf16_f32 v114, v192, v193\n\t" \
    "v_cvt_pk_bf16_f32 v115, v194, v195\n\t" \
    "v_cvt_pk_bf16_f32 v116, v196, v197\n\t" \
    "v_cvt_pk_bf16_f32 v117, v198, v199\n\t" \
    "v_cvt_pk_bf16_f32 v118, v200, v201\n\t" \
    "v_cvt_pk_bf16_f32 v119, v202, v203\n\t" \
    "v_mfma_f32_16x16x32_bf16 v[168:171], v[184:187], v[112:115], v[172:175]\n\t" \
    "s_nop 0\n\t" \
    "v_mfma_f32_16x16x32_bf16 v[168:171], v[184:187], v[116:119], v[168:171]\n\t" \
    "s_nop 15\n\t" \
    "s_branch LretB3v1_%=\n\t" \
    "LrareZ_%=:\n\t" \
    "s_nop 15\n\t" \
    "v_mfma_f32_32x32x16_bf16 v[188:203], v[152:155], v[136:139], v[96:111]\n\t" \
    "v_mfma_f32_32x32x16_bf16 v[188:203], v[156:159], v[140:143], v[188:203]\n\t" \
    "s_nop 11\n\t" \
    "v_max3_f32 v208, v188, v189, v190\n\t" \
    "v_max3_f32 v209, v191, v192, v193\n\t" \
    "v_max3_f32 v208, v208, v194, v195\n\t" \
    "v_max3_f32 v209, v209, v196, v197\n\t" \
    "v_max3_f32 v208, v208, v198, v199\n\t" \
    "v_max3_f32 v209, v209, v200, v201\n\t" \
    "v_max3_f32 v208, v208, v202, v203\n\t" \
    "v_max_f32 v208, v208, v209\n\t" \
    "v_mov_b32 v209, v208\n\t" \
    "s_nop 1\n\t" \
    "v_permlane32_swap_b32 v208, v209\n\t" \
    "v_max_f32 v208, v208, v209\n\t" \
    "v_log_f32 v210, v176\n\t" \
    "s_nop 0\n\t" \
    "v_max_f32 v208, v208, v210\n\t" \
    "v_add_f32 v208, s56, v208\n\t" \
    "v_sub_f32 v210, 0, v208\n\t" \
    "v_exp_f32 v210, v210\n\t" \
    "v_sub_f32 v188, v188, v208\n\t" \
    "v_exp_f32 v188, v188\n\t" \
    "v_sub_f32 v96, v96, v208\n\t" \
    "v_sub_f32 v32, v32, v208\n\t" \
    "v_mul_f32 v64, v64, v210\n\t" \
    "v_sub_f32 v189, v189, v208\n\t" \
    "v_exp_f32 v189, v189\n\t" \
    "v_sub_f32 v97, v97, v208\n\t" \
    "v_sub_f32 v33, v33, v208\n\t" \
    "v_mul_f32 v65, v65, v210\n\t" \
    "v_sub_f32 v190, v190, v208\n\t" \
    "v_exp_f32 v190, v190\n\t" \
    "v_sub_f32 v98, v98, v208\n\t" \
    "v_sub_f32 v34, v34, v208\n\t" \
    "v_mul_f32 v66, v66, v210\n\t" \
    "v_sub_f32 v191, v191, v208\n\t" \
    "v_exp_f32 v191, v191\n\t" \
    "v_sub_f32 v99, v99, v208\n\t" \
    "v_sub_f32 v35, v35, v208\n\t" \
    "v_mul_f32 v67, v67, v210\n\t" \
    "v_sub_f32 v192, v192, v208\n\t" \
    "v_exp_f32 v192, v192\n\t" \
    "v_sub_f32 v100, v100, v208\n\t" \
    "v_sub_f32 v36, v36, v208\n\t" \
    "v_mul_f32 v68, v68, v210\n\t" \
    "v_sub_f32 v193, v193, v208\n\t" \
    "v_exp_f32 v193, v193\n\t" \
    "v_sub_f32 v101, v101, v208\n\t" \
    "v_sub_f32 v37, v37, v208\n\t" \
    "v_mul_f32 v69, v69, v210\n\t" \
    "v_sub_f32 v194, v194, v208\n\t" \
    "v_exp_f32 v194, v194\n\t" \
    "v_sub_f32 v102, v102, v208\n\t" \
    "v_sub_f32 v38, v38, v208\n\t" \
    "v_mul_f32 v70, v70, v210\n\t" \
    "v_sub_f32 v195, v195, v208\n\t" \
    "v_exp_f32 v195, v195\n\t" \
    "v_sub_f32 v103, v103, v208\n\t" \
    "v_sub_f32 v39, v39, v208\n\t" \
    "v_mul_f32 v71, v71, v210\n\t" \
    "v_sub_f32 v196, v196, v208\n\t" \
    "v_exp_f32 v196, v196\n\t" \
    "v_sub_f32 v104, v104, v208\n\t" \
    "v_sub_f32 v40, v40, v208\n\t" \
    "v_mul_f32 v72, v72, v210\n\t" \
    "v_sub_f32 v197, v197, v208\n\t" \
    "v_exp_f32 v197, v197\n\t" \
    "v_sub_f32 v105, v105, v208\n\t" \
    "v_sub_f32 v41, v41, v208\n\t" \
    "v_mul_f32 v73, v73, v210\n\t" \
    "v_sub_f32 v198, v198, v208\n\t" \
    "v_exp_f32 v198, v198\n\t" \
    "v_sub_f32 v106, v106, v208\n\t" \
    "v_sub_f32 v42, v42, v208\n\t" \
    "v_mul_f32 v74, v74, v210\n\t" \
    "v_sub_f32 v199, v199, v208\n\t" \
    "v_exp_f32 v199, v199\n\t" \
    "v_sub_f32 v107, v107, v208\n\t" \
    "v_sub_f32 v43, v43, v208\n\t" \
    "v_mul_f32 v75, v75, v210\n\t" \
    "v_sub_f32 v200, v200, v208\n\t" \
    "v_exp_f32 v200, v200\n\t" \
    "v_sub_f32 v108, v108, v208\n\t" \
    "v_sub_f32 v44, v44, v208\n\t" \
    "v_mul_f32 v76, v76, v210\n\t" \
    "v_sub_f32 v201, v201, v208\n\t" \
    "v_exp_f32 v201, v201\n\t" \
    "v_sub_f32 v109, v109, v208\n\t" \
    "v_sub_f32 v45, v45, v208\n\t" \
    "v_mul_f32 v77, v77, v210\n\t" \
    "v_sub_f32 v202, v202, v208\n\t" \
    "v_exp_f32 v202, v202\n\t" \
    "v_sub_f32 v110, v110, v208\n\t" \
    "v_sub_f32 v46, v46, v208\n\t" \
    "v_mul_f32 v78, v78, v210\n\t" \
    "v_sub_f32 v203, v203, v208\n\t" \
    "v_exp_f32 v203, v203\n\t" \
    "v_sub_f32 v111, v111, v208\n\t" \
    "v_sub_f32 v47, v47, v208\n\t" \
    "v_mul_f32 v79, v79, v210\n\t" \
    "v_mul_f32 v176, v176, v210\n\t" \
    "v_cvt_pk_bf16_f32 v120, v188, v189\n\t" \
    "v_cvt_pk_bf16_f32 v121, v190, v191\n\t" \
    "v_cvt_pk_bf16_f32 v122, v192, v193\n\t" \
    "v_cvt_pk_bf16_f32 v123, v194, v195\n\t" \
    "v_cvt_pk_bf16_f32 v124, v196, v197\n\t" \
    "v_cvt_pk_bf16_f32 v125, v198, v199\n\t" \
    "v_cvt_pk_bf16_f32 v126, v200, v201\n\t" \
    "v_cvt_pk_bf16_f32 v127, v202, v203\n\t" \
    "v_mfma_f32_16x16x32_bf16 v[180:183], v[184:187], v[120:123], v[176:179]\n\t" \
    "s_nop 0\n\t" \
    "v_mfma_f32_16x16x32_bf16 v[180:183], v[184:187], v[124:127], v[180:183]\n\t" \
    "s_nop 15\n\t" \
    "s_branch LretZ_%=\n\t" \
    "Lend_%=:\n\t"
#define ATTN_CLOB "v16", "v17", "v18", "v19", "v20", "v21", "v22", "v23", "v24", "v25", "v26", "v27", "v28", "v29", "v30", "v31", "v32", "v33", "v34", "v35", "v36", "v37", "v38", "v39", "v40", "v41", "v42", "v43", "v44", "v45", "v46", "v47", "v48", "v49", "v50", "v51", "v52", "v53", "v54", "v55", "v56", "v57", "v58", "v59", "v60", "v61", "v62", "v63", "v64", "v65", "v66", "v67", "v68", "v69", "v70", "v71", "v72", "v73", "v74", "v75", "v76", "v77", "v78", "v79", "v80", "v81", "v82", "v83", "v84", "v85", "v86", "v87", "v88", "v89", "v90", "v91", "v92", "v93", "v94", "v95", "v96", "v97", "v98", "v99", "v100", "v101", "v102", "v103", "v104", "v105", "v106", "v107", "v108", "v109", "v110", "v111", "v112", "v113", "v114", "v115", "v116", "v117", "v118", "v119", "v120", "v121", "v122", "v123", "v124", "v125", "v126", "v127", "v128", "v129", "v130", "v131", "v132", "v133", "v134", "v135", "v136", "v137", "v138", "v139", "v140", "v141", "v142", "v143", "v144", "v145", "v146", "v147", "v148", "v149", "v150", "v151", "v152", "v153", "v154", "v155", "v156", "v157", "v158", "v159", "v160", "v161", "v162", "v163", "v164", "v165", "v166", "v167", "v168", "v169", "v170", "v171", "v172", "v173", "v174", "v175", "v176", "v177", "v178", "v179", "v180", "v181", "v182", "v183", "v184", "v185", "v186", "v187", "v188", "v189", "v190", "v191", "v192", "v193", "v194", "v195", "v196", "v197", "v198", "v199", "v200", "v201", "v202", "v203", "v204", "v205", "v206", "v207", "v208", "v209", "v210", "v211", "v212", "v213", "v214", "v215", "v216", "s40", "s41", "s42", "s43", "s44", "s45", "s46", "s47", "s48", "s49", "s50", "s51", "s52", "s53", "s54", "s55", "s56", "s57", "s58", "s59", "s60", "s61", "s62", "s63", "s72", "s73", "s74", "s75", "s76", "s77", "s78", "s79", "vcc", "scc", "memory"


constexpr unsigned UTS = 65536, OTS = 32768;
constexpr unsigned WS_QF = 0, WS_KVF = 4u << 20, WS_UF = 13u << 20, WS_OF = 22u << 20, WS_FL = 28u << 20, WS_USED = 29u << 20;
constexpr unsigned FL_E = WS_FL, FL_ST = WS_FL + 4096, FL_D1 = WS_FL + 8192, FL_D2 = WS_FL + 32768, FL_D3 = WS_FL + 40960, FL_TMO = WS_FL + 65536;
constexpr int LDS_BYTES = 163840;
constexpr float QSCALE = 0.17677669529663687f * 1.4426950408889634f;

DI unsigned cvtpk(float lo, float hi) { unsigned r; asm("v_cvt_pk_bf16_f32 %0, %1, %2" : "=v"(r) : "v"(lo), "v"(hi)); return r; }
DI unsigned rfrag_off(int lane) { const int r = lane & 15, g = lane >> 4; return r * 128 + ((g ^ ((r >> 1) & 7)) << 4); }
DI unsigned rdma_off(int lane, int par, unsigned pitch) { const int rr = lane >> 3; return rr * pitch + (((lane & 7) ^ ((4 * par + (rr >> 1)) & 7)) << 4); }
DI unsigned tdma_off(int lane, int hb, unsigned pitch) { const int kk = lane >> 3; return kk * pitch + (((lane & 7) ^ ((((kk >> 1) & 1) | (hb << 1)) << 1)) << 4); }
DI unsigned tfrag_off(int lane, int krow0) { const int g = lane >> 4, q = (lane & 15) >> 2, p = lane & 3, f = ((q >> 1) & 1) | ((g & 1) << 1); return (krow0 + q) * 128 + (((f << 1) + (p >> 1)) << 4) + (p & 1) * 8; }

DI srd_t ws_srd(char* ws) { return __builtin_amdgcn_make_buffer_rsrc((void*)ws, (short)0, (int)WS_USED, 0x00020000); }
DI void st16(srd_t r, unsigned off, u32x4 v) { __builtin_amdgcn_raw_buffer_store_b128(v, r, (int)off, 0, 16); }
DI u64 mix64(u64 z) { z += 0x9E3779B97F4A7C15ull; z = (z ^ (z >> 30)) * 0xBF58476D1CE4E5B9ull; z = (z ^ (z >> 27)) * 0x94D049BB133111EBull; return z ^ (z >> 31); }
DI void publish(char* ws, unsigned slot_off, u64 tag, int lane) {
    asm volatile("s_waitcnt vmcnt(0)" ::: "memory");
    if (lane == 0) __hip_atomic_store((u64*)(ws + slot_off), tag, __ATOMIC_RELAXED, __HIP_MEMORY_SCOPE_AGENT);
}
DI void wait_tags(char* ws, unsigned off, int nl, u64 tag, int lane) {
    const srd_t r = ws_srd(ws);
    const unsigned vo = off + (lane < nl ? lane : nl - 1) * 16;
    const u64 t0 = __builtin_amdgcn_s_memrealtime();
    for (;;) {
        asm volatile("" ::: "memory");
        const u32x4 v = __builtin_amdgcn_raw_buffer_load_b128(r, (int)vo, 0, 16);
        const bool ok = v[0] == (unsigned)tag && v[1] == (unsigned)(tag >> 32) && v[2] == (unsigned)tag && v[3] == (unsigned)(tag >> 32);
        if (__all(ok)) break;
        if (__builtin_amdgcn_s_memrealtime() - t0 > 2000000ull) {
            if (lane == 0) __hip_atomic_store((unsigned*)(ws + FL_TMO), off, __ATOMIC_RELAXED, __HIP_MEMORY_SCOPE_AGENT);
            break;
        }
        __builtin_amdgcn_s_sleep(1);
    }
}
DI void wg_barrier() { asm volatile("s_waitcnt lgkmcnt(0)\n\ts_barrier" ::: "memory"); }

DI u32x4 nat8(const f32x16& v, int n0, float sc) {
    unsigned x00 = cvtpk(v[4 * n0] * sc, v[4 * n0 + 1] * sc), x01 = cvtpk(v[4 * n0 + 2] * sc, v[4 * n0 + 3] * sc);
    unsigned x10 = cvtpk(v[4 * n0 + 4] * sc, v[4 * n0 + 5] * sc), x11 = cvtpk(v[4 * n0 + 6] * sc, v[4 * n0 + 7] * sc);
    auto s0 = __builtin_amdgcn_permlane32_swap(x00, x10, false, false); auto s1 = __builtin_amdgcn_permlane32_swap(x01, x11, false, false);
    auto t0 = __builtin_amdgcn_permlane16_swap(s0[0], s0[1], false, false); auto t1 = __builtin_amdgcn_permlane16_swap(s1[0], s1[1], false, false);
    return (u32x4){t0[0], t1[0], t0[1], t1[1]};
}

DI void qkv_tile(int tok0, int pair, const u16* __restrict__ x, const u16* __restrict__ wq, char* __restrict__ ws, unsigned lds0, u64& tag, int lwg) {
    const int lane = threadIdx.x & 63, wid = __builtin_amdgcn_readfirstlane(threadIdx.x >> 6), wm = wid >> 1, wn = wid & 1;
    const u64 pp = (u64)(x + (size_t)tok0 * 1024), pq = (u64)(wq + pair * 64), pe = (u64)(ws + FL_E);
    const unsigned bp = (2048 - tok0) * 2048, bq = 1024 * 6144 - pair * 128, m0b = lds0 + wid * 1024;
    const unsigned vp = rdma_off(lane, wid & 1, 2048) + wid * 8 * 2048, vq = tdma_off(lane, wid & 1, 6144) + wid * 8 * 6144;
    const unsigned ap = lds0 + rfrag_off(lane) + wm * 4 * 2048, aq = lds0 + 32768 + (tfrag_off(lane, 8 * (lane >> 4)) ^ (64 * wn));
    const u64 pf0 = (u64)((const char*)x + (size_t)(lwg * 8 + wid) * 2048), pf1 = (u64)((const char*)wq + (lwg * 8 + wid) * 1024);
    const unsigned vl16 = lane * 16;
    f32x16 a[6]; u64 e64 = 0;
    asm volatile(QKVS_ASM
                 : "=&{v[0:15]}"(a[0]), "=&{v[16:31]}"(a[1]), "=&{v[32:47]}"(a[2]), "=&{v[48:63]}"(a[3]), "=&{v[64:79]}"(a[4]), "=&{v[80:95]}"(a[5]), "=&{s[82:83]}"(e64)
                 : [pp] "s"(pp), [pq] "s"(pq), [bp] "s"(bp), [bq] "s"(bq), [m0b] "s"(m0b), [vp] "v"(vp), [vq] "v"(vq), [ap] "v"(ap), [aq] "v"(aq), [pe] "s"(pe), [pf0] "s"(pf0), [pf1] "s"(pf1), [vl16] "v"(vl16)
                 : QKVS_CLOB);
    tag = mix64(e64);
#define TA(m, n, j) a[(24 * (m) + 4 * (n) + (j)) >> 4][(24 * (m) + 4 * (n) + (j)) & 15]
#define FRAG(m0, n0, m1, n1, sc) ({ unsigned a0_ = cvtpk(TA(m0, n0, 0) * (sc), TA(m0, n0, 1) * (sc)), a1_ = cvtpk(TA(m0, n0, 2) * (sc), TA(m0, n0, 3) * (sc)); \
        unsigned b0_ = cvtpk(TA(m1, n1, 0) * (sc), TA(m1, n1, 1) * (sc)), b1_ = cvtpk(TA(m1, n1, 2) * (sc), TA(m1, n1, 3) * (sc)); \
        auto s0_ = __builtin_amdgcn_permlane16_swap(a0_, b0_, false, false); auto s1_ = __builtin_amdgcn_permlane16_swap(a1_, b1_, false, false); \
        (u32x4){s0_[0], s1_[0], s0_[1], s1_[1]}; })
    const srd_t r = ws_srd(ws);
    const int head = 2 * pair + wn, blk0 = (tok0 + wm * 64) >> 5;
    const int l2 = (lane & ~12) | ((lane & 4) << 1) | ((lane & 8) >> 1);
    const unsigned dq = WS_QF + (head * 64 + blk0) * 2048 + lane * 16, dk = WS_KVF + (head * 64 + blk0) * 4096 + l2 * 16, dv = WS_KVF + (head * 64 + blk0) * 4096 + 2048 + lane * 16;
#pragma unroll
    for (int b = 0; b < 2; ++b)
#pragma unroll
        for (int s = 0; s < 2; ++s) {
            st16(r, dk + b * 4096 + s * 1024, FRAG(2 * b, 2 + s, 2 * b + 1, 2 + s, 1.f));
            st16(r, dv + b * 4096 + s * 1024, FRAG(2 * b + s, 4, 2 * b + s, 5, 1.f));
            st16(r, dq + b * 2048 + s * 1024, FRAG(2 * b, s, 2 * b + 1, s, QSCALE));
        }
#undef TA
#undef FRAG
    publish(ws, FL_D1 + head * 256 + ((tok0 + wm * 64) >> 6) * 8, tag, lane);
}

struct Wv { int lane, wid, lw, wm, wn; unsigned isld, m0b; };
DI Wv wave_info(unsigned lds0) {
    Wv w; w.lane = threadIdx.x & 63; w.wid = __builtin_amdgcn_readfirstlane(threadIdx.x >> 6); w.lw = w.wid & 3; w.wm = (w.wid >> 1) & 1; w.wn = w.wid & 1;
    w.isld = w.wid >> 2; w.m0b = lds0 + w.lw * 1024; return w;
}
#define GEMM_IN : [pp] "s"(pp), [pq] "s"(pq), [bp] "s"(bp), [bq] "s"(bq), [m0b] "s"(w.m0b), [vp] "v"(vp), [vq] "v"(vq), [ap] "v"(ap), [aq] "v"(aq), [isld] "s"(w.isld)
#define ACC4_OUT : "=&{v[0:15]}"(a[0]), "=&{v[16:31]}"(a[1]), "=&{v[32:47]}"(a[2]), "=&{v[48:63]}"(a[3])
#define ACC8_OUT ACC4_OUT, "=&{v[64:79]}"(a[4]), "=&{v[80:95]}"(a[5]), "=&{v[96:111]}"(a[6]), "=&{v[112:127]}"(a[7])

DI void uu_tile(int tb, int db, const u16* __restrict__ cre, const u16* __restrict__ cim, const u16* __restrict__ x, char* __restrict__ ws, unsigned lds0, u64& tag, int lwg) {
    const Wv w = wave_info(lds0);
    const int im = tb >> 3, t0 = (tb & 7) * 256;
    const u64 pp = (u64)((im ? cim : cre) + (size_t)t0 * 2048), pq = (u64)(x + db * 128), pe = (u64)(ws + FL_E);
    const unsigned bp = (2048 - t0) * 4096, bq = 2048 * 2048 - db * 256;
    const unsigned vp = rdma_off(w.lane, w.lw & 1, 4096) + w.lw * 8 * 4096, vq = tdma_off(w.lane, w.lw & 1, 2048) + w.lw * 8 * 2048;
    const unsigned ap = lds0 + rfrag_off(w.lane) + w.wm * 8 * 2048, aq = lds0 + 32768 + w.wn * 8192 + tfrag_off(w.lane, 8 * (w.lane >> 4));
    const unsigned u = lwg * 4 + w.lw, vl16 = w.lane * 16;
    const u64 pf0 = (u64)((const char*)cre + (size_t)u * 4096), pf1 = (u64)((const char*)cim + (size_t)u * 4096), pf2 = (u64)((const char*)x + u * 1024);
    f32x16 a[8]; u64 e64 = 0;
    asm volatile(UU_ASM ACC8_OUT, "=&{s[82:83]}"(e64) GEMM_IN, [pe] "s"(pe), [pf0] "s"(pf0), [pf1] "s"(pf1), [pf2] "s"(pf2), [vl16] "v"(vl16) : UU_CLOB);
    tag = mix64(e64);
    if (!w.isld) {
        const srd_t r = ws_srd(ws);
        const float sgn = im ? -1.f : 1.f;
        const int tt0 = (t0 + w.wm * 128) >> 4, ks0 = im * 32 + ((db * 128 + w.wn * 64) >> 5);
        const unsigned d = WS_UF + tt0 * UTS + ks0 * 1024 + w.lane * 16;
#pragma unroll
        for (int m = 0; m < 8; ++m)
#pragma unroll
            for (int u = 0; u < 2; ++u) st16(r, d + m * UTS + u * 1024, nat8(a[m], 2 * u, sgn));
        publish(ws, FL_D2 + (((tb & 7) * 2 + w.wm) * 32 + im * 16 + db * 2 + w.wn) * 8, tag, w.lane);
    }
}

DI void store_rows(const f32x16 (&a)[4], u16* __restrict__ out, int row0, int col0, int lane) {
    const srd_t r = __builtin_amdgcn_make_buffer_rsrc((void*)out, (short)0, 2048 * 2048 * 2, 0x00020000);
#pragma unroll
    for (int m = 0; m < 4; ++m)
#pragma unroll
        for (int u = 0; u < 2; ++u) __builtin_amdgcn_raw_buffer_store_b128(nat8(a[m], 2 * u, 1.f), r, ((row0 + m * 16 + (lane & 15)) * 2048 + col0 + u * 32 + (lane >> 4) * 8) * 2, 0, 16);
}
DI void y_tile(int tb, int eb, const u16* __restrict__ hre, const u16* __restrict__ him, char* __restrict__ ws, u16* __restrict__ out, unsigned lds0) {
    const Wv w = wave_info(lds0);
    const u64 pp = (u64)(ws + WS_UF + (size_t)tb * 8 * UTS), pq = (u64)(hre + (size_t)eb * 128 * 1024), pq2 = (u64)(him + (size_t)eb * 128 * 1024);
    const unsigned bp = 8 * UTS, bq = (1024 - eb * 128) * 2048;
    const unsigned vp = w.lane * 16 + (w.lw >> 1) * UTS + (w.lw & 1) * 1024, vq = rdma_off(w.lane, w.lw & 1, 2048) + w.lw * 8 * 2048;
    const unsigned ap = lds0 + w.wm * 4 * 2048 + w.lane * 16, aq = lds0 + 16384 + rfrag_off(w.lane) + w.wn * 4 * 2048;
    f32x16 a[4];
    asm volatile(Y_ASM ACC4_OUT GEMM_IN, [pq2] "s"(pq2) : Y_CLOB);
    if (!w.isld) store_rows(a, out, tb * 128 + w.wm * 64, 1024 + eb * 128 + w.wn * 64, w.lane);
}
DI void op_tile(int tb, int nb, const u16* __restrict__ wo, char* __restrict__ ws, u16* __restrict__ out, unsigned lds0) {
    const Wv w = wave_info(lds0);
    const u64 pp = (u64)(ws + WS_OF + (size_t)tb * 8 * OTS), pq = (u64)(wo + nb * 128);
    const unsigned bp = 8 * OTS, bq = 1024 * 2048 - nb * 256;
    const unsigned vp = w.lane * 16 + (w.lw >> 1) * OTS + (w.lw & 1) * 1024, vq = tdma_off(w.lane, (w.lw >> 1) & 1, 2048) + w.lw * 8 * 2048;
    const int g = w.lane >> 4;
    const unsigned ap = lds0 + w.wm * 4 * 2048 + w.lane * 16, aq = lds0 + 16384 + w.wn * 8192 + tfrag_off(w.lane, 16 * (g & 1) + 4 * (g >> 1));
    f32x16 a[4];
    asm volatile(OP_ASM ACC4_OUT GEMM_IN : OP_CLOB);
    if (!w.isld) store_rows(a, out, tb * 128 + w.wm * 64, nb * 128 + w.wn * 64, w.lane);
}

DI void attn_unit(int head, int qblk, char* __restrict__ ws, unsigned lds0, u64 tag, u64 pa, u64 pb, unsigned na, unsigned nb, unsigned ps, unsigned vo) {
    const unsigned lane = threadIdx.x & 63; const int wid = __builtin_amdgcn_readfirstlane(threadIdx.x >> 6), lw = wid & 3; const unsigned hf = wid >> 2;
    const u64 pkv = (u64)(ws + WS_KVF + (size_t)head * 262144 + hf * 131072);
    const u64 pq = (u64)(ws + WS_QF + (size_t)(head * 64 + qblk * 8 + lw * 2) * 2048);
    const u64 po = (u64)(ws + WS_OF + (size_t)(qblk * 16 + lw * 4 + hf * 2) * OTS + head * 1024);
    const unsigned vpf = vo + lane * 16;
    const unsigned m0b = lds0 + hf * 16384 + lw * 1024, w1k = lw * 1024, ldsr = lds0 + hf * 16384, xw = lds0 + wid * 8192, xr = lds0 + (wid ^ 4) * 8192;
    asm volatile(ATTN_ASM : : [pkv] "s"(pkv), [pq] "s"(pq), [po] "s"(po), [m0b] "s"(m0b), [lds0] "s"(ldsr), [lane] "v"(lane), [w1k] "s"(w1k), [hf] "s"(hf), [xw] "s"(xw), [xr] "s"(xr),
                   [pa] "s"(pa), [pb] "s"(pb), [na] "s"(na), [nb] "s"(nb), [ps] "s"(ps), [vpf] "v"(vpf), [psb] "s"(ps), [vpb] "v"(vpf) : ATTN_CLOB);
    publish(ws, FL_D3 + ((qblk * 2 + (lw >> 1)) * 128 + head * 4 + (lw & 1) * 2 + hf) * 8, tag, lane);
}

extern __shared__ __attribute__((aligned(16))) char g_lds[];

__global__ __launch_bounds__(512, 2) void fused(const u16* __restrict__ ax, const u16* __restrict__ wqkv, const u16* __restrict__ wo, const u16* __restrict__ bx,
                                                const u16* __restrict__ cre, const u16* __restrict__ cim, const u16* __restrict__ hre, const u16* __restrict__ him,
                                                char* __restrict__ ws, u16* __restrict__ out) {
    {
        unsigned t0_, t1_; u64 pc_;
        asm volatile("s_getpc_b64 %[pc]\n\tLentry_pc_%=:\n\t"
                     "v_lshlrev_b32 %[t0], 7, %[tid]\n\t"
                     "v_add_u32 %[t1], 0x10000, %[t0]\n\t"
                     "v_min_u32 %[t0], fused_code_end-Lentry_pc_%=-4, %[t0]\n\t"
                     "v_min_u32 %[t1], fused_code_end-Lentry_pc_%=-4, %[t1]\n\t"
                     "v_and_b32 %[t0], -4, %[t0]\n\tv_and_b32 %[t1], -4, %[t1]\n\t"
                     "s_mov_b32 m0, %[dst]\n\ts_nop 0\n\t"
                     "global_load_lds_dword %[t0], %[pc]\n\t"
                     "global_load_lds_dword %[t1], %[pc]"
                     : [pc] "=&s"(pc_), [t0] "=&v"(t0_), [t1] "=&v"(t1_) : [tid] "v"(threadIdx.x), [dst] "s"((unsigned)(uintptr_t)(ldsp)g_lds + LDS_BYTES - 256) : "memory");
    }
    const unsigned lds0 = (unsigned)(uintptr_t)(ldsp)g_lds;
    const int xcd = blockIdx.x & 7, i = blockIdx.x >> 3;
    const int lane = threadIdx.x & 63, wid = __builtin_amdgcn_readfirstlane(threadIdx.x >> 6);
    const bool grpU = i < 16;
    u64 tag;
    if (grpU) uu_tile((i & 3) + 4 * (xcd & 3), (i >> 2) + 4 * (xcd >> 2), cre, cim, bx, ws, lds0, tag, xcd * 16 + (i & 15));
    else qkv_tile((4 * (xcd & 1) + (i & 3)) * 256, 4 * (xcd >> 1) + ((i & 15) >> 2), ax, wqkv, ws, lds0, tag, xcd * 16 + (i & 15));
    if (wid == 0 && lane == 0) __hip_atomic_store((u64*)(ws + FL_ST) + blockIdx.x, tag, __ATOMIC_RELAXED, __HIP_MEMORY_SCOPE_AGENT);
    const int head = xcd * 4 + (i >> 3), qblk = i & 7;
    if (wid == 4) wait_tags(ws, FL_D1 + head * 256, 16, tag, lane);
    if (wid == 5 && blockIdx.x == 0) {
        wait_tags(ws, FL_ST, 64, tag, lane); wait_tags(ws, FL_ST + 1024, 64, tag, lane);
        if (lane == 0) __hip_atomic_store((u64*)(ws + FL_E), tag, __ATOMIC_RELAXED, __HIP_MEMORY_SCOPE_AGENT);
    }
    wg_barrier();
    {
        const unsigned wx = (i & 15) * 8 + wid, half = xcd >> 2;
        const u64 pa = grpU ? (u64)((const char*)wo + half * 1024) : (u64)((const char*)hre + (half << 20)), pb = grpU ? (u64)wo : (u64)((const char*)him + (half << 20));
        attn_unit(head, qblk, ws, lds0, tag, pa, pb, grpU ? (2u << 20) - half * 1024 : 1u << 20, grpU ? 0u : 1u << 20, grpU ? 128u * 2048 : 128u * 1024, grpU ? wx * 2048 : wx * 1024);
    }
    const int tb = (i & 3) + 4 * (xcd & 3), cb = ((i & 15) >> 2) + 4 * (xcd >> 2);
    if (grpU) {
        if (wid == 4) wait_tags(ws, FL_D3 + tb * 1024, 64, tag, lane);
        wg_barrier();
        op_tile(tb, cb, wo, ws, out, lds0);
    } else {
        if (wid == 4) wait_tags(ws, FL_D2 + tb * 256, 16, tag, lane);
        wg_barrier();
        y_tile(tb, cb, hre, him, ws, out, lds0);
    }
    asm volatile("fused_code_end:");
}

extern "C" void kernel_launch(void* const* d_in, const int* in_sizes, int n_in, void* d_out, int out_size, void* d_ws, size_t ws_size, hipStream_t stream) {
    const u16* ax = (const u16*)d_in[0]; const u16* wqkv = (const u16*)d_in[1]; const u16* wo = (const u16*)d_in[2]; const u16* bx = (const u16*)d_in[3];
    const u16* cre = (const u16*)d_in[4]; const u16* cim = (const u16*)d_in[5]; const u16* hre = (const u16*)d_in[6]; const u16* him = (const u16*)d_in[7];
    u16* out = (u16*)d_out; char* ws = (char*)d_ws;
    (void)hipFuncSetAttribute((const void*)fused, hipFuncAttributeMaxDynamicSharedMemorySize, LDS_BYTES);
    fused<<<256, 512, LDS_BYTES, stream>>>(ax, wqkv, wo, bx, cre, cim, hre, him, ws, out);
}
```
